```python
import torch

def _softmax(x, axis=-1):
    return torch.softmax(x, dim=axis)
def _gelu(x):
    return torch.nn.functional.gelu(x, approximate="tanh")
def _silu(x):
    return torch.nn.functional.silu(x)
def _sigmoid(x):
    return torch.sigmoid(x)
def _layernorm(x, axis=-1, eps=1e-5):
    m = x.mean(dim=axis, keepdim=True)
    v = x.var(dim=axis, keepdim=True, unbiased=False)
    return (x - m) / torch.sqrt(v + eps)
def _scatter_add(tgt, idx, src, axis=0):
    return tgt.clone().index_add_(axis, idx, src)
def _segment_sum(src, seg, n):
    out = torch.zeros((n,) + src.shape[1:], dtype=src.dtype)
    return out.index_add_(0, seg, src)
def _pad(x, pw):
    flat = tuple(v for pair in reversed(pw) for v in pair)
    return torch.nn.functional.pad(x, flat)

kernel_name = 'recurrent_scan__sliding_block_cached__moe_arch_mod__prompt'

def setup_inputs():

    _w = (lambda a: (lambda t: t.to(torch.bfloat16) if t.is_floating_point() else t)(torch.as_tensor(a)))
    def _A_setup():
        B, Sq, D = 1, 2048, 1024
        Hq, Hkv, Sk, Dh = 32, 4, 2048, 128
        assert Hq % Hkv == 0, (Hq, Hkv)
        return {
            "x": _w((lambda *s: torch.randn(*s, dtype=torch.bfloat16) * 0.05)(B, Sq, D)),
            "k_cache": _w((lambda *s: torch.randn(*s, dtype=torch.bfloat16) * 0.8)(B, Hkv, Sk, Dh)),
            "v_cache": _w((lambda *s: torch.randn(*s, dtype=torch.bfloat16) * 0.2)(B, Hkv, Sk, Dh)),
            "wq": _w((lambda *s: torch.randn(*s, dtype=torch.bfloat16) * 0.8)(D, Hq * Dh) * (D ** -0.5)),
            "wo": _w((lambda *s: torch.randn(*s, dtype=torch.bfloat16) * 0.2)(Hq * Dh, D) * ((Hq * Dh) ** -0.5)),
        }
    _A_in = _A_setup()

    def _B_setup():
        B, S, D, F, C = 1, 2048, 1024, 3584, 1
        return {
            "x": _w((lambda *s: torch.randn(*s, dtype=torch.bfloat16) * 0.05)(B, S, D)),
            "w_r": _w((lambda *s: torch.randn(*s, dtype=torch.bfloat16) * 0.2)(D)),
            "w_gate": _w((lambda *s: torch.randn(*s, dtype=torch.bfloat16) * 0.1)(D, F)),
            "w_up": _w((lambda *s: torch.randn(*s, dtype=torch.bfloat16) * 0.1)(D, F)),
            "w_down": _w((lambda *s: torch.randn(*s, dtype=torch.bfloat16) * 0.1)(F, D)),
        }
    _B_in = _B_setup()

    return {
        "A_x": _A_in["x"],
        "A_k_cache": _A_in["k_cache"],
        "A_v_cache": _A_in["v_cache"],
        "A_wq": _A_in["wq"],
        "A_wo": _A_in["wo"],
        "B_x": _B_in["x"],
        "B_w_r": _B_in["w_r"],
        "B_w_gate": _B_in["w_gate"],
        "B_w_up": _B_in["w_up"],
        "B_w_down": _B_in["w_down"],
    }

def reference(A_x, A_k_cache, A_v_cache, A_wq, A_wo, B_x, B_w_r, B_w_gate, B_w_up, B_w_down):
    def _A_ref(x, k_cache, v_cache, wq, wo):
        B, Sq, D = x.shape
        Hq, Dh = 32, 128
        Hkv, Sk = k_cache.shape[1], k_cache.shape[2]
        W = min(4096, max(256, Sk // 4))
        rep = Hq // Hkv
        q = (lambda x, *d: x.permute(*d))((x @ wq).reshape(B, Sq, Hq, Dh), 0, 2, 1, 3)
        k = (lambda x, n, axis: torch.repeat_interleave(x, n, dim=axis))(k_cache[:, :, Sk - W:, :], rep, axis=1)
        v = (lambda x, n, axis: torch.repeat_interleave(x, n, dim=axis))(v_cache[:, :, Sk - W:, :], rep, axis=1)
        scores = torch.einsum("bhqd,bhkd->bhqk", q, k) * (Dh ** -0.5)
        p = _softmax(scores, axis=-1)
        o = torch.einsum("bhqk,bhkd->bhqd", p, v)
        return (lambda x, *d: x.permute(*d))(o, 0, 2, 1, 3).reshape(B, Sq, Hq * Dh) @ wo

    def _B_ref(x, w_r, w_gate, w_up, w_down):
        B, S, D = x.shape
        C = 1
        r = x @ w_r
        idx = torch.argsort(-r, axis=-1)[:, :C]
        idx_e = (lambda x, n, axis: torch.repeat_interleave(x, n, dim=axis))(idx.reshape(B, C, 1), D, axis=2)
        x_sel = (lambda x, i, axis: torch.take_along_dim(x, i, dim=axis))(x, idx_e, axis=1)
        g = _silu(x_sel @ w_gate)
        u = x_sel @ w_up
        y = (g * u) @ w_down
        r_sel = _sigmoid((lambda x, i, axis: torch.take_along_dim(x, i, dim=axis))(r, idx, axis=1))
        y = y * r_sel.reshape(B, C, 1)
        b_off = (torch.arange(B) * S).reshape(B, 1)
        flat_idx = (idx + b_off).reshape(B * C)
        zero = (x * 0.0).reshape(B * S, D)
        upd = _scatter_add(zero, flat_idx, y.reshape(B * C, D), axis=0)
        return x + upd.reshape(B, S, D)

    x = A_x
    xn = _layernorm(x, axis=-1)
    s = _A_ref(xn, A_k_cache, A_v_cache, A_wq, A_wo)
    g = _silu(_B_ref(xn, B_w_r, B_w_gate, B_w_up, B_w_down))
    return x + s * g
```

```cpp
#include <hip/hip_runtime.h>
#include <hip/hip_bf16.h>
#include <stdint.h>
typedef unsigned short u16;
typedef unsigned u32x4 __attribute__((ext_vector_type(4)));
typedef unsigned u32x2 __attribute__((ext_vector_type(2)));
typedef float f32x4 __attribute__((ext_vector_type(4)));
typedef unsigned long long u64;
#define DI __device__ __forceinline__

DI float bf2f(unsigned v) { return __uint_as_float(v << 16); }
DI float bflo(unsigned w) { return __uint_as_float(w << 16); }
DI float bfhi(unsigned w) { return __uint_as_float(w & 0xffff0000u); }
DI float rbf(float x) { return (float)(__bf16)x; }
DI unsigned f2bf(float x) { __bf16 b = (__bf16)x; return (unsigned)__builtin_bit_cast(u16, b); }
DI float silu_f(float x) { return x * __builtin_amdgcn_rcpf(1.f + __expf(-x)); }
DI unsigned rfl(unsigned x) { return __builtin_amdgcn_readfirstlane(x); }
template <int C> DI float dpp_add(float v) { return v + __int_as_float(__builtin_amdgcn_update_dpp(0, __float_as_int(v), C, 0xf, 0xf, true)); }
DI float wave_sum(float v) {
  v = dpp_add<0xB1>(v); v = dpp_add<0x4E>(v); v = dpp_add<0x141>(v); v = dpp_add<0x140>(v);
  auto a = __builtin_amdgcn_permlane16_swap(__float_as_uint(v), __float_as_uint(v), false, false);
  v = __uint_as_float(a[0]) + __uint_as_float(a[1]);
  auto b = __builtin_amdgcn_permlane32_swap(__float_as_uint(v), __float_as_uint(v), false, false);
  return __uint_as_float(b[0]) + __uint_as_float(b[1]);
}

typedef __bf16 bf16x8 __attribute__((ext_vector_type(8)));
typedef __attribute__((address_space(3))) unsigned lds_u32;
typedef __attribute__((address_space(3))) u32x2 lds_u32x2;
typedef __attribute__((address_space(3))) u32x4 lds_u32x4;
#define WS_OF   8388608u
#define WS_G    34603008u
#define G_RS    4096u
#define G_FH    147456u
#define G_HP    163840u
#define G_FO    (622592u)
#define G_FY    (G_FO + 65536u)
#define G_FX    (G_FY + 16384u)
#define G_YP    (G_FX + 65536u)
#define WS_XCH  (48u << 20)
#define RLX_AGENT __ATOMIC_RELAXED, __HIP_MEMORY_SCOPE_AGENT

DI unsigned ld_sc1(const char* p) { return __hip_atomic_load((const unsigned*)p, RLX_AGENT); }
DI void st_sc1(char* p, unsigned v) { __hip_atomic_store((unsigned*)p, v, RLX_AGENT); }
DI void wait_tags(const char* p, unsigned tag) {
  for (unsigned spins = 0; spins < (1u << 20); ++spins) {
    if (__all(ld_sc1(p) == tag)) break;
    __builtin_amdgcn_s_sleep(1);
  }
  __builtin_amdgcn_fence(__ATOMIC_ACQUIRE, "wavefront");
}
DI __amdgpu_buffer_rsrc_t mk_rsrc(const void* p, unsigned bytes) { return __builtin_amdgcn_make_buffer_rsrc((void*)p, 0, bytes, 0x00020000); }
DI f32x4 mfma16(u32x4 a, u32x4 b, f32x4 c) { return __builtin_amdgcn_mfma_f32_16x16x32_bf16(__builtin_bit_cast(bf16x8, a), __builtin_bit_cast(bf16x8, b), c, 0, 0, 0); }
DI unsigned pk2(float lo, float hi) { return f2bf(lo) | (f2bf(hi) << 16); }
template <int C> DI float dpp_mov(float v) { return __int_as_float(__builtin_amdgcn_update_dpp(0, __float_as_int(v), C, 0xf, 0xf, true)); }
DI float xmax4(float v) {
  auto a = __builtin_amdgcn_permlane16_swap(__float_as_uint(v), __float_as_uint(v), false, false);
  v = fmaxf(__uint_as_float(a[0]), __uint_as_float(a[1]));
  auto b = __builtin_amdgcn_permlane32_swap(__float_as_uint(v), __float_as_uint(v), false, false);
  return fmaxf(__uint_as_float(b[0]), __uint_as_float(b[1]));
}
DI float xsum4(float v) {
  auto a = __builtin_amdgcn_permlane16_swap(__float_as_uint(v), __float_as_uint(v), false, false);
  v = __uint_as_float(a[0]) + __uint_as_float(a[1]);
  auto b = __builtin_amdgcn_permlane32_swap(__float_as_uint(v), __float_as_uint(v), false, false);
  return __uint_as_float(b[0]) + __uint_as_float(b[1]);
}

DI unsigned stampv() { unsigned t; asm volatile("s_memrealtime s[98:99]\n\ts_waitcnt lgkmcnt(0)\n\tv_mov_b32 %0, s98" : "=v"(t) :: "memory", "s98", "s99"); return t; }
DI void slow_attention(char* oslot, const u16* kw, const u16* vw, int lane) {
  const int g = lane >> 4, r16 = lane & 15;
  const __amdgpu_buffer_rsrc_t rs = mk_rsrc(oslot, 4 * 131072);
#pragma unroll 1
  for (int qt = 0; qt < 4; ++qt) {
    u32x4 qf[4];
#pragma unroll
    for (int ks = 0; ks < 4; ++ks) qf[ks] = __builtin_amdgcn_raw_buffer_load_b128(rs, lane * 16 + ks * 1024, qt * 131072, 16);
    float m = -INFINITY;
#pragma unroll 1
    for (int kt = 0; kt < 32; ++kt) {
      f32x4 s = {0.f, 0.f, 0.f, 0.f};
#pragma unroll
      for (int ks = 0; ks < 4; ++ks) s = mfma16(*(const u32x4*)(kw + (kt * 16 + r16) * 128 + ks * 32 + g * 8), qf[ks], s);
      m = fmaxf(fmaxf(fmaxf(s[0], s[1]), fmaxf(s[2], s[3])), m);
    }
    m = xmax4(m);
    f32x4 o[8]; float l = 0.f;
#pragma unroll
    for (int dt = 0; dt < 8; ++dt) o[dt] = f32x4{0.f, 0.f, 0.f, 0.f};
#pragma unroll 1
    for (int u = 0; u < 16; ++u) {
      u32x4 p;
#pragma unroll
      for (int kt = 0; kt < 2; ++kt) {
        f32x4 s = {0.f, 0.f, 0.f, 0.f};
#pragma unroll
        for (int ks = 0; ks < 4; ++ks) s = mfma16(*(const u32x4*)(kw + (u * 32 + kt * 16 + r16) * 128 + ks * 32 + g * 8), qf[ks], s);
        float e[4];
#pragma unroll
        for (int i = 0; i < 4; ++i) { e[i] = rbf(__builtin_amdgcn_exp2f(s[i] - m)); l += e[i]; }
        p[2 * kt] = pk2(e[0], e[1]); p[2 * kt + 1] = pk2(e[2], e[3]);
      }
#pragma unroll
      for (int dt = 0; dt < 8; ++dt) {
        u32x4 vf;
#pragma unroll
        for (int jj = 0; jj < 4; ++jj) {
          const int k0 = u * 32 + (jj >> 1) * 16 + 4 * g + (jj & 1) * 2;
          vf[jj] = (unsigned)vw[k0 * 128 + dt * 16 + r16] | ((unsigned)vw[(k0 + 1) * 128 + dt * 16 + r16] << 16);
        }
        o[dt] = mfma16(vf, p, o[dt]);
      }
    }
    const float il = 1.f / xsum4(l);
#pragma unroll
    for (int ks = 0; ks < 4; ++ks) {
      u32x4 w = {pk2(o[2 * ks][0] * il, o[2 * ks][1] * il), pk2(o[2 * ks][2] * il, o[2 * ks][3] * il), pk2(o[2 * ks + 1][0] * il, o[2 * ks + 1][1] * il), pk2(o[2 * ks + 1][2] * il, o[2 * ks + 1][3] * il)};
      __builtin_amdgcn_raw_buffer_store_b128(w, rs, lane * 16 + ks * 1024, qt * 131072, 16);
    }
  }
}

__global__ __launch_bounds__(256, 1) void fused_k(const u16* a_x, const u16* a_kc, const u16* a_vc, const u16* a_wq, const u16* a_wo, const u16* a_wr, const u16* a_wg, const u16* a_wu, const u16* a_wd, u16* a_out, char* a_ws) {
  extern __shared__ __attribute__((aligned(16))) char lds[];
  int wid = rfl(threadIdx.x >> 6);
  int b = blockIdx.x;
  u64 ka = (u64)__builtin_amdgcn_kernarg_segment_ptr();
  u64 bad; unsigned key, tag, rsel; u32x4 op[22];
  asm volatile(
"s_load_dwordx2 s[60:61], %[ka], 0\n"
"s_load_dwordx2 s[44:45], %[ka], 24\n"
"s_load_dwordx2 s[64:65], %[ka], 40\n"
"s_getpc_b64 s[82:83]\n"
"v_mbcnt_lo_u32_b32 v108, -1, 0\n"
"v_mbcnt_hi_u32_b32 v108, -1, v108\n"
"v_lshlrev_b32 v153, 7, v108\n"
"global_load_dword v152, v153, s[82:83]\n"
"v_lshlrev_b32 v124, 4, v108\n"
"s_mov_b32 s68, %[wid]\n"
"s_mov_b32 s93, %[b]\n"
"s_and_b32 s73, s93, 3\n"
"s_lshl_b32 s73, s73, 3\n"
"s_bfe_u32 s91, s93, 0x30003\n"
"s_add_u32 s91, s91, s73\n"
"s_lshr_b32 s73, s93, 6\n"
"s_lshl_b32 s73, s73, 1\n"
"s_bfe_u32 s92, s93, 0x10002\n"
"s_add_u32 s92, s92, s73\n"
"s_lshl_b32 s77, s92, 5\n"
"s_add_u32 s77, s77, s91\n"
"s_lshl_b32 s77, s77, 2\n"
"s_add_u32 s77, s77, s68\n"
"s_lshl_b32 s77, s77, 1\n"
"s_mov_b32 s62, 4194304\n"
"s_mov_b32 s63, 0x00020000\n"
"s_mov_b32 s66, 2048\n"
"s_mov_b32 s67, 0x00020000\n"
"s_lshl_b32 s90, s77, 11\n"
"s_waitcnt lgkmcnt(0)\n"
"buffer_load_dwordx4 v[128:131], v124, s[60:63], s90 offen offset:0\n"
"buffer_load_dwordx4 v[132:135], v124, s[60:63], s90 offen offset:1024\n"
"buffer_load_dwordx4 v[136:139], v124, s[60:63], s90 offen offset:2048\n"
"buffer_load_dwordx4 v[140:143], v124, s[60:63], s90 offen offset:3072\n"
"buffer_load_dwordx4 v[144:147], v124, s[64:67], 0 offen offset:0\n"
"buffer_load_dwordx4 v[148:151], v124, s[64:67], 0 offen offset:1024\n"
"s_load_dwordx2 s[48:49], %[ka], 8\n"
"s_load_dwordx2 s[52:53], %[ka], 16\n"
"s_load_dwordx2 s[56:57], %[ka], 80\n"
"s_load_dwordx2 s[24:25], %[ka], 48\n"
"s_load_dwordx2 s[28:29], %[ka], 56\n"
"s_load_dwordx2 s[20:21], %[ka], 64\n"
"s_load_dwordx2 s[36:37], %[ka], 32\n"
"s_and_b32 s73, s68, 1\n"
"s_lshl_b32 s73, s73, 1\n"
"s_lshl_b32 s74, s68, 16\n"
"v_and_b32 v1, 3, v108\n"
"v_xor_b32 v1, s73, v1\n"
"v_lshlrev_b32 v1, 4, v1\n"
"v_lshrrev_b32 v2, 5, v108\n"
"v_lshl_add_u32 v3, v2, 6, v1\n"
"v_bfe_u32 v4, v108, 2, 3\n"
"v_lshl_add_u32 v104, v4, 13, v3\n"
"v_add_u32 v104, s74, v104\n"
"v_lshrrev_b32 v0, 4, v108\n"
"v_and_b32 v1, 7, v108\n"
"v_xor_b32 v1, v1, v0\n"
"v_lshlrev_b32 v1, 4, v1\n"
"v_lshrrev_b32 v2, 3, v108\n"
"v_lshl_add_u32 v101, v2, 11, v1\n"
"v_xor_b32 v102, 64, v101\n"
"s_lshl_b32 s69, s68, 11\n"
"s_lshl_b32 s70, s68, 13\n"
"s_mov_b32 s86, 0\n"
"s_mov_b32 s87, 0\n"
"s_lshl_b32 s73, s92, 2\n"
"s_add_u32 s73, s73, s68\n"
"s_lshl_b32 s74, s73, 17\n"
"s_add_u32 s40, s60, s74\n"
"s_addc_u32 s41, s61, 0\n"
"s_mov_b32 s42, 131072\n"
"s_mov_b32 s43, 0x00020000\n"
"s_lshl_b32 s73, s91, 8\n"
"s_add_u32 s44, s44, s73\n"
"s_addc_u32 s45, s45, 0\n"
"s_sub_u32 s44, s44, 1024\n"
"s_subb_u32 s45, s45, 0\n"
"s_sub_u32 s46, 8389632, s73\n"
"s_mov_b32 s47, 0x00020000\n"
"s_add_u32 m0, s69, 65536\n"
"s_add_u32 s75, s87, 1024\n"
"buffer_load_dwordx4 v104, s[44:47], s75 offen offset:0 lds\n"
"s_add_u32 s75, s87, 128\n"
"buffer_load_dwordx4 v104, s[44:47], s75 offen offset:1024 lds\n"
"s_add_u32 m0, s70, 81920\n"
"s_add_u32 s75, s86, 0\n"
"buffer_load_dwordx4 v101, s[40:43], s75 offen offset:0 lds\n"
"s_add_u32 s75, s86, 15360\n"
"buffer_load_dwordx4 v102, s[40:43], s75 offen offset:1024 lds\n"
"s_add_u32 s75, s86, 30720\n"
"buffer_load_dwordx4 v101, s[40:43], s75 offen offset:2048 lds\n"
"s_add_u32 s75, s86, 46080\n"
"buffer_load_dwordx4 v102, s[40:43], s75 offen offset:3072 lds\n"
"s_add_u32 m0, s69, 73728\n"
"s_add_u32 s75, s87, 263168\n"
"buffer_load_dwordx4 v104, s[44:47], s75 offen offset:0 lds\n"
"s_add_u32 s75, s87, 262272\n"
"buffer_load_dwordx4 v104, s[44:47], s75 offen offset:1024 lds\n"
"s_add_u32 m0, s70, 86016\n"
"s_add_u32 s75, s86, 65536\n"
"buffer_load_dwordx4 v101, s[40:43], s75 offen offset:0 lds\n"
"s_lshr_b32 s73, s93, 3\n"
"s_mul_i32 s73, s73, 640\n"
"s_add_u32 s73, s73, 7168\n"
"v_lshrrev_b32 v153, 4, v124\n"
"v_min_u32 v153, 4, v153\n"
"v_lshlrev_b32 v153, 7, v153\n"
"v_add_u32 v153, s73, v153\n"
"s_getpc_b64 s[88:89]\n"
"global_load_dword v152, v153, s[88:89]\n"
"s_lshr_b32 s84, s92, 1\n"
"s_add_u32 s84, s84, s91\n"
"s_lshl_b32 s84, s84, 2\n"
"s_and_b32 s84, s84, 12\n"
"s_mov_b32 s78, 0x7651c840\n"
"s_mov_b32 s79, 0x32fedba9\n"
"s_cmp_eq_u32 s84, 4\n"
"s_cselect_b32 s78, 0x7642d950, s78\n"
"s_cselect_b32 s79, 0x31fecba8, s79\n"
"s_cmp_eq_u32 s84, 8\n"
"s_cselect_b32 s78, 0x7543ea60, s78\n"
"s_cselect_b32 s79, 0x21fdcb98, s79\n"
"s_cmp_eq_u32 s84, 12\n"
"s_cselect_b32 s78, 0x8654fb70, s78\n"
"s_cselect_b32 s79, 0x321edca9, s79\n"
"s_waitcnt lgkmcnt(0)\n"
"s_mov_b64 s[82:83], s[56:57]\n"
"s_mov_b64 s[96:97], s[60:61]\n"
"s_mov_b32 s98, 4194304\n"
"s_mov_b32 s99, 0x00020000\n"
"s_and_b32 s73, s93, 7\n"
"s_lshr_b32 s74, s93, 3\n"
"s_cmp_lt_u32 s74, 28\n"
"s_cselect_b32 s95, -1, 0\n"
"s_mul_i32 s75, s73, 917504\n"
"s_lshl_b32 s90, s74, 8\n"
"s_add_u32 s75, s75, s90\n"
"s_and_b32 s75, s75, s95\n"
"s_add_u32 s24, s24, s75\n"
"s_addc_u32 s25, s25, 0\n"
"s_sub_u32 s24, s24, 1024\n"
"s_subb_u32 s25, s25, 0\n"
"s_sub_u32 s26, 7341056, s75\n"
"s_and_b32 s26, s26, s95\n"
"s_mov_b32 s27, 0x00020000\n"
"s_add_u32 s28, s28, s75\n"
"s_addc_u32 s29, s29, 0\n"
"s_sub_u32 s28, s28, 1024\n"
"s_subb_u32 s29, s29, 0\n"
"s_sub_u32 s30, 7341056, s75\n"
"s_and_b32 s30, s30, s95\n"
"s_mov_b32 s31, 0x00020000\n"
"s_lshl_b32 s75, s74, 18\n"
"s_lshl_b32 s90, s73, 8\n"
"s_add_u32 s75, s75, s90\n"
"s_and_b32 s75, s75, s95\n"
"s_add_u32 s20, s20, s75\n"
"s_addc_u32 s21, s21, 0\n"
"s_sub_u32 s20, s20, 1024\n"
"s_subb_u32 s21, s21, 0\n"
"s_sub_u32 s22, 7341056, s75\n"
"s_and_b32 s22, s22, s95\n"
"s_mov_b32 s23, 0x00020000\n"
"s_and_b32 s73, s93, 3\n"
"s_lshl_b32 s73, s73, 21\n"
"s_add_u32 s36, s36, s73\n"
"s_addc_u32 s37, s37, 0\n"
"s_sub_u32 s36, s36, 1024\n"
"s_subb_u32 s37, s37, 0\n"
"s_mov_b32 s38, 2098176\n"
"s_mov_b32 s39, 0x00020000\n"
"s_mov_b64 s[60:61], s[56:57]\n"
"s_add_u32 s60, s60, 34603008\n"
"s_addc_u32 s61, s61, 0\n"
"s_mov_b32 s62, 622592\n"
"s_mov_b32 s63, 0x00020000\n"
"s_mov_b64 s[64:65], s[60:61]\n"
"s_mov_b32 s66, 622592\n"
"s_mov_b32 s67, 0x00020000\n"
"s_cmp_eq_u32 s68, 0\n"
"s_cselect_b32 s66, s66, 0\n"
"s_lshr_b32 s73, s91, 3\n"
"s_lshl_b32 s73, s73, 19\n"
"s_add_u32 s73, s73, 393216\n"
"s_add_u32 s48, s48, s73\n"
"s_addc_u32 s49, s49, 0\n"
"s_mov_b32 s50, 131072\n"
"s_mov_b32 s51, 0x00020000\n"
"s_sub_u32 s73, s73, 2048\n"
"s_add_u32 s52, s52, s73\n"
"s_addc_u32 s53, s53, 0\n"
"s_mov_b32 s54, 133120\n"
"s_mov_b32 s55, 0x00020000\n"
"s_mov_b32 s81, 0x3e0293ee\n"
"v_lshrrev_b32 v1, 2, v4\n"
"v_lshlrev_b32 v1, 4, v1\n"
"v_and_b32 v2, 3, v4\n"
"v_add_u32 v1, v1, v2\n"
"v_lshl_add_u32 v122, v1, 8, v3\n"
"s_lshl_b32 s74, s68, 10\n"
"s_add_u32 s74, s74, 1024\n"
"v_add_u32 v122, s74, v122\n"
"v_and_b32 v1, 15, v108\n"
"v_xor_b32 v1, v1, v0\n"
"v_lshlrev_b32 v1, 4, v1\n"
"v_lshl_add_u32 v1, v0, 8, v1\n"
"s_lshl_b32 s74, s68, 6\n"
"v_xor_b32 v1, s74, v1\n"
"s_lshl_b32 s74, s68, 10\n"
"v_add_u32 v118, s74, v1\n"
"v_bfe_u32 v1, v108, 2, 2\n"
"v_lshlrev_b32 v1, 6, v1\n"
"v_lshl_add_u32 v1, v0, 11, v1\n"
"v_and_b32 v2, 3, v108\n"
"v_lshl_add_u32 v1, v2, 3, v1\n"
"v_and_b32 v2, 1, v0\n"
"v_lshl_add_u32 v112, v2, 5, v1\n"
"v_xor_b32 v113, 32, v112\n"
"v_add_u32 v105, 65536, v112\n"
"v_add_u32 v106, 65536, v113\n"
"v_and_b32 v1, 15, v108\n"
"v_lshrrev_b32 v2, 1, v1\n"
"v_xor_b32 v2, v2, v0\n"
"v_lshlrev_b32 v2, 4, v2\n"
"v_lshl_add_u32 v2, v1, 7, v2\n"
"s_add_u32 s74, s70, 16384\n"
"v_add_u32 v96, s74, v2\n"
"v_xor_b32 v97, 64, v96\n"
"v_add_u32 v98, 65536, v96\n"
"v_add_u32 v100, 65536, v97\n"
"v_xor_b32 v2, v1, v0\n"
"v_lshlrev_b32 v2, 4, v2\n"
"v_lshl_add_u32 v2, v1, 8, v2\n"
"v_add_u32 v114, 0, v2\n"
"v_xor_b32 v115, 64, v114\n"
"v_xor_b32 v116, 128, v114\n"
"v_xor_b32 v117, 192, v114\n"
"v_lshlrev_b32 v119, 4, v0\n"
"v_add_u32 v119, 0, v119\n"
"s_mul_i32 s74, s68, 8704\n"
"s_add_u32 s74, s74, 81920\n"
"v_mul_u32_u24 v2, 272, v1\n"
"v_add_u32 v2, s74, v2\n"
"v_lshl_add_u32 v125, v0, 3, v2\n"
"v_lshl_add_u32 v126, v0, 4, v2\n"
"s_lshl_b32 s73, s68, 10\n"
"s_add_u32 s71, s73, 0\n"
"s_mov_b32 s72, s69\n"
"v_mov_b32 v3, 0x3f803f80\n"
"v_accvgpr_write_b32 a192, v3\n"
"v_accvgpr_write_b32 a193, v3\n"
"v_accvgpr_write_b32 a194, v3\n"
"v_accvgpr_write_b32 a195, v3\n"
"v_accvgpr_write_b32 a212, v3\n"
"v_accvgpr_write_b32 a213, v3\n"
"v_accvgpr_write_b32 a214, v3\n"
"v_accvgpr_write_b32 a215, v3\n"
"v_accvgpr_write_b32 a216, v3\n"
"v_accvgpr_write_b32 a217, v3\n"
"v_accvgpr_write_b32 a218, v3\n"
"v_accvgpr_write_b32 a219, v3\n"
"v_accvgpr_write_b32 a220, v3\n"
"v_accvgpr_write_b32 a221, v3\n"
"v_accvgpr_write_b32 a222, v3\n"
"v_accvgpr_write_b32 a223, v3\n"
"v_accvgpr_write_b32 a224, v3\n"
"v_accvgpr_write_b32 a225, v3\n"
"v_accvgpr_write_b32 a226, v3\n"
"v_accvgpr_write_b32 a227, v3\n"
"s_lshl_b32 s73, s68, 9\n"
"v_add_u32 v99, s73, v112\n"
"v_add_u32 v103, s73, v113\n"
"v_add_u32 v107, s73, v105\n"
"v_add_u32 v111, s73, v106\n"
"v_cmp_eq_u32 vcc, 0, v1\n"
"s_nop 1\n"
"v_cndmask_b32 v4, 0, v3, vcc\n"
"v_accvgpr_write_b32 a0, v4\n"
"v_accvgpr_write_b32 a1, v4\n"
"v_accvgpr_write_b32 a2, v4\n"
"v_accvgpr_write_b32 a3, v4\n"
"v_cmp_eq_u32 vcc, 1, v1\n"
"s_nop 1\n"
"v_cndmask_b32 v4, 0, v3, vcc\n"
"v_accvgpr_write_b32 a4, v4\n"
"v_accvgpr_write_b32 a5, v4\n"
"v_accvgpr_write_b32 a6, v4\n"
"v_accvgpr_write_b32 a7, v4\n"
"s_lshl_b32 s73, s92, 2\n"
"s_add_u32 s73, s73, s68\n"
"s_lshl_b32 s74, s73, 19\n"
"s_lshl_b32 s73, s91, 12\n"
"s_add_u32 s74, s74, s73\n"
"s_add_u32 s74, s74, 8388608\n"
"s_add_u32 s56, s56, s74\n"
"s_addc_u32 s57, s57, 0\n"
"s_mov_b32 s58, 524288\n"
"s_mov_b32 s59, 0x00020000\n"
"s_and_b32 s73, s91, 7\n"
"s_cmp_lt_u32 s73, 4\n"
"s_cselect_b32 s58, s58, 0\n"
"s_waitcnt vmcnt(10)\n"
"s_load_dword s76, s[82:83], 0\n"
"v_lshlrev_b32 v160, 16, v128\n"
"v_and_b32 v161, 0xffff0000, v128\n"
"v_lshlrev_b32 v176, 16, v136\n"
"v_and_b32 v177, 0xffff0000, v136\n"
"v_lshlrev_b32 v162, 16, v129\n"
"v_and_b32 v163, 0xffff0000, v129\n"
"v_lshlrev_b32 v178, 16, v137\n"
"v_and_b32 v179, 0xffff0000, v137\n"
"v_lshlrev_b32 v164, 16, v130\n"
"v_and_b32 v165, 0xffff0000, v130\n"
"v_lshlrev_b32 v180, 16, v138\n"
"v_and_b32 v181, 0xffff0000, v138\n"
"v_lshlrev_b32 v166, 16, v131\n"
"v_and_b32 v167, 0xffff0000, v131\n"
"v_lshlrev_b32 v182, 16, v139\n"
"v_and_b32 v183, 0xffff0000, v139\n"
"v_lshlrev_b32 v168, 16, v132\n"
"v_and_b32 v169, 0xffff0000, v132\n"
"v_lshlrev_b32 v184, 16, v140\n"
"s_add_u32 s75, s86, 80896\n"
"buffer_load_dwordx4 v102, s[40:43], s75 offen offset:1024 lds\n"
"v_and_b32 v185, 0xffff0000, v140\n"
"v_lshlrev_b32 v170, 16, v133\n"
"v_and_b32 v171, 0xffff0000, v133\n"
"v_lshlrev_b32 v186, 16, v141\n"
"v_and_b32 v187, 0xffff0000, v141\n"
"v_lshlrev_b32 v172, 16, v134\n"
"v_and_b32 v173, 0xffff0000, v134\n"
"v_lshlrev_b32 v188, 16, v142\n"
"v_and_b32 v189, 0xffff0000, v142\n"
"v_lshlrev_b32 v174, 16, v135\n"
"v_and_b32 v175, 0xffff0000, v135\n"
"v_lshlrev_b32 v190, 16, v143\n"
"v_and_b32 v191, 0xffff0000, v143\n"
"v_pk_add_f32 v[224:225], v[160:161], v[162:163]\n"
"v_pk_add_f32 v[226:227], v[176:177], v[178:179]\n"
"v_pk_add_f32 v[224:225], v[224:225], v[164:165]\n"
"v_pk_add_f32 v[226:227], v[226:227], v[180:181]\n"
"v_pk_add_f32 v[224:225], v[224:225], v[166:167]\n"
"v_pk_add_f32 v[226:227], v[226:227], v[182:183]\n"
"s_add_u32 s75, s86, 96256\n"
"buffer_load_dwordx4 v101, s[40:43], s75 offen offset:2048 lds\n"
"v_pk_add_f32 v[224:225], v[224:225], v[168:169]\n"
"v_pk_add_f32 v[226:227], v[226:227], v[184:185]\n"
"v_pk_add_f32 v[224:225], v[224:225], v[170:171]\n"
"v_pk_add_f32 v[226:227], v[226:227], v[186:187]\n"
"v_pk_add_f32 v[224:225], v[224:225], v[172:173]\n"
"v_pk_add_f32 v[226:227], v[226:227], v[188:189]\n"
"v_pk_add_f32 v[224:225], v[224:225], v[174:175]\n"
"v_pk_add_f32 v[226:227], v[226:227], v[190:191]\n"
"v_add_f32 v228, v224, v225\n"
"v_add_f32 v230, v226, v227\n"
"s_nop 0\n"
"v_add_f32_dpp v228, v228, v228 quad_perm:[1,0,3,2] row_mask:0xf bank_mask:0xf\n"
"v_add_f32_dpp v230, v230, v230 quad_perm:[1,0,3,2] row_mask:0xf bank_mask:0xf\n"
"s_nop 0\n"
"v_add_f32_dpp v228, v228, v228 quad_perm:[2,3,0,1] row_mask:0xf bank_mask:0xf\n"
"v_add_f32_dpp v230, v230, v230 quad_perm:[2,3,0,1] row_mask:0xf bank_mask:0xf\n"
"s_nop 0\n"
"v_add_f32_dpp v228, v228, v228 row_half_mirror row_mask:0xf bank_mask:0xf\n"
"v_add_f32_dpp v230, v230, v230 row_half_mirror row_mask:0xf bank_mask:0xf\n"
"s_nop 0\n"
"v_add_f32_dpp v228, v228, v228 row_mirror row_mask:0xf bank_mask:0xf\n"
"v_add_f32_dpp v230, v230, v230 row_mirror row_mask:0xf bank_mask:0xf\n"
"v_mov_b32 v232, v228\n"
"s_add_u32 s75, s86, 111616\n"
"buffer_load_dwordx4 v102, s[40:43], s75 offen offset:3072 lds\n"
"v_mov_b32 v233, v230\n"
"v_permlane16_swap_b32 v228, v232\n"
"s_nop 0\n"
"v_permlane16_swap_b32 v230, v233\n"
"v_add_f32 v228, v228, v232\n"
"v_add_f32 v230, v230, v233\n"
"v_mov_b32 v232, v228\n"
"v_mov_b32 v233, v230\n"
"s_nop 0\n"
"v_permlane32_swap_b32 v228, v232\n"
"v_permlane32_swap_b32 v230, v233\n"
"v_add_f32 v228, v228, v232\n"
"v_add_f32 v230, v230, v233\n"
"v_mul_f32 v228, 0x3a800000, v228\n"
"v_mul_f32 v230, 0x3a800000, v230\n"
"v_mov_b32 v234, v228\n"
"v_mov_b32 v236, v230\n"
"v_pk_add_f32 v[192:193], v[160:161], v[228:229] op_sel_hi:[1,0] neg_lo:[0,1] neg_hi:[0,1]\n"
"v_pk_add_f32 v[208:209], v[176:177], v[230:231] op_sel_hi:[1,0] neg_lo:[0,1] neg_hi:[0,1]\n"
"v_pk_add_f32 v[194:195], v[162:163], v[228:229] op_sel_hi:[1,0] neg_lo:[0,1] neg_hi:[0,1]\n"
"v_pk_add_f32 v[210:211], v[178:179], v[230:231] op_sel_hi:[1,0] neg_lo:[0,1] neg_hi:[0,1]\n"
"s_bfe_u32 s85, s78, 0x40004\n"
"s_lshl_b32 s86, s85, 7\n"
"s_lshl_b32 s87, s85, 19\n"
"s_add_u32 m0, s69, 0\n"
"s_add_u32 s75, s87, 1024\n"
"buffer_load_dwordx4 v104, s[44:47], s75 offen offset:0 lds\n"
"v_pk_add_f32 v[196:197], v[164:165], v[228:229] op_sel_hi:[1,0] neg_lo:[0,1] neg_hi:[0,1]\n"
"v_pk_add_f32 v[212:213], v[180:181], v[230:231] op_sel_hi:[1,0] neg_lo:[0,1] neg_hi:[0,1]\n"
"v_pk_add_f32 v[198:199], v[166:167], v[228:229] op_sel_hi:[1,0] neg_lo:[0,1] neg_hi:[0,1]\n"
"v_pk_add_f32 v[214:215], v[182:183], v[230:231] op_sel_hi:[1,0] neg_lo:[0,1] neg_hi:[0,1]\n"
"v_pk_add_f32 v[200:201], v[168:169], v[228:229] op_sel_hi:[1,0] neg_lo:[0,1] neg_hi:[0,1]\n"
"v_pk_add_f32 v[216:217], v[184:185], v[230:231] op_sel_hi:[1,0] neg_lo:[0,1] neg_hi:[0,1]\n"
"v_pk_add_f32 v[202:203], v[170:171], v[228:229] op_sel_hi:[1,0] neg_lo:[0,1] neg_hi:[0,1]\n"
"v_pk_add_f32 v[218:219], v[186:187], v[230:231] op_sel_hi:[1,0] neg_lo:[0,1] neg_hi:[0,1]\n"
"v_pk_add_f32 v[204:205], v[172:173], v[228:229] op_sel_hi:[1,0] neg_lo:[0,1] neg_hi:[0,1]\n"
"v_pk_add_f32 v[220:221], v[188:189], v[230:231] op_sel_hi:[1,0] neg_lo:[0,1] neg_hi:[0,1]\n"
"v_pk_add_f32 v[206:207], v[174:175], v[228:229] op_sel_hi:[1,0] neg_lo:[0,1] neg_hi:[0,1]\n"
"v_pk_add_f32 v[222:223], v[190:191], v[230:231] op_sel_hi:[1,0] neg_lo:[0,1] neg_hi:[0,1]\n"
"v_pk_mul_f32 v[224:225], v[192:193], v[192:193]\n"
"v_pk_mul_f32 v[226:227], v[208:209], v[208:209]\n"
"v_pk_fma_f32 v[224:225], v[194:195], v[194:195], v[224:225]\n"
"v_pk_fma_f32 v[226:227], v[210:211], v[210:211], v[226:227]\n"
"v_pk_fma_f32 v[224:225], v[196:197], v[196:197], v[224:225]\n"
"v_pk_fma_f32 v[226:227], v[212:213], v[212:213], v[226:227]\n"
"v_pk_fma_f32 v[224:225], v[198:199], v[198:199], v[224:225]\n"
"s_add_u32 s75, s87, 128\n"
"buffer_load_dwordx4 v104, s[44:47], s75 offen offset:1024 lds\n"
"v_pk_fma_f32 v[226:227], v[214:215], v[214:215], v[226:227]\n"
"v_pk_fma_f32 v[224:225], v[200:201], v[200:201], v[224:225]\n"
"v_pk_fma_f32 v[226:227], v[216:217], v[216:217], v[226:227]\n"
"v_pk_fma_f32 v[224:225], v[202:203], v[202:203], v[224:225]\n"
"v_pk_fma_f32 v[226:227], v[218:219], v[218:219], v[226:227]\n"
"v_pk_fma_f32 v[224:225], v[204:205], v[204:205], v[224:225]\n"
"v_pk_fma_f32 v[226:227], v[220:221], v[220:221], v[226:227]\n"
"v_pk_fma_f32 v[224:225], v[206:207], v[206:207], v[224:225]\n"
"v_pk_fma_f32 v[226:227], v[222:223], v[222:223], v[226:227]\n"
"v_add_f32 v238, v224, v225\n"
"v_add_f32 v240, v226, v227\n"
"v_cvt_pk_bf16_f32 v234, v234, v234\n"
"v_cvt_pk_bf16_f32 v236, v236, v236\n"
"v_lshlrev_b32 v234, 16, v234\n"
"v_lshlrev_b32 v236, 16, v236\n"
"v_add_f32_dpp v238, v238, v238 quad_perm:[1,0,3,2] row_mask:0xf bank_mask:0xf\n"
"v_add_f32_dpp v240, v240, v240 quad_perm:[1,0,3,2] row_mask:0xf bank_mask:0xf\n"
"s_nop 0\n"
"v_add_f32_dpp v238, v238, v238 quad_perm:[2,3,0,1] row_mask:0xf bank_mask:0xf\n"
"v_add_f32_dpp v240, v240, v240 quad_perm:[2,3,0,1] row_mask:0xf bank_mask:0xf\n"
"s_add_u32 m0, s70, 16384\n"
"s_add_u32 s75, s86, 0\n"
"buffer_load_dwordx4 v101, s[40:43], s75 offen offset:0 lds\n"
"v_add_f32_dpp v238, v238, v238 row_half_mirror row_mask:0xf bank_mask:0xf\n"
"v_add_f32_dpp v240, v240, v240 row_half_mirror row_mask:0xf bank_mask:0xf\n"
"s_nop 0\n"
"v_add_f32_dpp v238, v238, v238 row_mirror row_mask:0xf bank_mask:0xf\n"
"v_add_f32_dpp v240, v240, v240 row_mirror row_mask:0xf bank_mask:0xf\n"
"v_mov_b32 v232, v238\n"
"v_mov_b32 v233, v240\n"
"s_nop 0\n"
"v_permlane16_swap_b32 v238, v232\n"
"v_permlane16_swap_b32 v240, v233\n"
"v_add_f32 v238, v238, v232\n"
"v_add_f32 v240, v240, v233\n"
"v_mov_b32 v232, v238\n"
"v_mov_b32 v233, v240\n"
"s_nop 0\n"
"v_permlane32_swap_b32 v238, v232\n"
"v_permlane32_swap_b32 v240, v233\n"
"v_add_f32 v238, v238, v232\n"
"v_add_f32 v240, v240, v233\n"
"v_pk_add_f32 v[192:193], v[160:161], v[234:235] op_sel_hi:[1,0] neg_lo:[0,1] neg_hi:[0,1]\n"
"v_pk_add_f32 v[208:209], v[176:177], v[236:237] op_sel_hi:[1,0] neg_lo:[0,1] neg_hi:[0,1]\n"
"v_pk_add_f32 v[194:195], v[162:163], v[234:235] op_sel_hi:[1,0] neg_lo:[0,1] neg_hi:[0,1]\n"
"s_add_u32 s75, s86, 15360\n"
"buffer_load_dwordx4 v102, s[40:43], s75 offen offset:1024 lds\n"
"v_pk_add_f32 v[210:211], v[178:179], v[236:237] op_sel_hi:[1,0] neg_lo:[0,1] neg_hi:[0,1]\n"
"v_pk_add_f32 v[196:197], v[164:165], v[234:235] op_sel_hi:[1,0] neg_lo:[0,1] neg_hi:[0,1]\n"
"v_pk_add_f32 v[212:213], v[180:181], v[236:237] op_sel_hi:[1,0] neg_lo:[0,1] neg_hi:[0,1]\n"
"v_pk_add_f32 v[198:199], v[166:167], v[234:235] op_sel_hi:[1,0] neg_lo:[0,1] neg_hi:[0,1]\n"
"v_pk_add_f32 v[214:215], v[182:183], v[236:237] op_sel_hi:[1,0] neg_lo:[0,1] neg_hi:[0,1]\n"
"v_pk_add_f32 v[200:201], v[168:169], v[234:235] op_sel_hi:[1,0] neg_lo:[0,1] neg_hi:[0,1]\n"
"v_pk_add_f32 v[216:217], v[184:185], v[236:237] op_sel_hi:[1,0] neg_lo:[0,1] neg_hi:[0,1]\n"
"v_pk_add_f32 v[202:203], v[170:171], v[234:235] op_sel_hi:[1,0] neg_lo:[0,1] neg_hi:[0,1]\n"
"v_pk_add_f32 v[218:219], v[186:187], v[236:237] op_sel_hi:[1,0] neg_lo:[0,1] neg_hi:[0,1]\n"
"v_pk_add_f32 v[204:205], v[172:173], v[234:235] op_sel_hi:[1,0] neg_lo:[0,1] neg_hi:[0,1]\n"
"v_pk_add_f32 v[220:221], v[188:189], v[236:237] op_sel_hi:[1,0] neg_lo:[0,1] neg_hi:[0,1]\n"
"v_pk_add_f32 v[206:207], v[174:175], v[234:235] op_sel_hi:[1,0] neg_lo:[0,1] neg_hi:[0,1]\n"
"v_pk_add_f32 v[222:223], v[190:191], v[236:237] op_sel_hi:[1,0] neg_lo:[0,1] neg_hi:[0,1]\n"
"v_mul_f32 v238, 0x3a800000, v238\n"
"v_mul_f32 v240, 0x3a800000, v240\n"
"v_cvt_pk_bf16_f32 v238, v238, v238\n"
"v_cvt_pk_bf16_f32 v240, v240, v240\n"
"v_lshlrev_b32 v238, 16, v238\n"
"v_lshlrev_b32 v240, 16, v240\n"
"s_add_u32 s75, s86, 30720\n"
"buffer_load_dwordx4 v101, s[40:43], s75 offen offset:2048 lds\n"
"v_add_f32 v238, 0x3727c5ac, v238\n"
"v_add_f32 v240, 0x3727c5ac, v240\n"
"v_cvt_pk_bf16_f32 v238, v238, v238\n"
"v_cvt_pk_bf16_f32 v240, v240, v240\n"
"v_lshlrev_b32 v238, 16, v238\n"
"v_lshlrev_b32 v240, 16, v240\n"
"v_sqrt_f32 v238, v238\n"
"v_sqrt_f32 v240, v240\n"
"v_cvt_pk_bf16_f32 v128, v192, v193\n"
"v_cvt_pk_bf16_f32 v136, v208, v209\n"
"v_cvt_pk_bf16_f32 v129, v194, v195\n"
"v_cvt_pk_bf16_f32 v137, v210, v211\n"
"v_cvt_pk_bf16_f32 v130, v196, v197\n"
"v_cvt_pk_bf16_f32 v138, v212, v213\n"
"v_cvt_pk_bf16_f32 v131, v198, v199\n"
"v_cvt_pk_bf16_f32 v139, v214, v215\n"
"v_cvt_pk_bf16_f32 v132, v200, v201\n"
"v_cvt_pk_bf16_f32 v140, v216, v217\n"
"v_cvt_pk_bf16_f32 v133, v202, v203\n"
"s_add_u32 s75, s86, 46080\n"
"buffer_load_dwordx4 v102, s[40:43], s75 offen offset:3072 lds\n"
"v_cvt_pk_bf16_f32 v141, v218, v219\n"
"v_cvt_pk_bf16_f32 v134, v204, v205\n"
"v_cvt_pk_bf16_f32 v142, v220, v221\n"
"v_cvt_pk_bf16_f32 v135, v206, v207\n"
"v_cvt_pk_bf16_f32 v143, v222, v223\n"
"v_cvt_pk_bf16_f32 v238, v238, v238\n"
"v_cvt_pk_bf16_f32 v240, v240, v240\n"
"v_lshlrev_b32 v238, 16, v238\n"
"v_lshlrev_b32 v240, 16, v240\n"
"v_rcp_f32 v242, v238\n"
"v_rcp_f32 v244, v240\n"
"v_lshlrev_b32 v192, 16, v128\n"
"v_and_b32 v193, 0xffff0000, v128\n"
"v_lshlrev_b32 v208, 16, v136\n"
"v_and_b32 v209, 0xffff0000, v136\n"
"v_lshlrev_b32 v194, 16, v129\n"
"v_and_b32 v195, 0xffff0000, v129\n"
"v_lshlrev_b32 v210, 16, v137\n"
"v_and_b32 v211, 0xffff0000, v137\n"
"s_add_u32 m0, s69, 8192\n"
"s_add_u32 s75, s87, 263168\n"
"buffer_load_dwordx4 v104, s[44:47], s75 offen offset:0 lds\n"
"v_lshlrev_b32 v196, 16, v130\n"
"v_and_b32 v197, 0xffff0000, v130\n"
"v_lshlrev_b32 v212, 16, v138\n"
"v_and_b32 v213, 0xffff0000, v138\n"
"v_lshlrev_b32 v198, 16, v131\n"
"v_and_b32 v199, 0xffff0000, v131\n"
"v_lshlrev_b32 v214, 16, v139\n"
"v_and_b32 v215, 0xffff0000, v139\n"
"v_lshlrev_b32 v200, 16, v132\n"
"v_and_b32 v201, 0xffff0000, v132\n"
"v_lshlrev_b32 v216, 16, v140\n"
"v_and_b32 v217, 0xffff0000, v140\n"
"v_lshlrev_b32 v202, 16, v133\n"
"v_and_b32 v203, 0xffff0000, v133\n"
"v_lshlrev_b32 v218, 16, v141\n"
"v_and_b32 v219, 0xffff0000, v141\n"
"v_lshlrev_b32 v204, 16, v134\n"
"v_and_b32 v205, 0xffff0000, v134\n"
"v_lshlrev_b32 v220, 16, v142\n"
"s_add_u32 s75, s87, 262272\n"
"buffer_load_dwordx4 v104, s[44:47], s75 offen offset:1024 lds\n"
"v_and_b32 v221, 0xffff0000, v142\n"
"v_lshlrev_b32 v206, 16, v135\n"
"v_and_b32 v207, 0xffff0000, v135\n"
"v_lshlrev_b32 v222, 16, v143\n"
"v_and_b32 v223, 0xffff0000, v143\n"
"v_pk_mul_f32 v[192:193], v[192:193], v[242:243] op_sel_hi:[1,0]\n"
"v_pk_mul_f32 v[208:209], v[208:209], v[244:245] op_sel_hi:[1,0]\n"
"v_pk_mul_f32 v[194:195], v[194:195], v[242:243] op_sel_hi:[1,0]\n"
"v_pk_mul_f32 v[210:211], v[210:211], v[244:245] op_sel_hi:[1,0]\n"
"v_pk_mul_f32 v[196:197], v[196:197], v[242:243] op_sel_hi:[1,0]\n"
"v_pk_mul_f32 v[212:213], v[212:213], v[244:245] op_sel_hi:[1,0]\n"
"v_pk_mul_f32 v[198:199], v[198:199], v[242:243] op_sel_hi:[1,0]\n"
"v_pk_mul_f32 v[214:215], v[214:215], v[244:245] op_sel_hi:[1,0]\n"
"v_pk_mul_f32 v[200:201], v[200:201], v[242:243] op_sel_hi:[1,0]\n"
"v_pk_mul_f32 v[216:217], v[216:217], v[244:245] op_sel_hi:[1,0]\n"
"v_pk_mul_f32 v[202:203], v[202:203], v[242:243] op_sel_hi:[1,0]\n"
"v_pk_mul_f32 v[218:219], v[218:219], v[244:245] op_sel_hi:[1,0]\n"
"v_pk_mul_f32 v[204:205], v[204:205], v[242:243] op_sel_hi:[1,0]\n"
"v_pk_mul_f32 v[220:221], v[220:221], v[244:245] op_sel_hi:[1,0]\n"
"s_add_u32 m0, s70, 20480\n"
"s_add_u32 s75, s86, 65536\n"
"buffer_load_dwordx4 v101, s[40:43], s75 offen offset:0 lds\n"
"v_pk_mul_f32 v[206:207], v[206:207], v[242:243] op_sel_hi:[1,0]\n"
"v_pk_mul_f32 v[222:223], v[222:223], v[244:245] op_sel_hi:[1,0]\n"
"v_cvt_pk_bf16_f32 v128, v192, v193\n"
"v_cvt_pk_bf16_f32 v136, v208, v209\n"
"v_cvt_pk_bf16_f32 v129, v194, v195\n"
"v_cvt_pk_bf16_f32 v137, v210, v211\n"
"v_cvt_pk_bf16_f32 v130, v196, v197\n"
"v_cvt_pk_bf16_f32 v138, v212, v213\n"
"v_cvt_pk_bf16_f32 v131, v198, v199\n"
"v_cvt_pk_bf16_f32 v139, v214, v215\n"
"v_cvt_pk_bf16_f32 v132, v200, v201\n"
"v_cvt_pk_bf16_f32 v140, v216, v217\n"
"v_cvt_pk_bf16_f32 v133, v202, v203\n"
"v_cvt_pk_bf16_f32 v141, v218, v219\n"
"v_cvt_pk_bf16_f32 v134, v204, v205\n"
"v_cvt_pk_bf16_f32 v142, v220, v221\n"
"v_cvt_pk_bf16_f32 v135, v206, v207\n"
"v_cvt_pk_bf16_f32 v143, v222, v223\n"
"v_dot2_f32_bf16 v228, v128, v144, 0\n"
"s_add_u32 s75, s86, 80896\n"
"buffer_load_dwordx4 v102, s[40:43], s75 offen offset:1024 lds\n"
"v_dot2_f32_bf16 v230, v136, v144, 0\n"
"v_dot2_f32_bf16 v228, v129, v145, v228\n"
"v_dot2_f32_bf16 v230, v137, v145, v230\n"
"v_dot2_f32_bf16 v228, v130, v146, v228\n"
"v_dot2_f32_bf16 v230, v138, v146, v230\n"
"v_dot2_f32_bf16 v228, v131, v147, v228\n"
"v_dot2_f32_bf16 v230, v139, v147, v230\n"
"v_dot2_f32_bf16 v228, v132, v148, v228\n"
"v_dot2_f32_bf16 v230, v140, v148, v230\n"
"v_dot2_f32_bf16 v228, v133, v149, v228\n"
"v_dot2_f32_bf16 v230, v141, v149, v230\n"
"v_dot2_f32_bf16 v228, v134, v150, v228\n"
"v_dot2_f32_bf16 v230, v142, v150, v230\n"
"v_dot2_f32_bf16 v228, v135, v151, v228\n"
"v_dot2_f32_bf16 v230, v143, v151, v230\n"
"v_lshrrev_b32 v238, 16, v238\n"
"v_or_b32 v246, v234, v238\n"
"v_lshrrev_b32 v240, 16, v240\n"
"v_or_b32 v247, v236, v240\n"
"s_add_u32 s75, s86, 96256\n"
"buffer_load_dwordx4 v101, s[40:43], s75 offen offset:2048 lds\n"
"v_lshlrev_b32 v232, 24, v108\n"
"s_lshl_b32 s90, s77, 2\n"
"s_add_u32 s90, s90, 4096\n"
"buffer_store_dwordx2 v[246:247], v232, s[60:63], s90 offen offset:0 sc1\n"
"s_nop 1\n"
"v_add_f32_dpp v228, v228, v228 quad_perm:[1,0,3,2] row_mask:0xf bank_mask:0xf\n"
"v_add_f32_dpp v230, v230, v230 quad_perm:[1,0,3,2] row_mask:0xf bank_mask:0xf\n"
"s_nop 0\n"
"v_add_f32_dpp v228, v228, v228 quad_perm:[2,3,0,1] row_mask:0xf bank_mask:0xf\n"
"v_add_f32_dpp v230, v230, v230 quad_perm:[2,3,0,1] row_mask:0xf bank_mask:0xf\n"
"s_nop 0\n"
"v_add_f32_dpp v228, v228, v228 row_half_mirror row_mask:0xf bank_mask:0xf\n"
"v_add_f32_dpp v230, v230, v230 row_half_mirror row_mask:0xf bank_mask:0xf\n"
"s_nop 0\n"
"v_add_f32_dpp v228, v228, v228 row_mirror row_mask:0xf bank_mask:0xf\n"
"v_add_f32_dpp v230, v230, v230 row_mirror row_mask:0xf bank_mask:0xf\n"
"v_mov_b32 v232, v228\n"
"v_mov_b32 v233, v230\n"
"s_nop 0\n"
"v_permlane16_swap_b32 v228, v232\n"
"v_permlane16_swap_b32 v230, v233\n"
"v_add_f32 v228, v228, v232\n"
"v_add_f32 v230, v230, v233\n"
"s_add_u32 s75, s86, 111616\n"
"buffer_load_dwordx4 v102, s[40:43], s75 offen offset:3072 lds\n"
"v_mov_b32 v232, v228\n"
"v_mov_b32 v233, v230\n"
"s_nop 0\n"
"v_permlane32_swap_b32 v228, v232\n"
"v_permlane32_swap_b32 v230, v233\n"
"v_add_f32 v228, v228, v232\n"
"v_add_f32 v230, v230, v233\n"
"v_cvt_pk_bf16_f32 v232, v228, v228\n"
"v_cvt_pk_bf16_f32 v233, v230, v230\n"
"v_ashrrev_i32 v234, 31, v232\n"
"v_ashrrev_i32 v236, 31, v233\n"
"v_or_b32 v234, 0x80000000, v234\n"
"v_or_b32 v236, 0x80000000, v236\n"
"v_xor_b32 v232, v232, v234\n"
"v_xor_b32 v233, v233, v236\n"
"v_and_b32 v232, 0xffff0000, v232\n"
"v_and_b32 v233, 0xffff0000, v233\n"
"v_cmp_o_f32 vcc, v228, v228\n"
"s_sub_u32 s73, 2047, s77\n"
"s_nop 0\n"
"v_cndmask_b32 v232, 0, v232, vcc\n"
"v_or_b32 v232, s73, v232\n"
"v_cmp_o_f32 vcc, v230, v230\n"
"s_sub_u32 s73, 2046, s77\n"
"s_nop 0\n"
"v_cndmask_b32 v233, 0, v233, vcc\n"
"v_or_b32 v233, s73, v233\n"
"v_max_u32 v232, v232, v233\n"
"s_lshl_b32 s73, s68, 2\n"
"s_add_u32 s73, s73, 147456\n"
"v_mov_b32 v233, s73\n"
"ds_write_b32 v233, v232 offset:0\n"
"s_bfe_u32 s85, s78, 0x40008\n"
"s_lshl_b32 s86, s85, 7\n"
"s_lshl_b32 s87, s85, 19\n"
"s_waitcnt vmcnt(13) lgkmcnt(0)\n"
"s_barrier\n"
"s_add_u32 s76, s76, 1\n"
"s_load_dwordx2 s[82:83], %[ka], 72\n"
"ds_read_b128 v[64:67], v98 offset:0\n"
"ds_read_b128 v[68:71], v98 offset:2048\n"
"ds_read_b128 v[72:75], v98 offset:4096\n"
"ds_read_b128 v[76:79], v98 offset:6144\n"
"ds_read_b64_tr_b16 v[0:1], v105 offset:0\n"
"ds_read_b64_tr_b16 v[2:3], v105 offset:256\n"
"ds_read_b64_tr_b16 v[4:5], v106 offset:0\n"
"ds_read_b64_tr_b16 v[6:7], v106 offset:256\n"
"ds_read_b64_tr_b16 v[8:9], v105 offset:512\n"
"ds_read_b64_tr_b16 v[10:11], v105 offset:768\n"
"ds_read_b64_tr_b16 v[12:13], v106 offset:512\n"
"ds_read_b64_tr_b16 v[14:15], v106 offset:768\n"
"ds_read_b64_tr_b16 v[16:17], v105 offset:1024\n"
"ds_read_b64_tr_b16 v[18:19], v105 offset:1280\n"
"ds_read_b64_tr_b16 v[20:21], v106 offset:1024\n"
"ds_read_b64_tr_b16 v[22:23], v106 offset:1280\n"
"ds_read_b64_tr_b16 v[24:25], v105 offset:1536\n"
"ds_read_b64_tr_b16 v[26:27], v105 offset:1792\n"
"ds_read_b64_tr_b16 v[28:29], v106 offset:1536\n"
"ds_read_b64_tr_b16 v[30:31], v106 offset:1792\n"
"ds_read_b64_tr_b16 a[44:45], v107 offset:0\n"
"ds_read_b64_tr_b16 a[46:47], v107 offset:256\n"
"ds_read_b64_tr_b16 a[48:49], v111 offset:0\n"
"ds_read_b64_tr_b16 a[50:51], v111 offset:256\n"
"s_waitcnt lgkmcnt(0)\n"
"v_mfma_f32_16x16x32_bf16 v[128:131], v[0:3], v[64:67], 0\n"
"ds_read_b128 v[80:83], v100 offset:0\n"
"v_mfma_f32_16x16x32_bf16 v[132:135], v[0:3], v[68:71], 0\n"
"s_add_u32 m0, s69, 49152\n"
"ds_read_b128 v[84:87], v100 offset:2048\n"
"v_mfma_f32_16x16x32_bf16 v[136:139], v[0:3], v[72:75], 0\n"
"s_add_u32 s75, s87, 1024\n"
"ds_read_b128 v[88:91], v100 offset:4096\n"
"buffer_load_dwordx4 v104, s[44:47], s75 offen offset:0 lds\n"
"v_mfma_f32_16x16x32_bf16 v[140:143], v[0:3], v[76:79], 0\n"
"ds_read_b128 v[92:95], v100 offset:6144\n"
"v_mfma_f32_16x16x32_bf16 a[36:39], a[0:3], a[44:47], 0\n"
"s_add_u32 s75, s87, 128\n"
"ds_read_b64_tr_b16 v[32:33], v105 offset:8192\n"
"buffer_load_dwordx4 v104, s[44:47], s75 offen offset:1024 lds\n"
"v_mfma_f32_16x16x32_bf16 v[156:159], v[4:7], v[76:79], 0\n"
"s_add_u32 m0, s70, 114688\n"
"ds_read_b64_tr_b16 v[34:35], v105 offset:8448\n"
"v_mfma_f32_16x16x32_bf16 v[152:155], v[4:7], v[72:75], 0\n"
"s_add_u32 s75, s86, 0\n"
"ds_read_b64_tr_b16 v[36:37], v106 offset:8192\n"
"buffer_load_dwordx4 v101, s[40:43], s75 offen offset:0 lds\n"
"v_mfma_f32_16x16x32_bf16 v[148:151], v[4:7], v[68:71], 0\n"
"ds_read_b64_tr_b16 v[38:39], v106 offset:8448\n"
"v_mfma_f32_16x16x32_bf16 v[144:147], v[4:7], v[64:67], 0\n"
"s_add_u32 s75, s86, 15360\n"
"ds_read_b64_tr_b16 v[40:41], v105 offset:8704\n"
"buffer_load_dwordx4 v102, s[40:43], s75 offen offset:1024 lds\n"
"v_mfma_f32_16x16x32_bf16 a[36:39], a[4:7], a[48:51], a[36:39]\n"
"ds_read_b64_tr_b16 v[42:43], v105 offset:8960\n"
"v_mfma_f32_16x16x32_bf16 v[160:163], v[8:11], v[64:67], 0\n"
"s_add_u32 s75, s86, 30720\n"
"ds_read_b64_tr_b16 v[44:45], v106 offset:8704\n"
"buffer_load_dwordx4 v101, s[40:43], s75 offen offset:2048 lds\n"
"v_mfma_f32_16x16x32_bf16 v[164:167], v[8:11], v[68:71], 0\n"
"ds_read_b64_tr_b16 v[46:47], v106 offset:8960\n"
"v_mfma_f32_16x16x32_bf16 v[168:171], v[8:11], v[72:75], 0\n"
"s_add_u32 s75, s86, 46080\n"
"ds_read_b64_tr_b16 v[48:49], v105 offset:9216\n"
"buffer_load_dwordx4 v102, s[40:43], s75 offen offset:3072 lds\n"
"v_mfma_f32_16x16x32_bf16 v[172:175], v[8:11], v[76:79], 0\n"
"s_add_u32 m0, s69, 57344\n"
"ds_read_b64_tr_b16 v[50:51], v105 offset:9472\n"
"v_mfma_f32_16x16x32_bf16 v[188:191], v[12:15], v[76:79], 0\n"
"s_add_u32 s75, s87, 263168\n"
"ds_read_b64_tr_b16 v[52:53], v106 offset:9216\n"
"buffer_load_dwordx4 v104, s[44:47], s75 offen offset:0 lds\n"
"v_mfma_f32_16x16x32_bf16 v[184:187], v[12:15], v[72:75], 0\n"
"ds_read_b64_tr_b16 v[54:55], v106 offset:9472\n"
"v_mfma_f32_16x16x32_bf16 v[180:183], v[12:15], v[68:71], 0\n"
"s_add_u32 s75, s87, 262272\n"
"ds_read_b64_tr_b16 v[56:57], v105 offset:9728\n"
"buffer_load_dwordx4 v104, s[44:47], s75 offen offset:1024 lds\n"
"v_mfma_f32_16x16x32_bf16 v[176:179], v[12:15], v[64:67], 0\n"
"s_add_u32 m0, s70, 118784\n"
"ds_read_b64_tr_b16 v[58:59], v105 offset:9984\n"
"v_mfma_f32_16x16x32_bf16 v[192:195], v[16:19], v[64:67], 0\n"
"s_add_u32 s75, s86, 65536\n"
"ds_read_b64_tr_b16 v[60:61], v106 offset:9728\n"
"buffer_load_dwordx4 v101, s[40:43], s75 offen offset:0 lds\n"
"v_mfma_f32_16x16x32_bf16 v[196:199], v[16:19], v[68:71], 0\n"
"ds_read_b64_tr_b16 v[62:63], v106 offset:9984\n"
"v_mfma_f32_16x16x32_bf16 v[200:203], v[16:19], v[72:75], 0\n"
"s_add_u32 s75, s86, 80896\n"
"ds_read_b64_tr_b16 a[52:53], v107 offset:8192\n"
"buffer_load_dwordx4 v102, s[40:43], s75 offen offset:1024 lds\n"
"v_mfma_f32_16x16x32_bf16 v[204:207], v[16:19], v[76:79], 0\n"
"ds_read_b64_tr_b16 a[54:55], v107 offset:8448\n"
"v_mfma_f32_16x16x32_bf16 v[220:223], v[20:23], v[76:79], 0\n"
"s_add_u32 s75, s86, 96256\n"
"ds_read_b64_tr_b16 a[56:57], v111 offset:8192\n"
"buffer_load_dwordx4 v101, s[40:43], s75 offen offset:2048 lds\n"
"v_mfma_f32_16x16x32_bf16 v[216:219], v[20:23], v[72:75], 0\n"
"ds_read_b64_tr_b16 a[58:59], v111 offset:8448\n"
"v_mfma_f32_16x16x32_bf16 v[212:215], v[20:23], v[68:71], 0\n"
"s_add_u32 s75, s86, 111616\n"
"buffer_load_dwordx4 v102, s[40:43], s75 offen offset:3072 lds\n"
"v_mfma_f32_16x16x32_bf16 v[208:211], v[20:23], v[64:67], 0\n"
"v_mfma_f32_16x16x32_bf16 v[224:227], v[24:27], v[64:67], 0\n"
"v_mfma_f32_16x16x32_bf16 v[228:231], v[24:27], v[68:71], 0\n"
"v_mfma_f32_16x16x32_bf16 v[232:235], v[24:27], v[72:75], 0\n"
"s_bfe_u32 s85, s78, 0x4000c\n"
"v_mfma_f32_16x16x32_bf16 v[236:239], v[24:27], v[76:79], 0\n"
"s_lshl_b32 s86, s85, 7\n"
"v_mfma_f32_16x16x32_bf16 v[252:255], v[28:31], v[76:79], 0\n"
"s_lshl_b32 s87, s85, 19\n"
"v_mfma_f32_16x16x32_bf16 v[248:251], v[28:31], v[72:75], 0\n"
"v_mfma_f32_16x16x32_bf16 v[244:247], v[28:31], v[68:71], 0\n"
"v_mfma_f32_16x16x32_bf16 v[240:243], v[28:31], v[64:67], 0\n"
"s_waitcnt vmcnt(12) lgkmcnt(0)\n"
"s_barrier\n"
"v_mfma_f32_16x16x32_bf16 v[128:131], v[32:35], v[80:83], v[128:131]\n"
"v_mfma_f32_16x16x32_bf16 v[132:135], v[32:35], v[84:87], v[132:135]\n"
"ds_read_b128 v[64:67], v96 offset:0\n"
"v_mfma_f32_16x16x32_bf16 v[136:139], v[32:35], v[88:91], v[136:139]\n"
"s_add_u32 m0, s69, 65536\n"
"ds_read_b128 v[68:71], v96 offset:2048\n"
"v_mfma_f32_16x16x32_bf16 v[140:143], v[32:35], v[92:95], v[140:143]\n"
"s_add_u32 s75, s87, 1024\n"
"ds_read_b128 v[72:75], v96 offset:4096\n"
"buffer_load_dwordx4 v104, s[44:47], s75 offen offset:0 lds\n"
"v_mfma_f32_16x16x32_bf16 a[36:39], a[0:3], a[52:55], a[36:39]\n"
"ds_read_b128 v[76:79], v96 offset:6144\n"
"v_mfma_f32_16x16x32_bf16 v[156:159], v[36:39], v[92:95], v[156:159]\n"
"ds_read_b64_tr_b16 v[0:1], v112 offset:0\n"
"v_mfma_f32_16x16x32_bf16 v[152:155], v[36:39], v[88:91], v[152:155]\n"
"ds_read_b64_tr_b16 v[2:3], v112 offset:256\n"
"v_mfma_f32_16x16x32_bf16 v[148:151], v[36:39], v[84:87], v[148:151]\n"
"ds_read_b64_tr_b16 v[4:5], v113 offset:0\n"
"v_mfma_f32_16x16x32_bf16 v[144:147], v[36:39], v[80:83], v[144:147]\n"
"s_add_u32 s75, s87, 128\n"
"ds_read_b64_tr_b16 v[6:7], v113 offset:256\n"
"buffer_load_dwordx4 v104, s[44:47], s75 offen offset:1024 lds\n"
"v_mfma_f32_16x16x32_bf16 a[36:39], a[4:7], a[56:59], a[36:39]\n"
"ds_read_b64_tr_b16 v[8:9], v112 offset:512\n"
"v_mfma_f32_16x16x32_bf16 v[160:163], v[40:43], v[80:83], v[160:163]\n"
"ds_read_b64_tr_b16 v[10:11], v112 offset:768\n"
"v_mfma_f32_16x16x32_bf16 v[164:167], v[40:43], v[84:87], v[164:167]\n"
"ds_read_b64_tr_b16 v[12:13], v113 offset:512\n"
"v_mfma_f32_16x16x32_bf16 v[168:171], v[40:43], v[88:91], v[168:171]\n"
"s_add_u32 m0, s70, 81920\n"
"ds_read_b64_tr_b16 v[14:15], v113 offset:768\n"
"v_mfma_f32_16x16x32_bf16 v[172:175], v[40:43], v[92:95], v[172:175]\n"
"s_add_u32 s75, s86, 0\n"
"ds_read_b64_tr_b16 v[16:17], v112 offset:1024\n"
"buffer_load_dwordx4 v101, s[40:43], s75 offen offset:0 lds\n"
"v_mfma_f32_16x16x32_bf16 v[188:191], v[44:47], v[92:95], v[188:191]\n"
"ds_read_b64_tr_b16 v[18:19], v112 offset:1280\n"
"v_mfma_f32_16x16x32_bf16 v[184:187], v[44:47], v[88:91], v[184:187]\n"
"ds_read_b64_tr_b16 v[20:21], v113 offset:1024\n"
"v_mfma_f32_16x16x32_bf16 v[180:183], v[44:47], v[84:87], v[180:183]\n"
"ds_read_b64_tr_b16 v[22:23], v113 offset:1280\n"
"v_mfma_f32_16x16x32_bf16 v[176:179], v[44:47], v[80:83], v[176:179]\n"
"ds_read_b64_tr_b16 v[24:25], v112 offset:1536\n"
"v_mfma_f32_16x16x32_bf16 v[192:195], v[48:51], v[80:83], v[192:195]\n"
"s_add_u32 s75, s86, 15360\n"
"ds_read_b64_tr_b16 v[26:27], v112 offset:1792\n"
"buffer_load_dwordx4 v102, s[40:43], s75 offen offset:1024 lds\n"
"v_mfma_f32_16x16x32_bf16 v[196:199], v[48:51], v[84:87], v[196:199]\n"
"ds_read_b64_tr_b16 v[28:29], v113 offset:1536\n"
"v_mfma_f32_16x16x32_bf16 v[200:203], v[48:51], v[88:91], v[200:203]\n"
"ds_read_b64_tr_b16 v[30:31], v113 offset:1792\n"
"v_mfma_f32_16x16x32_bf16 v[204:207], v[48:51], v[92:95], v[204:207]\n"
"ds_read_b64_tr_b16 a[44:45], v99 offset:0\n"
"v_mfma_f32_16x16x32_bf16 v[220:223], v[52:55], v[92:95], v[220:223]\n"
"ds_read_b64_tr_b16 a[46:47], v99 offset:256\n"
"v_mfma_f32_16x16x32_bf16 v[216:219], v[52:55], v[88:91], v[216:219]\n"
"s_add_u32 s75, s86, 30720\n"
"ds_read_b64_tr_b16 a[48:49], v103 offset:0\n"
"buffer_load_dwordx4 v101, s[40:43], s75 offen offset:2048 lds\n"
"v_mfma_f32_16x16x32_bf16 v[212:215], v[52:55], v[84:87], v[212:215]\n"
"ds_read_b64_tr_b16 a[50:51], v103 offset:256\n"
"v_mfma_f32_16x16x32_bf16 v[208:211], v[52:55], v[80:83], v[208:211]\n"
"v_mfma_f32_16x16x32_bf16 v[224:227], v[56:59], v[80:83], v[224:227]\n"
"v_mfma_f32_16x16x32_bf16 v[228:231], v[56:59], v[84:87], v[228:231]\n"
"v_mfma_f32_16x16x32_bf16 v[232:235], v[56:59], v[88:91], v[232:235]\n"
"s_add_u32 s75, s86, 46080\n"
"buffer_load_dwordx4 v102, s[40:43], s75 offen offset:3072 lds\n"
"v_mfma_f32_16x16x32_bf16 v[236:239], v[56:59], v[92:95], v[236:239]\n"
"v_mfma_f32_16x16x32_bf16 v[252:255], v[60:63], v[92:95], v[252:255]\n"
"v_mfma_f32_16x16x32_bf16 v[248:251], v[60:63], v[88:91], v[248:251]\n"
"v_mfma_f32_16x16x32_bf16 v[244:247], v[60:63], v[84:87], v[244:247]\n"
"v_mfma_f32_16x16x32_bf16 v[240:243], v[60:63], v[80:83], v[240:243]\n"
"s_waitcnt lgkmcnt(0)\n"
"v_mfma_f32_16x16x32_bf16 v[128:131], v[0:3], v[64:67], v[128:131]\n"
"ds_read_b128 v[80:83], v97 offset:0\n"
"v_mfma_f32_16x16x32_bf16 v[132:135], v[0:3], v[68:71], v[132:135]\n"
"s_add_u32 m0, s69, 73728\n"
"ds_read_b128 v[84:87], v97 offset:2048\n"
"v_mfma_f32_16x16x32_bf16 v[136:139], v[0:3], v[72:75], v[136:139]\n"
"s_add_u32 s75, s87, 263168\n"
"ds_read_b128 v[88:91], v97 offset:4096\n"
"buffer_load_dwordx4 v104, s[44:47], s75 offen offset:0 lds\n"
"v_mfma_f32_16x16x32_bf16 v[140:143], v[0:3], v[76:79], v[140:143]\n"
"ds_read_b128 v[92:95], v97 offset:6144\n"
"v_mfma_f32_16x16x32_bf16 a[32:35], a[0:3], a[44:47], 0\n"
"ds_read_b64_tr_b16 v[32:33], v112 offset:8192\n"
"v_mfma_f32_16x16x32_bf16 v[156:159], v[4:7], v[76:79], v[156:159]\n"
"ds_read_b64_tr_b16 v[34:35], v112 offset:8448\n"
"v_mfma_f32_16x16x32_bf16 v[152:155], v[4:7], v[72:75], v[152:155]\n"
"ds_read_b64_tr_b16 v[36:37], v113 offset:8192\n"
"v_mfma_f32_16x16x32_bf16 v[148:151], v[4:7], v[68:71], v[148:151]\n"
"s_add_u32 s75, s87, 262272\n"
"ds_read_b64_tr_b16 v[38:39], v113 offset:8448\n"
"buffer_load_dwordx4 v104, s[44:47], s75 offen offset:1024 lds\n"
"v_mfma_f32_16x16x32_bf16 v[144:147], v[4:7], v[64:67], v[144:147]\n"
"ds_read_b64_tr_b16 v[40:41], v112 offset:8704\n"
"v_mfma_f32_16x16x32_bf16 a[32:35], a[4:7], a[48:51], a[32:35]\n"
"ds_read_b64_tr_b16 v[42:43], v112 offset:8960\n"
"v_mfma_f32_16x16x32_bf16 v[160:163], v[8:11], v[64:67], v[160:163]\n"
"ds_read_b64_tr_b16 v[44:45], v113 offset:8704\n"
"v_mfma_f32_16x16x32_bf16 v[164:167], v[8:11], v[68:71], v[164:167]\n"
"s_add_u32 m0, s70, 86016\n"
"ds_read_b64_tr_b16 v[46:47], v113 offset:8960\n"
"v_mfma_f32_16x16x32_bf16 v[168:171], v[8:11], v[72:75], v[168:171]\n"
"s_add_u32 s75, s86, 65536\n"
"ds_read_b64_tr_b16 v[48:49], v112 offset:9216\n"
"buffer_load_dwordx4 v101, s[40:43], s75 offen offset:0 lds\n"
"v_mfma_f32_16x16x32_bf16 v[172:175], v[8:11], v[76:79], v[172:175]\n"
"ds_read_b64_tr_b16 v[50:51], v112 offset:9472\n"
"v_mfma_f32_16x16x32_bf16 v[188:191], v[12:15], v[76:79], v[188:191]\n"
"ds_read_b64_tr_b16 v[52:53], v113 offset:9216\n"
"v_mfma_f32_16x16x32_bf16 v[184:187], v[12:15], v[72:75], v[184:187]\n"
"ds_read_b64_tr_b16 v[54:55], v113 offset:9472\n"
"v_mfma_f32_16x16x32_bf16 v[180:183], v[12:15], v[68:71], v[180:183]\n"
"ds_read_b64_tr_b16 v[56:57], v112 offset:9728\n"
"v_mfma_f32_16x16x32_bf16 v[176:179], v[12:15], v[64:67], v[176:179]\n"
"s_add_u32 s75, s86, 80896\n"
"ds_read_b64_tr_b16 v[58:59], v112 offset:9984\n"
"buffer_load_dwordx4 v102, s[40:43], s75 offen offset:1024 lds\n"
"v_mfma_f32_16x16x32_bf16 v[192:195], v[16:19], v[64:67], v[192:195]\n"
"ds_read_b64_tr_b16 v[60:61], v113 offset:9728\n"
"v_mfma_f32_16x16x32_bf16 v[196:199], v[16:19], v[68:71], v[196:199]\n"
"ds_read_b64_tr_b16 v[62:63], v113 offset:9984\n"
"v_mfma_f32_16x16x32_bf16 v[200:203], v[16:19], v[72:75], v[200:203]\n"
"ds_read_b64_tr_b16 a[52:53], v99 offset:8192\n"
"v_mfma_f32_16x16x32_bf16 v[204:207], v[16:19], v[76:79], v[204:207]\n"
"ds_read_b64_tr_b16 a[54:55], v99 offset:8448\n"
"v_mfma_f32_16x16x32_bf16 v[220:223], v[20:23], v[76:79], v[220:223]\n"
"s_add_u32 s75, s86, 96256\n"
"ds_read_b64_tr_b16 a[56:57], v103 offset:8192\n"
"buffer_load_dwordx4 v101, s[40:43], s75 offen offset:2048 lds\n"
"v_mfma_f32_16x16x32_bf16 v[216:219], v[20:23], v[72:75], v[216:219]\n"
"ds_read_b64_tr_b16 a[58:59], v103 offset:8448\n"
"v_mfma_f32_16x16x32_bf16 v[212:215], v[20:23], v[68:71], v[212:215]\n"
"v_mfma_f32_16x16x32_bf16 v[208:211], v[20:23], v[64:67], v[208:211]\n"
"v_mfma_f32_16x16x32_bf16 v[224:227], v[24:27], v[64:67], v[224:227]\n"
"v_mfma_f32_16x16x32_bf16 v[228:231], v[24:27], v[68:71], v[228:231]\n"
"s_add_u32 s75, s86, 111616\n"
"buffer_load_dwordx4 v102, s[40:43], s75 offen offset:3072 lds\n"
"v_mfma_f32_16x16x32_bf16 v[232:235], v[24:27], v[72:75], v[232:235]\n"
"s_bfe_u32 s85, s78, 0x40010\n"
"v_mfma_f32_16x16x32_bf16 v[236:239], v[24:27], v[76:79], v[236:239]\n"
"s_lshl_b32 s86, s85, 7\n"
"v_mfma_f32_16x16x32_bf16 v[252:255], v[28:31], v[76:79], v[252:255]\n"
"s_lshl_b32 s87, s85, 19\n"
"v_mfma_f32_16x16x32_bf16 v[248:251], v[28:31], v[72:75], v[248:251]\n"
"v_mfma_f32_16x16x32_bf16 v[244:247], v[28:31], v[68:71], v[244:247]\n"
"v_mfma_f32_16x16x32_bf16 v[240:243], v[28:31], v[64:67], v[240:243]\n"
"s_waitcnt vmcnt(12) lgkmcnt(0)\n"
"s_barrier\n"
"v_mfma_f32_16x16x32_bf16 v[128:131], v[32:35], v[80:83], v[128:131]\n"
"v_mfma_f32_16x16x32_bf16 v[132:135], v[32:35], v[84:87], v[132:135]\n"
"ds_read_b128 v[64:67], v98 offset:32768\n"
"v_mfma_f32_16x16x32_bf16 v[136:139], v[32:35], v[88:91], v[136:139]\n"
"s_add_u32 m0, s69, 0\n"
"ds_read_b128 v[68:71], v98 offset:34816\n"
"v_mfma_f32_16x16x32_bf16 v[140:143], v[32:35], v[92:95], v[140:143]\n"
"s_add_u32 s75, s87, 1024\n"
"ds_read_b128 v[72:75], v98 offset:36864\n"
"buffer_load_dwordx4 v104, s[44:47], s75 offen offset:0 lds\n"
"v_mfma_f32_16x16x32_bf16 a[32:35], a[0:3], a[52:55], a[32:35]\n"
"ds_read_b128 v[76:79], v98 offset:38912\n"
"v_mfma_f32_16x16x32_bf16 v[156:159], v[36:39], v[92:95], v[156:159]\n"
"ds_read_b64_tr_b16 v[0:1], v112 offset:49152\n"
"v_mfma_f32_16x16x32_bf16 v[152:155], v[36:39], v[88:91], v[152:155]\n"
"ds_read_b64_tr_b16 v[2:3], v112 offset:49408\n"
"v_mfma_f32_16x16x32_bf16 v[148:151], v[36:39], v[84:87], v[148:151]\n"
"ds_read_b64_tr_b16 v[4:5], v113 offset:49152\n"
"v_mfma_f32_16x16x32_bf16 v[144:147], v[36:39], v[80:83], v[144:147]\n"
"s_add_u32 s75, s87, 128\n"
"ds_read_b64_tr_b16 v[6:7], v113 offset:49408\n"
"buffer_load_dwordx4 v104, s[44:47], s75 offen offset:1024 lds\n"
"v_mfma_f32_16x16x32_bf16 a[32:35], a[4:7], a[56:59], a[32:35]\n"
"ds_read_b64_tr_b16 v[8:9], v112 offset:49664\n"
"v_mfma_f32_16x16x32_bf16 v[160:163], v[40:43], v[80:83], v[160:163]\n"
"ds_read_b64_tr_b16 v[10:11], v112 offset:49920\n"
"v_mfma_f32_16x16x32_bf16 v[164:167], v[40:43], v[84:87], v[164:167]\n"
"ds_read_b64_tr_b16 v[12:13], v113 offset:49664\n"
"v_mfma_f32_16x16x32_bf16 v[168:171], v[40:43], v[88:91], v[168:171]\n"
"s_add_u32 m0, s70, 16384\n"
"ds_read_b64_tr_b16 v[14:15], v113 offset:49920\n"
"v_mfma_f32_16x16x32_bf16 v[172:175], v[40:43], v[92:95], v[172:175]\n"
"s_add_u32 s75, s86, 0\n"
"ds_read_b64_tr_b16 v[16:17], v112 offset:50176\n"
"buffer_load_dwordx4 v101, s[40:43], s75 offen offset:0 lds\n"
"v_mfma_f32_16x16x32_bf16 v[188:191], v[44:47], v[92:95], v[188:191]\n"
"ds_read_b64_tr_b16 v[18:19], v112 offset:50432\n"
"v_mfma_f32_16x16x32_bf16 v[184:187], v[44:47], v[88:91], v[184:187]\n"
"ds_read_b64_tr_b16 v[20:21], v113 offset:50176\n"
"v_mfma_f32_16x16x32_bf16 v[180:183], v[44:47], v[84:87], v[180:183]\n"
"ds_read_b64_tr_b16 v[22:23], v113 offset:50432\n"
"v_mfma_f32_16x16x32_bf16 v[176:179], v[44:47], v[80:83], v[176:179]\n"
"ds_read_b64_tr_b16 v[24:25], v112 offset:50688\n"
"v_mfma_f32_16x16x32_bf16 v[192:195], v[48:51], v[80:83], v[192:195]\n"
"s_add_u32 s75, s86, 15360\n"
"ds_read_b64_tr_b16 v[26:27], v112 offset:50944\n"
"buffer_load_dwordx4 v102, s[40:43], s75 offen offset:1024 lds\n"
"v_mfma_f32_16x16x32_bf16 v[196:199], v[48:51], v[84:87], v[196:199]\n"
"ds_read_b64_tr_b16 v[28:29], v113 offset:50688\n"
"v_mfma_f32_16x16x32_bf16 v[200:203], v[48:51], v[88:91], v[200:203]\n"
"ds_read_b64_tr_b16 v[30:31], v113 offset:50944\n"
"v_mfma_f32_16x16x32_bf16 v[204:207], v[48:51], v[92:95], v[204:207]\n"
"ds_read_b64_tr_b16 a[44:45], v99 offset:49152\n"
"v_mfma_f32_16x16x32_bf16 v[220:223], v[52:55], v[92:95], v[220:223]\n"
"ds_read_b64_tr_b16 a[46:47], v99 offset:49408\n"
"v_mfma_f32_16x16x32_bf16 v[216:219], v[52:55], v[88:91], v[216:219]\n"
"s_add_u32 s75, s86, 30720\n"
"ds_read_b64_tr_b16 a[48:49], v103 offset:49152\n"
"buffer_load_dwordx4 v101, s[40:43], s75 offen offset:2048 lds\n"
"v_mfma_f32_16x16x32_bf16 v[212:215], v[52:55], v[84:87], v[212:215]\n"
"ds_read_b64_tr_b16 a[50:51], v103 offset:49408\n"
"v_mfma_f32_16x16x32_bf16 v[208:211], v[52:55], v[80:83], v[208:211]\n"
"v_mfma_f32_16x16x32_bf16 v[224:227], v[56:59], v[80:83], v[224:227]\n"
"v_mfma_f32_16x16x32_bf16 v[228:231], v[56:59], v[84:87], v[228:231]\n"
"v_mfma_f32_16x16x32_bf16 v[232:235], v[56:59], v[88:91], v[232:235]\n"
"s_add_u32 s75, s86, 46080\n"
"buffer_load_dwordx4 v102, s[40:43], s75 offen offset:3072 lds\n"
"v_mfma_f32_16x16x32_bf16 v[236:239], v[56:59], v[92:95], v[236:239]\n"
"v_mfma_f32_16x16x32_bf16 v[252:255], v[60:63], v[92:95], v[252:255]\n"
"v_mfma_f32_16x16x32_bf16 v[248:251], v[60:63], v[88:91], v[248:251]\n"
"v_mfma_f32_16x16x32_bf16 v[244:247], v[60:63], v[84:87], v[244:247]\n"
"v_mfma_f32_16x16x32_bf16 v[240:243], v[60:63], v[80:83], v[240:243]\n"
"s_waitcnt lgkmcnt(0)\n"
"v_mfma_f32_16x16x32_bf16 v[128:131], v[0:3], v[64:67], v[128:131]\n"
"ds_read_b128 v[80:83], v100 offset:32768\n"
"v_mfma_f32_16x16x32_bf16 v[132:135], v[0:3], v[68:71], v[132:135]\n"
"s_add_u32 m0, s69, 8192\n"
"ds_read_b128 v[84:87], v100 offset:34816\n"
"v_mfma_f32_16x16x32_bf16 v[136:139], v[0:3], v[72:75], v[136:139]\n"
"s_add_u32 s75, s87, 263168\n"
"ds_read_b128 v[88:91], v100 offset:36864\n"
"buffer_load_dwordx4 v104, s[44:47], s75 offen offset:0 lds\n"
"v_mfma_f32_16x16x32_bf16 v[140:143], v[0:3], v[76:79], v[140:143]\n"
"ds_read_b128 v[92:95], v100 offset:38912\n"
"v_mfma_f32_16x16x32_bf16 a[32:35], a[0:3], a[44:47], a[32:35]\n"
"ds_read_b64_tr_b16 v[32:33], v112 offset:57344\n"
"v_mfma_f32_16x16x32_bf16 v[156:159], v[4:7], v[76:79], v[156:159]\n"
"ds_read_b64_tr_b16 v[34:35], v112 offset:57600\n"
"v_mfma_f32_16x16x32_bf16 v[152:155], v[4:7], v[72:75], v[152:155]\n"
"ds_read_b64_tr_b16 v[36:37], v113 offset:57344\n"
"v_mfma_f32_16x16x32_bf16 v[148:151], v[4:7], v[68:71], v[148:151]\n"
"s_add_u32 s75, s87, 262272\n"
"ds_read_b64_tr_b16 v[38:39], v113 offset:57600\n"
"buffer_load_dwordx4 v104, s[44:47], s75 offen offset:1024 lds\n"
"v_mfma_f32_16x16x32_bf16 v[144:147], v[4:7], v[64:67], v[144:147]\n"
"ds_read_b64_tr_b16 v[40:41], v112 offset:57856\n"
"v_mfma_f32_16x16x32_bf16 a[32:35], a[4:7], a[48:51], a[32:35]\n"
"ds_read_b64_tr_b16 v[42:43], v112 offset:58112\n"
"v_mfma_f32_16x16x32_bf16 v[160:163], v[8:11], v[64:67], v[160:163]\n"
"ds_read_b64_tr_b16 v[44:45], v113 offset:57856\n"
"v_mfma_f32_16x16x32_bf16 v[164:167], v[8:11], v[68:71], v[164:167]\n"
"s_add_u32 m0, s70, 20480\n"
"ds_read_b64_tr_b16 v[46:47], v113 offset:58112\n"
"v_mfma_f32_16x16x32_bf16 v[168:171], v[8:11], v[72:75], v[168:171]\n"
"s_add_u32 s75, s86, 65536\n"
"ds_read_b64_tr_b16 v[48:49], v112 offset:58368\n"
"buffer_load_dwordx4 v101, s[40:43], s75 offen offset:0 lds\n"
"v_mfma_f32_16x16x32_bf16 v[172:175], v[8:11], v[76:79], v[172:175]\n"
"ds_read_b64_tr_b16 v[50:51], v112 offset:58624\n"
"v_mfma_f32_16x16x32_bf16 v[188:191], v[12:15], v[76:79], v[188:191]\n"
"ds_read_b64_tr_b16 v[52:53], v113 offset:58368\n"
"v_mfma_f32_16x16x32_bf16 v[184:187], v[12:15], v[72:75], v[184:187]\n"
"ds_read_b64_tr_b16 v[54:55], v113 offset:58624\n"
"v_mfma_f32_16x16x32_bf16 v[180:183], v[12:15], v[68:71], v[180:183]\n"
"ds_read_b64_tr_b16 v[56:57], v112 offset:58880\n"
"v_mfma_f32_16x16x32_bf16 v[176:179], v[12:15], v[64:67], v[176:179]\n"
"s_add_u32 s75, s86, 80896\n"
"ds_read_b64_tr_b16 v[58:59], v112 offset:59136\n"
"buffer_load_dwordx4 v102, s[40:43], s75 offen offset:1024 lds\n"
"v_mfma_f32_16x16x32_bf16 v[192:195], v[16:19], v[64:67], v[192:195]\n"
"ds_read_b64_tr_b16 v[60:61], v113 offset:58880\n"
"v_mfma_f32_16x16x32_bf16 v[196:199], v[16:19], v[68:71], v[196:199]\n"
"ds_read_b64_tr_b16 v[62:63], v113 offset:59136\n"
"v_mfma_f32_16x16x32_bf16 v[200:203], v[16:19], v[72:75], v[200:203]\n"
"ds_read_b64_tr_b16 a[52:53], v99 offset:57344\n"
"v_mfma_f32_16x16x32_bf16 v[204:207], v[16:19], v[76:79], v[204:207]\n"
"ds_read_b64_tr_b16 a[54:55], v99 offset:57600\n"
"v_mfma_f32_16x16x32_bf16 v[220:223], v[20:23], v[76:79], v[220:223]\n"
"s_add_u32 s75, s86, 96256\n"
"ds_read_b64_tr_b16 a[56:57], v103 offset:57344\n"
"buffer_load_dwordx4 v101, s[40:43], s75 offen offset:2048 lds\n"
"v_mfma_f32_16x16x32_bf16 v[216:219], v[20:23], v[72:75], v[216:219]\n"
"ds_read_b64_tr_b16 a[58:59], v103 offset:57600\n"
"v_mfma_f32_16x16x32_bf16 v[212:215], v[20:23], v[68:71], v[212:215]\n"
"v_mfma_f32_16x16x32_bf16 v[208:211], v[20:23], v[64:67], v[208:211]\n"
"v_mfma_f32_16x16x32_bf16 v[224:227], v[24:27], v[64:67], v[224:227]\n"
"v_mfma_f32_16x16x32_bf16 v[228:231], v[24:27], v[68:71], v[228:231]\n"
"s_add_u32 s75, s86, 111616\n"
"buffer_load_dwordx4 v102, s[40:43], s75 offen offset:3072 lds\n"
"v_mfma_f32_16x16x32_bf16 v[232:235], v[24:27], v[72:75], v[232:235]\n"
"s_bfe_u32 s85, s78, 0x40014\n"
"v_mfma_f32_16x16x32_bf16 v[236:239], v[24:27], v[76:79], v[236:239]\n"
"s_lshl_b32 s86, s85, 7\n"
"v_mfma_f32_16x16x32_bf16 v[252:255], v[28:31], v[76:79], v[252:255]\n"
"s_lshl_b32 s87, s85, 19\n"
"v_mfma_f32_16x16x32_bf16 v[248:251], v[28:31], v[72:75], v[248:251]\n"
"v_mfma_f32_16x16x32_bf16 v[244:247], v[28:31], v[68:71], v[244:247]\n"
"v_mfma_f32_16x16x32_bf16 v[240:243], v[28:31], v[64:67], v[240:243]\n"
"s_waitcnt vmcnt(12) lgkmcnt(0)\n"
"s_barrier\n"
"v_mfma_f32_16x16x32_bf16 v[128:131], v[32:35], v[80:83], v[128:131]\n"
"v_mfma_f32_16x16x32_bf16 v[132:135], v[32:35], v[84:87], v[132:135]\n"
"ds_read_b128 v[64:67], v98 offset:0\n"
"v_mfma_f32_16x16x32_bf16 v[136:139], v[32:35], v[88:91], v[136:139]\n"
"s_add_u32 m0, s69, 49152\n"
"ds_read_b128 v[68:71], v98 offset:2048\n"
"v_mfma_f32_16x16x32_bf16 v[140:143], v[32:35], v[92:95], v[140:143]\n"
"s_add_u32 s75, s87, 1024\n"
"ds_read_b128 v[72:75], v98 offset:4096\n"
"buffer_load_dwordx4 v104, s[44:47], s75 offen offset:0 lds\n"
"v_mfma_f32_16x16x32_bf16 a[32:35], a[0:3], a[52:55], a[32:35]\n"
"ds_read_b128 v[76:79], v98 offset:6144\n"
"v_mfma_f32_16x16x32_bf16 v[156:159], v[36:39], v[92:95], v[156:159]\n"
"ds_read_b64_tr_b16 v[0:1], v105 offset:0\n"
"v_mfma_f32_16x16x32_bf16 v[152:155], v[36:39], v[88:91], v[152:155]\n"
"ds_read_b64_tr_b16 v[2:3], v105 offset:256\n"
"v_mfma_f32_16x16x32_bf16 v[148:151], v[36:39], v[84:87], v[148:151]\n"
"ds_read_b64_tr_b16 v[4:5], v106 offset:0\n"
"v_mfma_f32_16x16x32_bf16 v[144:147], v[36:39], v[80:83], v[144:147]\n"
"s_add_u32 s75, s87, 128\n"
"ds_read_b64_tr_b16 v[6:7], v106 offset:256\n"
"buffer_load_dwordx4 v104, s[44:47], s75 offen offset:1024 lds\n"
"v_mfma_f32_16x16x32_bf16 a[32:35], a[4:7], a[56:59], a[32:35]\n"
"ds_read_b64_tr_b16 v[8:9], v105 offset:512\n"
"v_mfma_f32_16x16x32_bf16 v[160:163], v[40:43], v[80:83], v[160:163]\n"
"ds_read_b64_tr_b16 v[10:11], v105 offset:768\n"
"v_mfma_f32_16x16x32_bf16 v[164:167], v[40:43], v[84:87], v[164:167]\n"
"ds_read_b64_tr_b16 v[12:13], v106 offset:512\n"
"v_mfma_f32_16x16x32_bf16 v[168:171], v[40:43], v[88:91], v[168:171]\n"
"s_add_u32 m0, s70, 114688\n"
"ds_read_b64_tr_b16 v[14:15], v106 offset:768\n"
"v_mfma_f32_16x16x32_bf16 v[172:175], v[40:43], v[92:95], v[172:175]\n"
"s_add_u32 s75, s86, 0\n"
"ds_read_b64_tr_b16 v[16:17], v105 offset:1024\n"
"buffer_load_dwordx4 v101, s[40:43], s75 offen offset:0 lds\n"
"v_mfma_f32_16x16x32_bf16 v[188:191], v[44:47], v[92:95], v[188:191]\n"
"ds_read_b64_tr_b16 v[18:19], v105 offset:1280\n"
"v_mfma_f32_16x16x32_bf16 v[184:187], v[44:47], v[88:91], v[184:187]\n"
"ds_read_b64_tr_b16 v[20:21], v106 offset:1024\n"
"v_mfma_f32_16x16x32_bf16 v[180:183], v[44:47], v[84:87], v[180:183]\n"
"ds_read_b64_tr_b16 v[22:23], v106 offset:1280\n"
"v_mfma_f32_16x16x32_bf16 v[176:179], v[44:47], v[80:83], v[176:179]\n"
"ds_read_b64_tr_b16 v[24:25], v105 offset:1536\n"
"v_mfma_f32_16x16x32_bf16 v[192:195], v[48:51], v[80:83], v[192:195]\n"
"s_add_u32 s75, s86, 15360\n"
"ds_read_b64_tr_b16 v[26:27], v105 offset:1792\n"
"buffer_load_dwordx4 v102, s[40:43], s75 offen offset:1024 lds\n"
"v_mfma_f32_16x16x32_bf16 v[196:199], v[48:51], v[84:87], v[196:199]\n"
"ds_read_b64_tr_b16 v[28:29], v106 offset:1536\n"
"v_mfma_f32_16x16x32_bf16 v[200:203], v[48:51], v[88:91], v[200:203]\n"
"ds_read_b64_tr_b16 v[30:31], v106 offset:1792\n"
"v_mfma_f32_16x16x32_bf16 v[204:207], v[48:51], v[92:95], v[204:207]\n"
"ds_read_b64_tr_b16 a[44:45], v107 offset:0\n"
"v_mfma_f32_16x16x32_bf16 v[220:223], v[52:55], v[92:95], v[220:223]\n"
"ds_read_b64_tr_b16 a[46:47], v107 offset:256\n"
"v_mfma_f32_16x16x32_bf16 v[216:219], v[52:55], v[88:91], v[216:219]\n"
"s_add_u32 s75, s86, 30720\n"
"ds_read_b64_tr_b16 a[48:49], v111 offset:0\n"
"buffer_load_dwordx4 v101, s[40:43], s75 offen offset:2048 lds\n"
"v_mfma_f32_16x16x32_bf16 v[212:215], v[52:55], v[84:87], v[212:215]\n"
"ds_read_b64_tr_b16 a[50:51], v111 offset:256\n"
"v_mfma_f32_16x16x32_bf16 v[208:211], v[52:55], v[80:83], v[208:211]\n"
"v_mfma_f32_16x16x32_bf16 v[224:227], v[56:59], v[80:83], v[224:227]\n"
"v_mfma_f32_16x16x32_bf16 v[228:231], v[56:59], v[84:87], v[228:231]\n"
"v_mfma_f32_16x16x32_bf16 v[232:235], v[56:59], v[88:91], v[232:235]\n"
"s_add_u32 s75, s86, 46080\n"
"buffer_load_dwordx4 v102, s[40:43], s75 offen offset:3072 lds\n"
"v_mfma_f32_16x16x32_bf16 v[236:239], v[56:59], v[92:95], v[236:239]\n"
"v_mfma_f32_16x16x32_bf16 v[252:255], v[60:63], v[92:95], v[252:255]\n"
"v_mfma_f32_16x16x32_bf16 v[248:251], v[60:63], v[88:91], v[248:251]\n"
"v_mfma_f32_16x16x32_bf16 v[244:247], v[60:63], v[84:87], v[244:247]\n"
"v_mfma_f32_16x16x32_bf16 v[240:243], v[60:63], v[80:83], v[240:243]\n"
"s_waitcnt lgkmcnt(0)\n"
"v_mfma_f32_16x16x32_bf16 v[128:131], v[0:3], v[64:67], v[128:131]\n"
"ds_read_b128 v[80:83], v100 offset:0\n"
"v_mfma_f32_16x16x32_bf16 v[132:135], v[0:3], v[68:71], v[132:135]\n"
"s_add_u32 m0, s69, 57344\n"
"ds_read_b128 v[84:87], v100 offset:2048\n"
"v_mfma_f32_16x16x32_bf16 v[136:139], v[0:3], v[72:75], v[136:139]\n"
"s_add_u32 s75, s87, 263168\n"
"ds_read_b128 v[88:91], v100 offset:4096\n"
"buffer_load_dwordx4 v104, s[44:47], s75 offen offset:0 lds\n"
"v_mfma_f32_16x16x32_bf16 v[140:143], v[0:3], v[76:79], v[140:143]\n"
"ds_read_b128 v[92:95], v100 offset:6144\n"
"v_mfma_f32_16x16x32_bf16 a[32:35], a[0:3], a[44:47], a[32:35]\n"
"ds_read_b64_tr_b16 v[32:33], v105 offset:8192\n"
"v_mfma_f32_16x16x32_bf16 v[156:159], v[4:7], v[76:79], v[156:159]\n"
"ds_read_b64_tr_b16 v[34:35], v105 offset:8448\n"
"v_mfma_f32_16x16x32_bf16 v[152:155], v[4:7], v[72:75], v[152:155]\n"
"ds_read_b64_tr_b16 v[36:37], v106 offset:8192\n"
"v_mfma_f32_16x16x32_bf16 v[148:151], v[4:7], v[68:71], v[148:151]\n"
"s_add_u32 s75, s87, 262272\n"
"ds_read_b64_tr_b16 v[38:39], v106 offset:8448\n"
"buffer_load_dwordx4 v104, s[44:47], s75 offen offset:1024 lds\n"
"v_mfma_f32_16x16x32_bf16 v[144:147], v[4:7], v[64:67], v[144:147]\n"
"ds_read_b64_tr_b16 v[40:41], v105 offset:8704\n"
"v_mfma_f32_16x16x32_bf16 a[32:35], a[4:7], a[48:51], a[32:35]\n"
"ds_read_b64_tr_b16 v[42:43], v105 offset:8960\n"
"v_mfma_f32_16x16x32_bf16 v[160:163], v[8:11], v[64:67], v[160:163]\n"
"ds_read_b64_tr_b16 v[44:45], v106 offset:8704\n"
"v_mfma_f32_16x16x32_bf16 v[164:167], v[8:11], v[68:71], v[164:167]\n"
"s_add_u32 m0, s70, 118784\n"
"ds_read_b64_tr_b16 v[46:47], v106 offset:8960\n"
"v_mfma_f32_16x16x32_bf16 v[168:171], v[8:11], v[72:75], v[168:171]\n"
"s_add_u32 s75, s86, 65536\n"
"ds_read_b64_tr_b16 v[48:49], v105 offset:9216\n"
"buffer_load_dwordx4 v101, s[40:43], s75 offen offset:0 lds\n"
"v_mfma_f32_16x16x32_bf16 v[172:175], v[8:11], v[76:79], v[172:175]\n"
"ds_read_b64_tr_b16 v[50:51], v105 offset:9472\n"
"v_mfma_f32_16x16x32_bf16 v[188:191], v[12:15], v[76:79], v[188:191]\n"
"ds_read_b64_tr_b16 v[52:53], v106 offset:9216\n"
"v_mfma_f32_16x16x32_bf16 v[184:187], v[12:15], v[72:75], v[184:187]\n"
"ds_read_b64_tr_b16 v[54:55], v106 offset:9472\n"
"v_mfma_f32_16x16x32_bf16 v[180:183], v[12:15], v[68:71], v[180:183]\n"
"ds_read_b64_tr_b16 v[56:57], v105 offset:9728\n"
"v_mfma_f32_16x16x32_bf16 v[176:179], v[12:15], v[64:67], v[176:179]\n"
"s_add_u32 s75, s86, 80896\n"
"ds_read_b64_tr_b16 v[58:59], v105 offset:9984\n"
"buffer_load_dwordx4 v102, s[40:43], s75 offen offset:1024 lds\n"
"v_mfma_f32_16x16x32_bf16 v[192:195], v[16:19], v[64:67], v[192:195]\n"
"ds_read_b64_tr_b16 v[60:61], v106 offset:9728\n"
"v_mfma_f32_16x16x32_bf16 v[196:199], v[16:19], v[68:71], v[196:199]\n"
"ds_read_b64_tr_b16 v[62:63], v106 offset:9984\n"
"v_mfma_f32_16x16x32_bf16 v[200:203], v[16:19], v[72:75], v[200:203]\n"
"ds_read_b64_tr_b16 a[52:53], v107 offset:8192\n"
"v_mfma_f32_16x16x32_bf16 v[204:207], v[16:19], v[76:79], v[204:207]\n"
"ds_read_b64_tr_b16 a[54:55], v107 offset:8448\n"
"v_mfma_f32_16x16x32_bf16 v[220:223], v[20:23], v[76:79], v[220:223]\n"
"s_add_u32 s75, s86, 96256\n"
"ds_read_b64_tr_b16 a[56:57], v111 offset:8192\n"
"buffer_load_dwordx4 v101, s[40:43], s75 offen offset:2048 lds\n"
"v_mfma_f32_16x16x32_bf16 v[216:219], v[20:23], v[72:75], v[216:219]\n"
"ds_read_b64_tr_b16 a[58:59], v111 offset:8448\n"
"v_mfma_f32_16x16x32_bf16 v[212:215], v[20:23], v[68:71], v[212:215]\n"
"v_mfma_f32_16x16x32_bf16 v[208:211], v[20:23], v[64:67], v[208:211]\n"
"v_mfma_f32_16x16x32_bf16 v[224:227], v[24:27], v[64:67], v[224:227]\n"
"v_mfma_f32_16x16x32_bf16 v[228:231], v[24:27], v[68:71], v[228:231]\n"
"s_add_u32 s75, s86, 111616\n"
"buffer_load_dwordx4 v102, s[40:43], s75 offen offset:3072 lds\n"
"v_mfma_f32_16x16x32_bf16 v[232:235], v[24:27], v[72:75], v[232:235]\n"
"s_bfe_u32 s85, s78, 0x40018\n"
"v_mfma_f32_16x16x32_bf16 v[236:239], v[24:27], v[76:79], v[236:239]\n"
"s_lshl_b32 s86, s85, 7\n"
"v_mfma_f32_16x16x32_bf16 v[252:255], v[28:31], v[76:79], v[252:255]\n"
"s_lshl_b32 s87, s85, 19\n"
"v_mfma_f32_16x16x32_bf16 v[248:251], v[28:31], v[72:75], v[248:251]\n"
"v_mfma_f32_16x16x32_bf16 v[244:247], v[28:31], v[68:71], v[244:247]\n"
"v_mfma_f32_16x16x32_bf16 v[240:243], v[28:31], v[64:67], v[240:243]\n"
"s_waitcnt vmcnt(12) lgkmcnt(0)\n"
"s_barrier\n"
"v_mfma_f32_16x16x32_bf16 v[128:131], v[32:35], v[80:83], v[128:131]\n"
"v_mfma_f32_16x16x32_bf16 v[132:135], v[32:35], v[84:87], v[132:135]\n"
"ds_read_b128 v[64:67], v96 offset:0\n"
"v_mfma_f32_16x16x32_bf16 v[136:139], v[32:35], v[88:91], v[136:139]\n"
"s_add_u32 m0, s69, 65536\n"
"ds_read_b128 v[68:71], v96 offset:2048\n"
"v_mfma_f32_16x16x32_bf16 v[140:143], v[32:35], v[92:95], v[140:143]\n"
"s_add_u32 s75, s87, 1024\n"
"ds_read_b128 v[72:75], v96 offset:4096\n"
"buffer_load_dwordx4 v104, s[44:47], s75 offen offset:0 lds\n"
"v_mfma_f32_16x16x32_bf16 a[32:35], a[0:3], a[52:55], a[32:35]\n"
"ds_read_b128 v[76:79], v96 offset:6144\n"
"v_mfma_f32_16x16x32_bf16 v[156:159], v[36:39], v[92:95], v[156:159]\n"
"ds_read_b64_tr_b16 v[0:1], v112 offset:0\n"
"v_mfma_f32_16x16x32_bf16 v[152:155], v[36:39], v[88:91], v[152:155]\n"
"ds_read_b64_tr_b16 v[2:3], v112 offset:256\n"
"v_mfma_f32_16x16x32_bf16 v[148:151], v[36:39], v[84:87], v[148:151]\n"
"ds_read_b64_tr_b16 v[4:5], v113 offset:0\n"
"v_mfma_f32_16x16x32_bf16 v[144:147], v[36:39], v[80:83], v[144:147]\n"
"s_add_u32 s75, s87, 128\n"
"ds_read_b64_tr_b16 v[6:7], v113 offset:256\n"
"buffer_load_dwordx4 v104, s[44:47], s75 offen offset:1024 lds\n"
"v_mfma_f32_16x16x32_bf16 a[32:35], a[4:7], a[56:59], a[32:35]\n"
"ds_read_b64_tr_b16 v[8:9], v112 offset:512\n"
"v_mfma_f32_16x16x32_bf16 v[160:163], v[40:43], v[80:83], v[160:163]\n"
"ds_read_b64_tr_b16 v[10:11], v112 offset:768\n"
"v_mfma_f32_16x16x32_bf16 v[164:167], v[40:43], v[84:87], v[164:167]\n"
"ds_read_b64_tr_b16 v[12:13], v113 offset:512\n"
"v_mfma_f32_16x16x32_bf16 v[168:171], v[40:43], v[88:91], v[168:171]\n"
"s_add_u32 m0, s70, 81920\n"
"ds_read_b64_tr_b16 v[14:15], v113 offset:768\n"
"v_mfma_f32_16x16x32_bf16 v[172:175], v[40:43], v[92:95], v[172:175]\n"
"s_add_u32 s75, s86, 0\n"
"ds_read_b64_tr_b16 v[16:17], v112 offset:1024\n"
"buffer_load_dwordx4 v101, s[40:43], s75 offen offset:0 lds\n"
"v_mfma_f32_16x16x32_bf16 v[188:191], v[44:47], v[92:95], v[188:191]\n"
"ds_read_b64_tr_b16 v[18:19], v112 offset:1280\n"
"v_mfma_f32_16x16x32_bf16 v[184:187], v[44:47], v[88:91], v[184:187]\n"
"ds_read_b64_tr_b16 v[20:21], v113 offset:1024\n"
"v_mfma_f32_16x16x32_bf16 v[180:183], v[44:47], v[84:87], v[180:183]\n"
"ds_read_b64_tr_b16 v[22:23], v113 offset:1280\n"
"v_mfma_f32_16x16x32_bf16 v[176:179], v[44:47], v[80:83], v[176:179]\n"
"ds_read_b64_tr_b16 v[24:25], v112 offset:1536\n"
"v_mfma_f32_16x16x32_bf16 v[192:195], v[48:51], v[80:83], v[192:195]\n"
"s_add_u32 s75, s86, 15360\n"
"ds_read_b64_tr_b16 v[26:27], v112 offset:1792\n"
"buffer_load_dwordx4 v102, s[40:43], s75 offen offset:1024 lds\n"
"v_mfma_f32_16x16x32_bf16 v[196:199], v[48:51], v[84:87], v[196:199]\n"
"ds_read_b64_tr_b16 v[28:29], v113 offset:1536\n"
"v_mfma_f32_16x16x32_bf16 v[200:203], v[48:51], v[88:91], v[200:203]\n"
"ds_read_b64_tr_b16 v[30:31], v113 offset:1792\n"
"v_mfma_f32_16x16x32_bf16 v[204:207], v[48:51], v[92:95], v[204:207]\n"
"ds_read_b64_tr_b16 a[44:45], v99 offset:0\n"
"v_mfma_f32_16x16x32_bf16 v[220:223], v[52:55], v[92:95], v[220:223]\n"
"ds_read_b64_tr_b16 a[46:47], v99 offset:256\n"
"v_mfma_f32_16x16x32_bf16 v[216:219], v[52:55], v[88:91], v[216:219]\n"
"s_add_u32 s75, s86, 30720\n"
"ds_read_b64_tr_b16 a[48:49], v103 offset:0\n"
"buffer_load_dwordx4 v101, s[40:43], s75 offen offset:2048 lds\n"
"v_mfma_f32_16x16x32_bf16 v[212:215], v[52:55], v[84:87], v[212:215]\n"
"ds_read_b64_tr_b16 a[50:51], v103 offset:256\n"
"v_mfma_f32_16x16x32_bf16 v[208:211], v[52:55], v[80:83], v[208:211]\n"
"v_mfma_f32_16x16x32_bf16 v[224:227], v[56:59], v[80:83], v[224:227]\n"
"v_mfma_f32_16x16x32_bf16 v[228:231], v[56:59], v[84:87], v[228:231]\n"
"v_mfma_f32_16x16x32_bf16 v[232:235], v[56:59], v[88:91], v[232:235]\n"
"s_add_u32 s75, s86, 46080\n"
"buffer_load_dwordx4 v102, s[40:43], s75 offen offset:3072 lds\n"
"v_mfma_f32_16x16x32_bf16 v[236:239], v[56:59], v[92:95], v[236:239]\n"
"v_mfma_f32_16x16x32_bf16 v[252:255], v[60:63], v[92:95], v[252:255]\n"
"v_mfma_f32_16x16x32_bf16 v[248:251], v[60:63], v[88:91], v[248:251]\n"
"v_mfma_f32_16x16x32_bf16 v[244:247], v[60:63], v[84:87], v[244:247]\n"
"v_mfma_f32_16x16x32_bf16 v[240:243], v[60:63], v[80:83], v[240:243]\n"
"s_waitcnt lgkmcnt(0)\n"
"v_mfma_f32_16x16x32_bf16 v[128:131], v[0:3], v[64:67], v[128:131]\n"
"ds_read_b128 v[80:83], v97 offset:0\n"
"v_mfma_f32_16x16x32_bf16 v[132:135], v[0:3], v[68:71], v[132:135]\n"
"s_add_u32 m0, s69, 73728\n"
"ds_read_b128 v[84:87], v97 offset:2048\n"
"v_mfma_f32_16x16x32_bf16 v[136:139], v[0:3], v[72:75], v[136:139]\n"
"s_add_u32 s75, s87, 263168\n"
"ds_read_b128 v[88:91], v97 offset:4096\n"
"buffer_load_dwordx4 v104, s[44:47], s75 offen offset:0 lds\n"
"v_mfma_f32_16x16x32_bf16 v[140:143], v[0:3], v[76:79], v[140:143]\n"
"ds_read_b128 v[92:95], v97 offset:6144\n"
"v_mfma_f32_16x16x32_bf16 a[40:43], a[0:3], a[44:47], 0\n"
"ds_read_b64_tr_b16 v[32:33], v112 offset:8192\n"
"v_mfma_f32_16x16x32_bf16 v[156:159], v[4:7], v[76:79], v[156:159]\n"
"ds_read_b64_tr_b16 v[34:35], v112 offset:8448\n"
"v_mfma_f32_16x16x32_bf16 v[152:155], v[4:7], v[72:75], v[152:155]\n"
"ds_read_b64_tr_b16 v[36:37], v113 offset:8192\n"
"v_mfma_f32_16x16x32_bf16 v[148:151], v[4:7], v[68:71], v[148:151]\n"
"s_add_u32 s75, s87, 262272\n"
"ds_read_b64_tr_b16 v[38:39], v113 offset:8448\n"
"buffer_load_dwordx4 v104, s[44:47], s75 offen offset:1024 lds\n"
"v_mfma_f32_16x16x32_bf16 v[144:147], v[4:7], v[64:67], v[144:147]\n"
"ds_read_b64_tr_b16 v[40:41], v112 offset:8704\n"
"v_mfma_f32_16x16x32_bf16 a[40:43], a[4:7], a[48:51], a[40:43]\n"
"ds_read_b64_tr_b16 v[42:43], v112 offset:8960\n"
"v_mfma_f32_16x16x32_bf16 v[160:163], v[8:11], v[64:67], v[160:163]\n"
"ds_read_b64_tr_b16 v[44:45], v113 offset:8704\n"
"v_mfma_f32_16x16x32_bf16 v[164:167], v[8:11], v[68:71], v[164:167]\n"
"s_add_u32 m0, s70, 86016\n"
"ds_read_b64_tr_b16 v[46:47], v113 offset:8960\n"
"v_mfma_f32_16x16x32_bf16 v[168:171], v[8:11], v[72:75], v[168:171]\n"
"s_add_u32 s75, s86, 65536\n"
"ds_read_b64_tr_b16 v[48:49], v112 offset:9216\n"
"buffer_load_dwordx4 v101, s[40:43], s75 offen offset:0 lds\n"
"v_mfma_f32_16x16x32_bf16 v[172:175], v[8:11], v[76:79], v[172:175]\n"
"ds_read_b64_tr_b16 v[50:51], v112 offset:9472\n"
"v_mfma_f32_16x16x32_bf16 v[188:191], v[12:15], v[76:79], v[188:191]\n"
"ds_read_b64_tr_b16 v[52:53], v113 offset:9216\n"
"v_mfma_f32_16x16x32_bf16 v[184:187], v[12:15], v[72:75], v[184:187]\n"
"ds_read_b64_tr_b16 v[54:55], v113 offset:9472\n"
"v_mfma_f32_16x16x32_bf16 v[180:183], v[12:15], v[68:71], v[180:183]\n"
"ds_read_b64_tr_b16 v[56:57], v112 offset:9728\n"
"v_mfma_f32_16x16x32_bf16 v[176:179], v[12:15], v[64:67], v[176:179]\n"
"s_add_u32 s75, s86, 80896\n"
"ds_read_b64_tr_b16 v[58:59], v112 offset:9984\n"
"buffer_load_dwordx4 v102, s[40:43], s75 offen offset:1024 lds\n"
"v_mfma_f32_16x16x32_bf16 v[192:195], v[16:19], v[64:67], v[192:195]\n"
"ds_read_b64_tr_b16 v[60:61], v113 offset:9728\n"
"v_mfma_f32_16x16x32_bf16 v[196:199], v[16:19], v[68:71], v[196:199]\n"
"ds_read_b64_tr_b16 v[62:63], v113 offset:9984\n"
"v_mfma_f32_16x16x32_bf16 v[200:203], v[16:19], v[72:75], v[200:203]\n"
"ds_read_b64_tr_b16 a[52:53], v99 offset:8192\n"
"v_mfma_f32_16x16x32_bf16 v[204:207], v[16:19], v[76:79], v[204:207]\n"
"ds_read_b64_tr_b16 a[54:55], v99 offset:8448\n"
"v_mfma_f32_16x16x32_bf16 v[220:223], v[20:23], v[76:79], v[220:223]\n"
"s_add_u32 s75, s86, 96256\n"
"ds_read_b64_tr_b16 a[56:57], v103 offset:8192\n"
"buffer_load_dwordx4 v101, s[40:43], s75 offen offset:2048 lds\n"
"v_mfma_f32_16x16x32_bf16 v[216:219], v[20:23], v[72:75], v[216:219]\n"
"ds_read_b64_tr_b16 a[58:59], v103 offset:8448\n"
"v_mfma_f32_16x16x32_bf16 v[212:215], v[20:23], v[68:71], v[212:215]\n"
"v_mfma_f32_16x16x32_bf16 v[208:211], v[20:23], v[64:67], v[208:211]\n"
"v_mfma_f32_16x16x32_bf16 v[224:227], v[24:27], v[64:67], v[224:227]\n"
"v_mfma_f32_16x16x32_bf16 v[228:231], v[24:27], v[68:71], v[228:231]\n"
"s_add_u32 s75, s86, 111616\n"
"buffer_load_dwordx4 v102, s[40:43], s75 offen offset:3072 lds\n"
"v_mfma_f32_16x16x32_bf16 v[232:235], v[24:27], v[72:75], v[232:235]\n"
"s_bfe_u32 s85, s78, 0x4001c\n"
"v_mfma_f32_16x16x32_bf16 v[236:239], v[24:27], v[76:79], v[236:239]\n"
"s_lshl_b32 s86, s85, 7\n"
"v_mfma_f32_16x16x32_bf16 v[252:255], v[28:31], v[76:79], v[252:255]\n"
"s_lshl_b32 s87, s85, 19\n"
"v_mfma_f32_16x16x32_bf16 v[248:251], v[28:31], v[72:75], v[248:251]\n"
"v_mfma_f32_16x16x32_bf16 v[244:247], v[28:31], v[68:71], v[244:247]\n"
"v_mfma_f32_16x16x32_bf16 v[240:243], v[28:31], v[64:67], v[240:243]\n"
"s_waitcnt vmcnt(12) lgkmcnt(0)\n"
"s_barrier\n"
"v_mfma_f32_16x16x32_bf16 v[128:131], v[32:35], v[80:83], v[128:131]\n"
"v_mfma_f32_16x16x32_bf16 v[132:135], v[32:35], v[84:87], v[132:135]\n"
"ds_read_b128 v[64:67], v98 offset:32768\n"
"v_mfma_f32_16x16x32_bf16 v[136:139], v[32:35], v[88:91], v[136:139]\n"
"s_add_u32 m0, s69, 0\n"
"ds_read_b128 v[68:71], v98 offset:34816\n"
"v_mfma_f32_16x16x32_bf16 v[140:143], v[32:35], v[92:95], v[140:143]\n"
"s_add_u32 s75, s87, 1024\n"
"ds_read_b128 v[72:75], v98 offset:36864\n"
"buffer_load_dwordx4 v104, s[44:47], s75 offen offset:0 lds\n"
"v_mfma_f32_16x16x32_bf16 a[40:43], a[0:3], a[52:55], a[40:43]\n"
"ds_read_b128 v[76:79], v98 offset:38912\n"
"v_mfma_f32_16x16x32_bf16 v[156:159], v[36:39], v[92:95], v[156:159]\n"
"ds_read_b64_tr_b16 v[0:1], v112 offset:49152\n"
"v_mfma_f32_16x16x32_bf16 v[152:155], v[36:39], v[88:91], v[152:155]\n"
"ds_read_b64_tr_b16 v[2:3], v112 offset:49408\n"
"v_mfma_f32_16x16x32_bf16 v[148:151], v[36:39], v[84:87], v[148:151]\n"
"ds_read_b64_tr_b16 v[4:5], v113 offset:49152\n"
"v_mfma_f32_16x16x32_bf16 v[144:147], v[36:39], v[80:83], v[144:147]\n"
"s_add_u32 s75, s87, 128\n"
"ds_read_b64_tr_b16 v[6:7], v113 offset:49408\n"
"buffer_load_dwordx4 v104, s[44:47], s75 offen offset:1024 lds\n"
"v_mfma_f32_16x16x32_bf16 a[40:43], a[4:7], a[56:59], a[40:43]\n"
"ds_read_b64_tr_b16 v[8:9], v112 offset:49664\n"
"v_mfma_f32_16x16x32_bf16 v[160:163], v[40:43], v[80:83], v[160:163]\n"
"ds_read_b64_tr_b16 v[10:11], v112 offset:49920\n"
"v_mfma_f32_16x16x32_bf16 v[164:167], v[40:43], v[84:87], v[164:167]\n"
"ds_read_b64_tr_b16 v[12:13], v113 offset:49664\n"
"v_mfma_f32_16x16x32_bf16 v[168:171], v[40:43], v[88:91], v[168:171]\n"
"s_add_u32 m0, s70, 16384\n"
"ds_read_b64_tr_b16 v[14:15], v113 offset:49920\n"
"v_mfma_f32_16x16x32_bf16 v[172:175], v[40:43], v[92:95], v[172:175]\n"
"s_add_u32 s75, s86, 0\n"
"ds_read_b64_tr_b16 v[16:17], v112 offset:50176\n"
"buffer_load_dwordx4 v101, s[40:43], s75 offen offset:0 lds\n"
"v_mfma_f32_16x16x32_bf16 v[188:191], v[44:47], v[92:95], v[188:191]\n"
"ds_read_b64_tr_b16 v[18:19], v112 offset:50432\n"
"v_mfma_f32_16x16x32_bf16 v[184:187], v[44:47], v[88:91], v[184:187]\n"
"ds_read_b64_tr_b16 v[20:21], v113 offset:50176\n"
"v_mfma_f32_16x16x32_bf16 v[180:183], v[44:47], v[84:87], v[180:183]\n"
"ds_read_b64_tr_b16 v[22:23], v113 offset:50432\n"
"v_mfma_f32_16x16x32_bf16 v[176:179], v[44:47], v[80:83], v[176:179]\n"
"ds_read_b64_tr_b16 v[24:25], v112 offset:50688\n"
"v_mfma_f32_16x16x32_bf16 v[192:195], v[48:51], v[80:83], v[192:195]\n"
"s_add_u32 s75, s86, 15360\n"
"ds_read_b64_tr_b16 v[26:27], v112 offset:50944\n"
"buffer_load_dwordx4 v102, s[40:43], s75 offen offset:1024 lds\n"
"v_mfma_f32_16x16x32_bf16 v[196:199], v[48:51], v[84:87], v[196:199]\n"
"ds_read_b64_tr_b16 v[28:29], v113 offset:50688\n"
"v_mfma_f32_16x16x32_bf16 v[200:203], v[48:51], v[88:91], v[200:203]\n"
"ds_read_b64_tr_b16 v[30:31], v113 offset:50944\n"
"v_mfma_f32_16x16x32_bf16 v[204:207], v[48:51], v[92:95], v[204:207]\n"
"v_mfma_f32_16x16x32_bf16 v[220:223], v[52:55], v[92:95], v[220:223]\n"
"v_mfma_f32_16x16x32_bf16 v[216:219], v[52:55], v[88:91], v[216:219]\n"
"s_add_u32 s75, s86, 30720\n"
"buffer_load_dwordx4 v101, s[40:43], s75 offen offset:2048 lds\n"
"v_mfma_f32_16x16x32_bf16 v[212:215], v[52:55], v[84:87], v[212:215]\n"
"v_mfma_f32_16x16x32_bf16 v[208:211], v[52:55], v[80:83], v[208:211]\n"
"v_mfma_f32_16x16x32_bf16 v[224:227], v[56:59], v[80:83], v[224:227]\n"
"v_mfma_f32_16x16x32_bf16 v[228:231], v[56:59], v[84:87], v[228:231]\n"
"v_mfma_f32_16x16x32_bf16 v[232:235], v[56:59], v[88:91], v[232:235]\n"
"s_add_u32 s75, s86, 46080\n"
"buffer_load_dwordx4 v102, s[40:43], s75 offen offset:3072 lds\n"
"v_mfma_f32_16x16x32_bf16 v[236:239], v[56:59], v[92:95], v[236:239]\n"
"v_mfma_f32_16x16x32_bf16 v[252:255], v[60:63], v[92:95], v[252:255]\n"
"v_mfma_f32_16x16x32_bf16 v[248:251], v[60:63], v[88:91], v[248:251]\n"
"v_mfma_f32_16x16x32_bf16 v[244:247], v[60:63], v[84:87], v[244:247]\n"
"v_mfma_f32_16x16x32_bf16 v[240:243], v[60:63], v[80:83], v[240:243]\n"
"s_waitcnt lgkmcnt(0)\n"
"v_mfma_f32_16x16x32_bf16 v[128:131], v[0:3], v[64:67], v[128:131]\n"
"ds_read_b128 v[80:83], v100 offset:32768\n"
"v_mfma_f32_16x16x32_bf16 v[132:135], v[0:3], v[68:71], v[132:135]\n"
"s_add_u32 m0, s69, 8192\n"
"ds_read_b128 v[84:87], v100 offset:34816\n"
"v_mfma_f32_16x16x32_bf16 v[136:139], v[0:3], v[72:75], v[136:139]\n"
"s_add_u32 s75, s87, 263168\n"
"ds_read_b128 v[88:91], v100 offset:36864\n"
"buffer_load_dwordx4 v104, s[44:47], s75 offen offset:0 lds\n"
"v_mfma_f32_16x16x32_bf16 v[140:143], v[0:3], v[76:79], v[140:143]\n"
"ds_read_b128 v[92:95], v100 offset:38912\n"
"v_mfma_f32_16x16x32_bf16 v[156:159], v[4:7], v[76:79], v[156:159]\n"
"ds_read_b64_tr_b16 v[32:33], v112 offset:57344\n"
"v_mfma_f32_16x16x32_bf16 v[152:155], v[4:7], v[72:75], v[152:155]\n"
"ds_read_b64_tr_b16 v[34:35], v112 offset:57600\n"
"v_mfma_f32_16x16x32_bf16 v[148:151], v[4:7], v[68:71], v[148:151]\n"
"ds_read_b64_tr_b16 v[36:37], v113 offset:57344\n"
"v_and_b32 v99, 240, v124\n"
"v_mfma_f32_16x16x32_bf16 v[144:147], v[4:7], v[64:67], v[144:147]\n"
"s_add_u32 s75, s87, 262272\n"
"ds_read_b64_tr_b16 v[38:39], v113 offset:57600\n"
"buffer_load_dwordx4 v104, s[44:47], s75 offen offset:1024 lds\n"
"v_mfma_f32_16x16x32_bf16 v[160:163], v[8:11], v[64:67], v[160:163]\n"
"ds_read_b64_tr_b16 v[40:41], v112 offset:57856\n"
"v_lshrrev_b32 v103, 4, v108\n"
"v_mfma_f32_16x16x32_bf16 v[164:167], v[8:11], v[68:71], v[164:167]\n"
"ds_read_b64_tr_b16 v[42:43], v112 offset:58112\n"
"v_lshl_add_u32 v99, v103, 24, v99\n"
"v_mfma_f32_16x16x32_bf16 v[168:171], v[8:11], v[72:75], v[168:171]\n"
"s_lshl_b32 s74, s68, 9\n"
"ds_read_b64_tr_b16 v[44:45], v113 offset:57856\n"
"v_add_u32 v99, s74, v99\n"
"v_mfma_f32_16x16x32_bf16 v[172:175], v[8:11], v[76:79], v[172:175]\n"
"s_add_u32 m0, s70, 20480\n"
"s_lshl_b32 s90, s91, 12\n"
"ds_read_b64_tr_b16 v[46:47], v113 offset:58112\n"
"v_mfma_f32_16x16x32_bf16 v[188:191], v[12:15], v[76:79], v[188:191]\n"
"s_add_u32 s75, s86, 65536\n"
"ds_read_b64_tr_b16 v[48:49], v112 offset:58368\n"
"buffer_load_dwordx4 v101, s[40:43], s75 offen offset:0 lds\n"
"v_mfma_f32_16x16x32_bf16 v[184:187], v[12:15], v[72:75], v[184:187]\n"
"s_add_u32 s90, s90, 16384\n"
"s_and_b32 s73, s92, 1\n"
"ds_read_b64_tr_b16 v[50:51], v112 offset:58624\n"
"v_mfma_f32_16x16x32_bf16 v[180:183], v[12:15], v[68:71], v[180:183]\n"
"s_lshl_b32 s73, s73, 11\n"
"s_add_u32 s90, s90, s73\n"
"ds_read_b64_tr_b16 v[52:53], v113 offset:58368\n"
"v_mfma_f32_16x16x32_bf16 v[176:179], v[12:15], v[64:67], v[176:179]\n"
"s_lshr_b32 s73, s92, 1\n"
"s_lshl_b32 s73, s73, 2\n"
"ds_read_b64_tr_b16 v[54:55], v113 offset:58624\n"
"v_mfma_f32_16x16x32_bf16 v[192:195], v[16:19], v[64:67], v[192:195]\n"
"s_add_u32 s90, s90, s73\n"
"s_cmp_eq_u32 s84, 12\ns_cselect_b64 vcc, -1, 0\n"
"ds_read_b64_tr_b16 v[56:57], v112 offset:58880\n"
"v_mfma_f32_16x16x32_bf16 v[196:199], v[16:19], v[68:71], v[196:199]\n"
"s_add_u32 s75, s86, 80896\n"
"ds_read_b64_tr_b16 v[58:59], v112 offset:59136\n"
"buffer_load_dwordx4 v102, s[40:43], s75 offen offset:1024 lds\n"
"v_mfma_f32_16x16x32_bf16 v[200:203], v[16:19], v[72:75], v[200:203]\n"
"ds_read_b64_tr_b16 v[60:61], v113 offset:58880\n"
"v_accvgpr_read_b32 v109, a40\n"
"v_mfma_f32_16x16x32_bf16 v[204:207], v[16:19], v[76:79], v[204:207]\n"
"ds_read_b64_tr_b16 v[62:63], v113 offset:59136\n"
"v_accvgpr_read_b32 v110, a36\n"
"v_mfma_f32_16x16x32_bf16 v[220:223], v[20:23], v[76:79], v[220:223]\n"
"v_cndmask_b32 v110, v109, v110, vcc\n"
"v_accvgpr_read_b32 v109, a32\n"
"v_mfma_f32_16x16x32_bf16 v[216:219], v[20:23], v[72:75], v[216:219]\n"
"v_add_f32 v109, v109, v110\n"
"v_cvt_pk_bf16_f32 v110, v109, v109\n"
"v_mfma_f32_16x16x32_bf16 v[212:215], v[20:23], v[68:71], v[212:215]\n"
"s_add_u32 s75, s86, 96256\n"
"buffer_load_dwordx4 v101, s[40:43], s75 offen offset:2048 lds\n"
"v_lshlrev_b32 v110, 16, v110\n"
"v_mfma_f32_16x16x32_bf16 v[208:211], v[20:23], v[64:67], v[208:211]\n"
"v_sub_f32 v110, v109, v110\n"
"v_cvt_pk_bf16_f32 v107, v109, v110\n"
"v_mfma_f32_16x16x32_bf16 v[224:227], v[24:27], v[64:67], v[224:227]\n"
"buffer_store_dword v107, v99, s[60:63], s90 offen offset:0 sc1\n"
"v_mfma_f32_16x16x32_bf16 v[228:231], v[24:27], v[68:71], v[228:231]\n"
"s_cmp_eq_u32 s84, 12\ns_cselect_b64 vcc, -1, 0\n"
"v_accvgpr_read_b32 v109, a41\n"
"v_accvgpr_read_b32 v110, a37\n"
"v_mfma_f32_16x16x32_bf16 v[232:235], v[24:27], v[72:75], v[232:235]\n"
"v_cndmask_b32 v110, v109, v110, vcc\n"
"v_accvgpr_read_b32 v109, a33\n"
"v_mfma_f32_16x16x32_bf16 v[236:239], v[24:27], v[76:79], v[236:239]\n"
"s_add_u32 s75, s86, 111616\n"
"buffer_load_dwordx4 v102, s[40:43], s75 offen offset:3072 lds\n"
"v_add_f32 v109, v109, v110\n"
"v_mfma_f32_16x16x32_bf16 v[252:255], v[28:31], v[76:79], v[252:255]\n"
"s_bfe_u32 s85, s79, 0x40000\n"
"v_cvt_pk_bf16_f32 v110, v109, v109\n"
"v_lshlrev_b32 v110, 16, v110\n"
"v_mfma_f32_16x16x32_bf16 v[248:251], v[28:31], v[72:75], v[248:251]\n"
"s_lshl_b32 s86, s85, 7\n"
"v_sub_f32 v110, v109, v110\n"
"v_cvt_pk_bf16_f32 v111, v109, v110\n"
"v_mfma_f32_16x16x32_bf16 v[244:247], v[28:31], v[68:71], v[244:247]\n"
"s_lshl_b32 s87, s85, 19\n"
"buffer_store_dword v111, v99, s[60:63], s90 offen offset:256 sc1\n"
"v_mfma_f32_16x16x32_bf16 v[240:243], v[28:31], v[64:67], v[240:243]\n"
"s_waitcnt vmcnt(14) lgkmcnt(0)\n"
"s_barrier\n"
"v_mfma_f32_16x16x32_bf16 v[128:131], v[32:35], v[80:83], v[128:131]\n"
"v_mfma_f32_16x16x32_bf16 v[132:135], v[32:35], v[84:87], v[132:135]\n"
"ds_read_b128 v[64:67], v98 offset:0\n"
"v_mfma_f32_16x16x32_bf16 v[136:139], v[32:35], v[88:91], v[136:139]\n"
"s_add_u32 m0, s69, 49152\n"
"ds_read_b128 v[68:71], v98 offset:2048\n"
"v_mfma_f32_16x16x32_bf16 v[140:143], v[32:35], v[92:95], v[140:143]\n"
"s_add_u32 s75, s87, 1024\n"
"ds_read_b128 v[72:75], v98 offset:4096\n"
"buffer_load_dwordx4 v104, s[44:47], s75 offen offset:0 lds\n"
"v_mfma_f32_16x16x32_bf16 v[156:159], v[36:39], v[92:95], v[156:159]\n"
"ds_read_b128 v[76:79], v98 offset:6144\n"
"v_mfma_f32_16x16x32_bf16 v[152:155], v[36:39], v[88:91], v[152:155]\n"
"ds_read_b64_tr_b16 v[0:1], v105 offset:0\n"
"v_mfma_f32_16x16x32_bf16 v[148:151], v[36:39], v[84:87], v[148:151]\n"
"ds_read_b64_tr_b16 v[2:3], v105 offset:256\n"
"v_mfma_f32_16x16x32_bf16 v[144:147], v[36:39], v[80:83], v[144:147]\n"
"ds_read_b64_tr_b16 v[4:5], v106 offset:0\n"
"v_mfma_f32_16x16x32_bf16 v[160:163], v[40:43], v[80:83], v[160:163]\n"
"s_add_u32 s75, s87, 128\n"
"ds_read_b64_tr_b16 v[6:7], v106 offset:256\n"
"buffer_load_dwordx4 v104, s[44:47], s75 offen offset:1024 lds\n"
"v_mfma_f32_16x16x32_bf16 v[164:167], v[40:43], v[84:87], v[164:167]\n"
"ds_read_b64_tr_b16 v[8:9], v105 offset:512\n"
"v_mfma_f32_16x16x32_bf16 v[168:171], v[40:43], v[88:91], v[168:171]\n"
"ds_read_b64_tr_b16 v[10:11], v105 offset:768\n"
"v_mfma_f32_16x16x32_bf16 v[172:175], v[40:43], v[92:95], v[172:175]\n"
"ds_read_b64_tr_b16 v[12:13], v106 offset:512\n"
"v_mfma_f32_16x16x32_bf16 v[188:191], v[44:47], v[92:95], v[188:191]\n"
"s_add_u32 m0, s70, 114688\n"
"ds_read_b64_tr_b16 v[14:15], v106 offset:768\n"
"v_mfma_f32_16x16x32_bf16 v[184:187], v[44:47], v[88:91], v[184:187]\n"
"s_add_u32 s75, s86, 0\n"
"ds_read_b64_tr_b16 v[16:17], v105 offset:1024\n"
"buffer_load_dwordx4 v101, s[40:43], s75 offen offset:0 lds\n"
"v_mfma_f32_16x16x32_bf16 v[180:183], v[44:47], v[84:87], v[180:183]\n"
"ds_read_b64_tr_b16 v[18:19], v105 offset:1280\n"
"v_mfma_f32_16x16x32_bf16 v[176:179], v[44:47], v[80:83], v[176:179]\n"
"ds_read_b64_tr_b16 v[20:21], v106 offset:1024\n"
"v_mfma_f32_16x16x32_bf16 v[192:195], v[48:51], v[80:83], v[192:195]\n"
"ds_read_b64_tr_b16 v[22:23], v106 offset:1280\n"
"v_mfma_f32_16x16x32_bf16 v[196:199], v[48:51], v[84:87], v[196:199]\n"
"ds_read_b64_tr_b16 v[24:25], v105 offset:1536\n"
"v_mfma_f32_16x16x32_bf16 v[200:203], v[48:51], v[88:91], v[200:203]\n"
"s_add_u32 s75, s86, 15360\n"
"ds_read_b64_tr_b16 v[26:27], v105 offset:1792\n"
"buffer_load_dwordx4 v102, s[40:43], s75 offen offset:1024 lds\n"
"v_mfma_f32_16x16x32_bf16 v[204:207], v[48:51], v[92:95], v[204:207]\n"
"ds_read_b64_tr_b16 v[28:29], v106 offset:1536\n"
"v_mfma_f32_16x16x32_bf16 v[220:223], v[52:55], v[92:95], v[220:223]\n"
"ds_read_b64_tr_b16 v[30:31], v106 offset:1792\n"
"v_mfma_f32_16x16x32_bf16 v[216:219], v[52:55], v[88:91], v[216:219]\n"
"v_mfma_f32_16x16x32_bf16 v[212:215], v[52:55], v[84:87], v[212:215]\n"
"v_mfma_f32_16x16x32_bf16 v[208:211], v[52:55], v[80:83], v[208:211]\n"
"s_add_u32 s75, s86, 30720\n"
"buffer_load_dwordx4 v101, s[40:43], s75 offen offset:2048 lds\n"
"v_mfma_f32_16x16x32_bf16 v[224:227], v[56:59], v[80:83], v[224:227]\n"
"v_mfma_f32_16x16x32_bf16 v[228:231], v[56:59], v[84:87], v[228:231]\n"
"v_mfma_f32_16x16x32_bf16 v[232:235], v[56:59], v[88:91], v[232:235]\n"
"v_mfma_f32_16x16x32_bf16 v[236:239], v[56:59], v[92:95], v[236:239]\n"
"v_mfma_f32_16x16x32_bf16 v[252:255], v[60:63], v[92:95], v[252:255]\n"
"s_add_u32 s75, s86, 46080\n"
"buffer_load_dwordx4 v102, s[40:43], s75 offen offset:3072 lds\n"
"v_mfma_f32_16x16x32_bf16 v[248:251], v[60:63], v[88:91], v[248:251]\n"
"v_mfma_f32_16x16x32_bf16 v[244:247], v[60:63], v[84:87], v[244:247]\n"
"v_mfma_f32_16x16x32_bf16 v[240:243], v[60:63], v[80:83], v[240:243]\n"
"s_waitcnt lgkmcnt(0)\n"
"v_mfma_f32_16x16x32_bf16 v[128:131], v[0:3], v[64:67], v[128:131]\n"
"ds_read_b128 v[80:83], v100 offset:0\n"
"v_mov_b32 v109, 147456\n"
"v_mfma_f32_16x16x32_bf16 v[132:135], v[0:3], v[68:71], v[132:135]\n"
"s_add_u32 m0, s69, 57344\n"
"ds_read_b128 v[84:87], v100 offset:2048\n"
"ds_read_b32 v99, v109 offset:0\n"
"v_mfma_f32_16x16x32_bf16 v[136:139], v[0:3], v[72:75], v[136:139]\n"
"s_add_u32 s75, s87, 263168\n"
"ds_read_b128 v[88:91], v100 offset:4096\n"
"buffer_load_dwordx4 v104, s[44:47], s75 offen offset:0 lds\n"
"v_mfma_f32_16x16x32_bf16 v[140:143], v[0:3], v[76:79], v[140:143]\n"
"ds_read_b128 v[92:95], v100 offset:6144\n"
"ds_read_b32 v103, v109 offset:4\n"
"v_mfma_f32_16x16x32_bf16 v[156:159], v[4:7], v[76:79], v[156:159]\n"
"ds_read_b64_tr_b16 v[32:33], v105 offset:8192\n"
"ds_read_b32 v107, v109 offset:8\n"
"v_mfma_f32_16x16x32_bf16 v[152:155], v[4:7], v[72:75], v[152:155]\n"
"ds_read_b64_tr_b16 v[34:35], v105 offset:8448\n"
"ds_read_b32 v111, v109 offset:12\n"
"v_mfma_f32_16x16x32_bf16 v[148:151], v[4:7], v[68:71], v[148:151]\n"
"ds_read_b64_tr_b16 v[36:37], v106 offset:8192\n"
"v_mfma_f32_16x16x32_bf16 v[144:147], v[4:7], v[64:67], v[144:147]\n"
"s_add_u32 s75, s87, 262272\n"
"ds_read_b64_tr_b16 v[38:39], v106 offset:8448\n"
"buffer_load_dwordx4 v104, s[44:47], s75 offen offset:1024 lds\n"
"v_mfma_f32_16x16x32_bf16 v[160:163], v[8:11], v[64:67], v[160:163]\n"
"ds_read_b64_tr_b16 v[40:41], v105 offset:8704\n"
"v_mfma_f32_16x16x32_bf16 v[164:167], v[8:11], v[68:71], v[164:167]\n"
"ds_read_b64_tr_b16 v[42:43], v105 offset:8960\n"
"v_mfma_f32_16x16x32_bf16 v[168:171], v[8:11], v[72:75], v[168:171]\n"
"ds_read_b64_tr_b16 v[44:45], v106 offset:8704\n"
"v_mfma_f32_16x16x32_bf16 v[172:175], v[8:11], v[76:79], v[172:175]\n"
"s_add_u32 m0, s70, 118784\n"
"ds_read_b64_tr_b16 v[46:47], v106 offset:8960\n"
"v_mfma_f32_16x16x32_bf16 v[188:191], v[12:15], v[76:79], v[188:191]\n"
"s_add_u32 s75, s86, 65536\n"
"ds_read_b64_tr_b16 v[48:49], v105 offset:9216\n"
"buffer_load_dwordx4 v101, s[40:43], s75 offen offset:0 lds\n"
"v_mfma_f32_16x16x32_bf16 v[184:187], v[12:15], v[72:75], v[184:187]\n"
"ds_read_b64_tr_b16 v[50:51], v105 offset:9472\n"
"v_mfma_f32_16x16x32_bf16 v[180:183], v[12:15], v[68:71], v[180:183]\n"
"ds_read_b64_tr_b16 v[52:53], v106 offset:9216\n"
"v_mfma_f32_16x16x32_bf16 v[176:179], v[12:15], v[64:67], v[176:179]\n"
"ds_read_b64_tr_b16 v[54:55], v106 offset:9472\n"
"v_mfma_f32_16x16x32_bf16 v[192:195], v[16:19], v[64:67], v[192:195]\n"
"ds_read_b64_tr_b16 v[56:57], v105 offset:9728\n"
"v_mfma_f32_16x16x32_bf16 v[196:199], v[16:19], v[68:71], v[196:199]\n"
"s_add_u32 s75, s86, 80896\n"
"ds_read_b64_tr_b16 v[58:59], v105 offset:9984\n"
"buffer_load_dwordx4 v102, s[40:43], s75 offen offset:1024 lds\n"
"v_mfma_f32_16x16x32_bf16 v[200:203], v[16:19], v[72:75], v[200:203]\n"
"ds_read_b64_tr_b16 v[60:61], v106 offset:9728\n"
"v_mfma_f32_16x16x32_bf16 v[204:207], v[16:19], v[76:79], v[204:207]\n"
"ds_read_b64_tr_b16 v[62:63], v106 offset:9984\n"
"v_mfma_f32_16x16x32_bf16 v[220:223], v[20:23], v[76:79], v[220:223]\n"
"v_mfma_f32_16x16x32_bf16 v[216:219], v[20:23], v[72:75], v[216:219]\n"
"v_mfma_f32_16x16x32_bf16 v[212:215], v[20:23], v[68:71], v[212:215]\n"
"s_add_u32 s75, s86, 96256\n"
"buffer_load_dwordx4 v101, s[40:43], s75 offen offset:2048 lds\n"
"v_mfma_f32_16x16x32_bf16 v[208:211], v[20:23], v[64:67], v[208:211]\n"
"v_mfma_f32_16x16x32_bf16 v[224:227], v[24:27], v[64:67], v[224:227]\n"
"v_mfma_f32_16x16x32_bf16 v[228:231], v[24:27], v[68:71], v[228:231]\n"
"v_mfma_f32_16x16x32_bf16 v[232:235], v[24:27], v[72:75], v[232:235]\n"
"v_mfma_f32_16x16x32_bf16 v[236:239], v[24:27], v[76:79], v[236:239]\n"
"s_add_u32 s75, s86, 111616\n"
"buffer_load_dwordx4 v102, s[40:43], s75 offen offset:3072 lds\n"
"v_mfma_f32_16x16x32_bf16 v[252:255], v[28:31], v[76:79], v[252:255]\n"
"s_bfe_u32 s85, s79, 0x40004\n"
"v_mfma_f32_16x16x32_bf16 v[248:251], v[28:31], v[72:75], v[248:251]\n"
"s_lshl_b32 s86, s85, 7\n"
"v_mfma_f32_16x16x32_bf16 v[244:247], v[28:31], v[68:71], v[244:247]\n"
"s_lshl_b32 s87, s85, 19\n"
"v_mfma_f32_16x16x32_bf16 v[240:243], v[28:31], v[64:67], v[240:243]\n"
"s_waitcnt vmcnt(12) lgkmcnt(0)\n"
"s_barrier\n"
"v_mfma_f32_16x16x32_bf16 v[128:131], v[32:35], v[80:83], v[128:131]\n"
"v_max3_u32 v110, v99, v103, v107\n"
"v_max_u32 v110, v110, v111\n"
"v_mfma_f32_16x16x32_bf16 v[132:135], v[32:35], v[84:87], v[132:135]\n"
"ds_read_b128 v[64:67], v96 offset:0\n"
"v_mov_b32 v111, s76\n"
"v_mfma_f32_16x16x32_bf16 v[136:139], v[32:35], v[88:91], v[136:139]\n"
"s_add_u32 m0, s69, 65536\n"
"ds_read_b128 v[68:71], v96 offset:2048\n"
"v_lshlrev_b32 v99, 24, v108\n"
"v_mfma_f32_16x16x32_bf16 v[140:143], v[32:35], v[92:95], v[140:143]\n"
"s_add_u32 s75, s87, 1024\n"
"ds_read_b128 v[72:75], v96 offset:4096\n"
"buffer_load_dwordx4 v104, s[44:47], s75 offen offset:0 lds\n"
"v_mfma_f32_16x16x32_bf16 v[156:159], v[36:39], v[92:95], v[156:159]\n"
"s_lshl_b32 s90, s93, 3\n"
"ds_read_b128 v[76:79], v96 offset:6144\n"
"buffer_store_dwordx2 v[110:111], v99, s[64:67], s90 offen offset:0 sc1\n"
"v_mfma_f32_16x16x32_bf16 v[152:155], v[36:39], v[88:91], v[152:155]\n"
"ds_read_b64_tr_b16 v[0:1], v112 offset:0\n"
"v_mfma_f32_16x16x32_bf16 v[148:151], v[36:39], v[84:87], v[148:151]\n"
"ds_read_b64_tr_b16 v[2:3], v112 offset:256\n"
"v_mfma_f32_16x16x32_bf16 v[144:147], v[36:39], v[80:83], v[144:147]\n"
"ds_read_b64_tr_b16 v[4:5], v113 offset:0\n"
"v_mfma_f32_16x16x32_bf16 v[160:163], v[40:43], v[80:83], v[160:163]\n"
"s_add_u32 s75, s87, 128\n"
"ds_read_b64_tr_b16 v[6:7], v113 offset:256\n"
"buffer_load_dwordx4 v104, s[44:47], s75 offen offset:1024 lds\n"
"v_mfma_f32_16x16x32_bf16 v[164:167], v[40:43], v[84:87], v[164:167]\n"
"ds_read_b64_tr_b16 v[8:9], v112 offset:512\n"
"v_mfma_f32_16x16x32_bf16 v[168:171], v[40:43], v[88:91], v[168:171]\n"
"ds_read_b64_tr_b16 v[10:11], v112 offset:768\n"
"v_mfma_f32_16x16x32_bf16 v[172:175], v[40:43], v[92:95], v[172:175]\n"
"ds_read_b64_tr_b16 v[12:13], v113 offset:512\n"
"v_mfma_f32_16x16x32_bf16 v[188:191], v[44:47], v[92:95], v[188:191]\n"
"s_add_u32 m0, s70, 81920\n"
"ds_read_b64_tr_b16 v[14:15], v113 offset:768\n"
"v_mfma_f32_16x16x32_bf16 v[184:187], v[44:47], v[88:91], v[184:187]\n"
"s_add_u32 s75, s86, 0\n"
"ds_read_b64_tr_b16 v[16:17], v112 offset:1024\n"
"buffer_load_dwordx4 v101, s[40:43], s75 offen offset:0 lds\n"
"v_mfma_f32_16x16x32_bf16 v[180:183], v[44:47], v[84:87], v[180:183]\n"
"ds_read_b64_tr_b16 v[18:19], v112 offset:1280\n"
"v_mfma_f32_16x16x32_bf16 v[176:179], v[44:47], v[80:83], v[176:179]\n"
"ds_read_b64_tr_b16 v[20:21], v113 offset:1024\n"
"v_mfma_f32_16x16x32_bf16 v[192:195], v[48:51], v[80:83], v[192:195]\n"
"ds_read_b64_tr_b16 v[22:23], v113 offset:1280\n"
"v_mfma_f32_16x16x32_bf16 v[196:199], v[48:51], v[84:87], v[196:199]\n"
"ds_read_b64_tr_b16 v[24:25], v112 offset:1536\n"
"v_mfma_f32_16x16x32_bf16 v[200:203], v[48:51], v[88:91], v[200:203]\n"
"s_add_u32 s75, s86, 15360\n"
"ds_read_b64_tr_b16 v[26:27], v112 offset:1792\n"
"buffer_load_dwordx4 v102, s[40:43], s75 offen offset:1024 lds\n"
"v_mfma_f32_16x16x32_bf16 v[204:207], v[48:51], v[92:95], v[204:207]\n"
"ds_read_b64_tr_b16 v[28:29], v113 offset:1536\n"
"v_mfma_f32_16x16x32_bf16 v[220:223], v[52:55], v[92:95], v[220:223]\n"
"ds_read_b64_tr_b16 v[30:31], v113 offset:1792\n"
"v_mfma_f32_16x16x32_bf16 v[216:219], v[52:55], v[88:91], v[216:219]\n"
"v_mfma_f32_16x16x32_bf16 v[212:215], v[52:55], v[84:87], v[212:215]\n"
"v_mfma_f32_16x16x32_bf16 v[208:211], v[52:55], v[80:83], v[208:211]\n"
"s_add_u32 s75, s86, 30720\n"
"buffer_load_dwordx4 v101, s[40:43], s75 offen offset:2048 lds\n"
"v_mfma_f32_16x16x32_bf16 v[224:227], v[56:59], v[80:83], v[224:227]\n"
"v_mfma_f32_16x16x32_bf16 v[228:231], v[56:59], v[84:87], v[228:231]\n"
"v_mfma_f32_16x16x32_bf16 v[232:235], v[56:59], v[88:91], v[232:235]\n"
"v_mfma_f32_16x16x32_bf16 v[236:239], v[56:59], v[92:95], v[236:239]\n"
"v_mfma_f32_16x16x32_bf16 v[252:255], v[60:63], v[92:95], v[252:255]\n"
"s_add_u32 s75, s86, 46080\n"
"buffer_load_dwordx4 v102, s[40:43], s75 offen offset:3072 lds\n"
"v_mfma_f32_16x16x32_bf16 v[248:251], v[60:63], v[88:91], v[248:251]\n"
"v_mfma_f32_16x16x32_bf16 v[244:247], v[60:63], v[84:87], v[244:247]\n"
"v_mfma_f32_16x16x32_bf16 v[240:243], v[60:63], v[80:83], v[240:243]\n"
"s_waitcnt lgkmcnt(0)\n"
"v_mfma_f32_16x16x32_bf16 v[128:131], v[0:3], v[64:67], v[128:131]\n"
"ds_read_b128 v[80:83], v97 offset:0\n"
"v_mfma_f32_16x16x32_bf16 v[132:135], v[0:3], v[68:71], v[132:135]\n"
"s_add_u32 m0, s69, 73728\n"
"ds_read_b128 v[84:87], v97 offset:2048\n"
"v_mfma_f32_16x16x32_bf16 v[136:139], v[0:3], v[72:75], v[136:139]\n"
"s_add_u32 s75, s87, 263168\n"
"ds_read_b128 v[88:91], v97 offset:4096\n"
"buffer_load_dwordx4 v104, s[44:47], s75 offen offset:0 lds\n"
"v_mfma_f32_16x16x32_bf16 v[140:143], v[0:3], v[76:79], v[140:143]\n"
"ds_read_b128 v[92:95], v97 offset:6144\n"
"v_mfma_f32_16x16x32_bf16 v[156:159], v[4:7], v[76:79], v[156:159]\n"
"ds_read_b64_tr_b16 v[32:33], v112 offset:8192\n"
"v_mfma_f32_16x16x32_bf16 v[152:155], v[4:7], v[72:75], v[152:155]\n"
"ds_read_b64_tr_b16 v[34:35], v112 offset:8448\n"
"v_mfma_f32_16x16x32_bf16 v[148:151], v[4:7], v[68:71], v[148:151]\n"
"ds_read_b64_tr_b16 v[36:37], v113 offset:8192\n"
"v_mfma_f32_16x16x32_bf16 v[144:147], v[4:7], v[64:67], v[144:147]\n"
"s_add_u32 s75, s87, 262272\n"
"ds_read_b64_tr_b16 v[38:39], v113 offset:8448\n"
"buffer_load_dwordx4 v104, s[44:47], s75 offen offset:1024 lds\n"
"v_mfma_f32_16x16x32_bf16 v[160:163], v[8:11], v[64:67], v[160:163]\n"
"ds_read_b64_tr_b16 v[40:41], v112 offset:8704\n"
"v_mfma_f32_16x16x32_bf16 v[164:167], v[8:11], v[68:71], v[164:167]\n"
"ds_read_b64_tr_b16 v[42:43], v112 offset:8960\n"
"v_mfma_f32_16x16x32_bf16 v[168:171], v[8:11], v[72:75], v[168:171]\n"
"ds_read_b64_tr_b16 v[44:45], v113 offset:8704\n"
"v_mfma_f32_16x16x32_bf16 v[172:175], v[8:11], v[76:79], v[172:175]\n"
"s_add_u32 m0, s70, 86016\n"
"ds_read_b64_tr_b16 v[46:47], v113 offset:8960\n"
"v_mfma_f32_16x16x32_bf16 v[188:191], v[12:15], v[76:79], v[188:191]\n"
"s_add_u32 s75, s86, 65536\n"
"ds_read_b64_tr_b16 v[48:49], v112 offset:9216\n"
"buffer_load_dwordx4 v101, s[40:43], s75 offen offset:0 lds\n"
"v_mfma_f32_16x16x32_bf16 v[184:187], v[12:15], v[72:75], v[184:187]\n"
"ds_read_b64_tr_b16 v[50:51], v112 offset:9472\n"
"v_mfma_f32_16x16x32_bf16 v[180:183], v[12:15], v[68:71], v[180:183]\n"
"ds_read_b64_tr_b16 v[52:53], v113 offset:9216\n"
"v_mfma_f32_16x16x32_bf16 v[176:179], v[12:15], v[64:67], v[176:179]\n"
"ds_read_b64_tr_b16 v[54:55], v113 offset:9472\n"
"v_mfma_f32_16x16x32_bf16 v[192:195], v[16:19], v[64:67], v[192:195]\n"
"ds_read_b64_tr_b16 v[56:57], v112 offset:9728\n"
"v_mfma_f32_16x16x32_bf16 v[196:199], v[16:19], v[68:71], v[196:199]\n"
"s_add_u32 s75, s86, 80896\n"
"ds_read_b64_tr_b16 v[58:59], v112 offset:9984\n"
"buffer_load_dwordx4 v102, s[40:43], s75 offen offset:1024 lds\n"
"v_mfma_f32_16x16x32_bf16 v[200:203], v[16:19], v[72:75], v[200:203]\n"
"ds_read_b64_tr_b16 v[60:61], v113 offset:9728\n"
"v_mfma_f32_16x16x32_bf16 v[204:207], v[16:19], v[76:79], v[204:207]\n"
"ds_read_b64_tr_b16 v[62:63], v113 offset:9984\n"
"v_mfma_f32_16x16x32_bf16 v[220:223], v[20:23], v[76:79], v[220:223]\n"
"v_mfma_f32_16x16x32_bf16 v[216:219], v[20:23], v[72:75], v[216:219]\n"
"v_mfma_f32_16x16x32_bf16 v[212:215], v[20:23], v[68:71], v[212:215]\n"
"s_add_u32 s75, s86, 96256\n"
"buffer_load_dwordx4 v101, s[40:43], s75 offen offset:2048 lds\n"
"v_mfma_f32_16x16x32_bf16 v[208:211], v[20:23], v[64:67], v[208:211]\n"
"v_mfma_f32_16x16x32_bf16 v[224:227], v[24:27], v[64:67], v[224:227]\n"
"v_mfma_f32_16x16x32_bf16 v[228:231], v[24:27], v[68:71], v[228:231]\n"
"v_mfma_f32_16x16x32_bf16 v[232:235], v[24:27], v[72:75], v[232:235]\n"
"v_mfma_f32_16x16x32_bf16 v[236:239], v[24:27], v[76:79], v[236:239]\n"
"s_add_u32 s75, s86, 111616\n"
"buffer_load_dwordx4 v102, s[40:43], s75 offen offset:3072 lds\n"
"v_mfma_f32_16x16x32_bf16 v[252:255], v[28:31], v[76:79], v[252:255]\n"
"s_bfe_u32 s85, s79, 0x40008\n"
"v_mfma_f32_16x16x32_bf16 v[248:251], v[28:31], v[72:75], v[248:251]\n"
"s_lshl_b32 s86, s85, 7\n"
"v_mfma_f32_16x16x32_bf16 v[244:247], v[28:31], v[68:71], v[244:247]\n"
"s_lshl_b32 s87, s85, 19\n"
"v_mfma_f32_16x16x32_bf16 v[240:243], v[28:31], v[64:67], v[240:243]\n"
"s_waitcnt vmcnt(13) lgkmcnt(0)\n"
"s_barrier\n"
"v_mfma_f32_16x16x32_bf16 v[128:131], v[32:35], v[80:83], v[128:131]\n"
"v_mfma_f32_16x16x32_bf16 v[132:135], v[32:35], v[84:87], v[132:135]\n"
"ds_read_b128 v[64:67], v98 offset:32768\n"
"v_mfma_f32_16x16x32_bf16 v[136:139], v[32:35], v[88:91], v[136:139]\n"
"s_add_u32 m0, s69, 0\n"
"ds_read_b128 v[68:71], v98 offset:34816\n"
"v_mfma_f32_16x16x32_bf16 v[140:143], v[32:35], v[92:95], v[140:143]\n"
"s_add_u32 s75, s87, 1024\n"
"ds_read_b128 v[72:75], v98 offset:36864\n"
"buffer_load_dwordx4 v104, s[44:47], s75 offen offset:0 lds\n"
"v_mfma_f32_16x16x32_bf16 v[156:159], v[36:39], v[92:95], v[156:159]\n"
"ds_read_b128 v[76:79], v98 offset:38912\n"
"v_mfma_f32_16x16x32_bf16 v[152:155], v[36:39], v[88:91], v[152:155]\n"
"ds_read_b64_tr_b16 v[0:1], v112 offset:49152\n"
"v_mfma_f32_16x16x32_bf16 v[148:151], v[36:39], v[84:87], v[148:151]\n"
"ds_read_b64_tr_b16 v[2:3], v112 offset:49408\n"
"v_mfma_f32_16x16x32_bf16 v[144:147], v[36:39], v[80:83], v[144:147]\n"
"ds_read_b64_tr_b16 v[4:5], v113 offset:49152\n"
"v_mfma_f32_16x16x32_bf16 v[160:163], v[40:43], v[80:83], v[160:163]\n"
"s_add_u32 s75, s87, 128\n"
"ds_read_b64_tr_b16 v[6:7], v113 offset:49408\n"
"buffer_load_dwordx4 v104, s[44:47], s75 offen offset:1024 lds\n"
"v_mfma_f32_16x16x32_bf16 v[164:167], v[40:43], v[84:87], v[164:167]\n"
"ds_read_b64_tr_b16 v[8:9], v112 offset:49664\n"
"v_mfma_f32_16x16x32_bf16 v[168:171], v[40:43], v[88:91], v[168:171]\n"
"ds_read_b64_tr_b16 v[10:11], v112 offset:49920\n"
"v_mfma_f32_16x16x32_bf16 v[172:175], v[40:43], v[92:95], v[172:175]\n"
"ds_read_b64_tr_b16 v[12:13], v113 offset:49664\n"
"v_mfma_f32_16x16x32_bf16 v[188:191], v[44:47], v[92:95], v[188:191]\n"
"s_add_u32 m0, s70, 16384\n"
"ds_read_b64_tr_b16 v[14:15], v113 offset:49920\n"
"v_mfma_f32_16x16x32_bf16 v[184:187], v[44:47], v[88:91], v[184:187]\n"
"s_add_u32 s75, s86, 0\n"
"ds_read_b64_tr_b16 v[16:17], v112 offset:50176\n"
"buffer_load_dwordx4 v101, s[40:43], s75 offen offset:0 lds\n"
"v_mfma_f32_16x16x32_bf16 v[180:183], v[44:47], v[84:87], v[180:183]\n"
"ds_read_b64_tr_b16 v[18:19], v112 offset:50432\n"
"v_mfma_f32_16x16x32_bf16 v[176:179], v[44:47], v[80:83], v[176:179]\n"
"ds_read_b64_tr_b16 v[20:21], v113 offset:50176\n"
"v_mfma_f32_16x16x32_bf16 v[192:195], v[48:51], v[80:83], v[192:195]\n"
"ds_read_b64_tr_b16 v[22:23], v113 offset:50432\n"
"v_mfma_f32_16x16x32_bf16 v[196:199], v[48:51], v[84:87], v[196:199]\n"
"ds_read_b64_tr_b16 v[24:25], v112 offset:50688\n"
"v_mfma_f32_16x16x32_bf16 v[200:203], v[48:51], v[88:91], v[200:203]\n"
"s_add_u32 s75, s86, 15360\n"
"ds_read_b64_tr_b16 v[26:27], v112 offset:50944\n"
"buffer_load_dwordx4 v102, s[40:43], s75 offen offset:1024 lds\n"
"v_mfma_f32_16x16x32_bf16 v[204:207], v[48:51], v[92:95], v[204:207]\n"
"ds_read_b64_tr_b16 v[28:29], v113 offset:50688\n"
"v_mfma_f32_16x16x32_bf16 v[220:223], v[52:55], v[92:95], v[220:223]\n"
"ds_read_b64_tr_b16 v[30:31], v113 offset:50944\n"
"v_mfma_f32_16x16x32_bf16 v[216:219], v[52:55], v[88:91], v[216:219]\n"
"v_mfma_f32_16x16x32_bf16 v[212:215], v[52:55], v[84:87], v[212:215]\n"
"v_mfma_f32_16x16x32_bf16 v[208:211], v[52:55], v[80:83], v[208:211]\n"
"s_add_u32 s75, s86, 30720\n"
"buffer_load_dwordx4 v101, s[40:43], s75 offen offset:2048 lds\n"
"v_mfma_f32_16x16x32_bf16 v[224:227], v[56:59], v[80:83], v[224:227]\n"
"v_mfma_f32_16x16x32_bf16 v[228:231], v[56:59], v[84:87], v[228:231]\n"
"v_mfma_f32_16x16x32_bf16 v[232:235], v[56:59], v[88:91], v[232:235]\n"
"v_mfma_f32_16x16x32_bf16 v[236:239], v[56:59], v[92:95], v[236:239]\n"
"v_mfma_f32_16x16x32_bf16 v[252:255], v[60:63], v[92:95], v[252:255]\n"
"s_add_u32 s75, s86, 46080\n"
"buffer_load_dwordx4 v102, s[40:43], s75 offen offset:3072 lds\n"
"v_mfma_f32_16x16x32_bf16 v[248:251], v[60:63], v[88:91], v[248:251]\n"
"v_mfma_f32_16x16x32_bf16 v[244:247], v[60:63], v[84:87], v[244:247]\n"
"v_mfma_f32_16x16x32_bf16 v[240:243], v[60:63], v[80:83], v[240:243]\n"
"s_waitcnt lgkmcnt(0)\n"
"v_mfma_f32_16x16x32_bf16 v[128:131], v[0:3], v[64:67], v[128:131]\n"
"ds_read_b128 v[80:83], v100 offset:32768\n"
"v_mfma_f32_16x16x32_bf16 v[132:135], v[0:3], v[68:71], v[132:135]\n"
"s_add_u32 m0, s69, 8192\n"
"ds_read_b128 v[84:87], v100 offset:34816\n"
"v_mfma_f32_16x16x32_bf16 v[136:139], v[0:3], v[72:75], v[136:139]\n"
"s_add_u32 s75, s87, 263168\n"
"ds_read_b128 v[88:91], v100 offset:36864\n"
"buffer_load_dwordx4 v104, s[44:47], s75 offen offset:0 lds\n"
"v_mfma_f32_16x16x32_bf16 v[140:143], v[0:3], v[76:79], v[140:143]\n"
"ds_read_b128 v[92:95], v100 offset:38912\n"
"v_mfma_f32_16x16x32_bf16 v[156:159], v[4:7], v[76:79], v[156:159]\n"
"ds_read_b64_tr_b16 v[32:33], v112 offset:57344\n"
"v_mfma_f32_16x16x32_bf16 v[152:155], v[4:7], v[72:75], v[152:155]\n"
"ds_read_b64_tr_b16 v[34:35], v112 offset:57600\n"
"v_mfma_f32_16x16x32_bf16 v[148:151], v[4:7], v[68:71], v[148:151]\n"
"ds_read_b64_tr_b16 v[36:37], v113 offset:57344\n"
"v_mfma_f32_16x16x32_bf16 v[144:147], v[4:7], v[64:67], v[144:147]\n"
"s_add_u32 s75, s87, 262272\n"
"ds_read_b64_tr_b16 v[38:39], v113 offset:57600\n"
"buffer_load_dwordx4 v104, s[44:47], s75 offen offset:1024 lds\n"
"v_mfma_f32_16x16x32_bf16 v[160:163], v[8:11], v[64:67], v[160:163]\n"
"ds_read_b64_tr_b16 v[40:41], v112 offset:57856\n"
"v_mfma_f32_16x16x32_bf16 v[164:167], v[8:11], v[68:71], v[164:167]\n"
"ds_read_b64_tr_b16 v[42:43], v112 offset:58112\n"
"v_mfma_f32_16x16x32_bf16 v[168:171], v[8:11], v[72:75], v[168:171]\n"
"ds_read_b64_tr_b16 v[44:45], v113 offset:57856\n"
"v_mfma_f32_16x16x32_bf16 v[172:175], v[8:11], v[76:79], v[172:175]\n"
"s_add_u32 m0, s70, 20480\n"
"ds_read_b64_tr_b16 v[46:47], v113 offset:58112\n"
"v_mfma_f32_16x16x32_bf16 v[188:191], v[12:15], v[76:79], v[188:191]\n"
"s_add_u32 s75, s86, 65536\n"
"ds_read_b64_tr_b16 v[48:49], v112 offset:58368\n"
"buffer_load_dwordx4 v101, s[40:43], s75 offen offset:0 lds\n"
"v_mfma_f32_16x16x32_bf16 v[184:187], v[12:15], v[72:75], v[184:187]\n"
"ds_read_b64_tr_b16 v[50:51], v112 offset:58624\n"
"v_mfma_f32_16x16x32_bf16 v[180:183], v[12:15], v[68:71], v[180:183]\n"
"ds_read_b64_tr_b16 v[52:53], v113 offset:58368\n"
"v_mfma_f32_16x16x32_bf16 v[176:179], v[12:15], v[64:67], v[176:179]\n"
"ds_read_b64_tr_b16 v[54:55], v113 offset:58624\n"
"v_mfma_f32_16x16x32_bf16 v[192:195], v[16:19], v[64:67], v[192:195]\n"
"ds_read_b64_tr_b16 v[56:57], v112 offset:58880\n"
"v_mfma_f32_16x16x32_bf16 v[196:199], v[16:19], v[68:71], v[196:199]\n"
"s_add_u32 s75, s86, 80896\n"
"ds_read_b64_tr_b16 v[58:59], v112 offset:59136\n"
"buffer_load_dwordx4 v102, s[40:43], s75 offen offset:1024 lds\n"
"v_mfma_f32_16x16x32_bf16 v[200:203], v[16:19], v[72:75], v[200:203]\n"
"ds_read_b64_tr_b16 v[60:61], v113 offset:58880\n"
"v_mfma_f32_16x16x32_bf16 v[204:207], v[16:19], v[76:79], v[204:207]\n"
"ds_read_b64_tr_b16 v[62:63], v113 offset:59136\n"
"v_mfma_f32_16x16x32_bf16 v[220:223], v[20:23], v[76:79], v[220:223]\n"
"v_mfma_f32_16x16x32_bf16 v[216:219], v[20:23], v[72:75], v[216:219]\n"
"v_mfma_f32_16x16x32_bf16 v[212:215], v[20:23], v[68:71], v[212:215]\n"
"s_add_u32 s75, s86, 96256\n"
"buffer_load_dwordx4 v101, s[40:43], s75 offen offset:2048 lds\n"
"v_mfma_f32_16x16x32_bf16 v[208:211], v[20:23], v[64:67], v[208:211]\n"
"v_mfma_f32_16x16x32_bf16 v[224:227], v[24:27], v[64:67], v[224:227]\n"
"v_mfma_f32_16x16x32_bf16 v[228:231], v[24:27], v[68:71], v[228:231]\n"
"v_mfma_f32_16x16x32_bf16 v[232:235], v[24:27], v[72:75], v[232:235]\n"
"v_mfma_f32_16x16x32_bf16 v[236:239], v[24:27], v[76:79], v[236:239]\n"
"s_add_u32 s75, s86, 111616\n"
"buffer_load_dwordx4 v102, s[40:43], s75 offen offset:3072 lds\n"
"v_mfma_f32_16x16x32_bf16 v[252:255], v[28:31], v[76:79], v[252:255]\n"
"s_bfe_u32 s85, s79, 0x4000c\n"
"v_mfma_f32_16x16x32_bf16 v[248:251], v[28:31], v[72:75], v[248:251]\n"
"s_lshl_b32 s86, s85, 7\n"
"v_mfma_f32_16x16x32_bf16 v[244:247], v[28:31], v[68:71], v[244:247]\n"
"s_lshl_b32 s87, s85, 19\n"
"v_mfma_f32_16x16x32_bf16 v[240:243], v[28:31], v[64:67], v[240:243]\n"
"s_waitcnt vmcnt(12) lgkmcnt(0)\n"
"s_barrier\n"
"v_mfma_f32_16x16x32_bf16 v[128:131], v[32:35], v[80:83], v[128:131]\n"
"v_mfma_f32_16x16x32_bf16 v[132:135], v[32:35], v[84:87], v[132:135]\n"
"ds_read_b128 v[64:67], v98 offset:0\n"
"v_mfma_f32_16x16x32_bf16 v[136:139], v[32:35], v[88:91], v[136:139]\n"
"s_add_u32 m0, s69, 49152\n"
"ds_read_b128 v[68:71], v98 offset:2048\n"
"v_mfma_f32_16x16x32_bf16 v[140:143], v[32:35], v[92:95], v[140:143]\n"
"s_add_u32 s75, s87, 1024\n"
"ds_read_b128 v[72:75], v98 offset:4096\n"
"buffer_load_dwordx4 v104, s[44:47], s75 offen offset:0 lds\n"
"v_mfma_f32_16x16x32_bf16 v[156:159], v[36:39], v[92:95], v[156:159]\n"
"ds_read_b128 v[76:79], v98 offset:6144\n"
"v_mfma_f32_16x16x32_bf16 v[152:155], v[36:39], v[88:91], v[152:155]\n"
"ds_read_b64_tr_b16 v[0:1], v105 offset:0\n"
"v_mfma_f32_16x16x32_bf16 v[148:151], v[36:39], v[84:87], v[148:151]\n"
"ds_read_b64_tr_b16 v[2:3], v105 offset:256\n"
"v_mfma_f32_16x16x32_bf16 v[144:147], v[36:39], v[80:83], v[144:147]\n"
"ds_read_b64_tr_b16 v[4:5], v106 offset:0\n"
"v_mfma_f32_16x16x32_bf16 v[160:163], v[40:43], v[80:83], v[160:163]\n"
"s_add_u32 s75, s87, 128\n"
"ds_read_b64_tr_b16 v[6:7], v106 offset:256\n"
"buffer_load_dwordx4 v104, s[44:47], s75 offen offset:1024 lds\n"
"v_mfma_f32_16x16x32_bf16 v[164:167], v[40:43], v[84:87], v[164:167]\n"
"ds_read_b64_tr_b16 v[8:9], v105 offset:512\n"
"v_mfma_f32_16x16x32_bf16 v[168:171], v[40:43], v[88:91], v[168:171]\n"
"ds_read_b64_tr_b16 v[10:11], v105 offset:768\n"
"v_mfma_f32_16x16x32_bf16 v[172:175], v[40:43], v[92:95], v[172:175]\n"
"ds_read_b64_tr_b16 v[12:13], v106 offset:512\n"
"v_mfma_f32_16x16x32_bf16 v[188:191], v[44:47], v[92:95], v[188:191]\n"
"s_add_u32 m0, s70, 114688\n"
"ds_read_b64_tr_b16 v[14:15], v106 offset:768\n"
"v_mfma_f32_16x16x32_bf16 v[184:187], v[44:47], v[88:91], v[184:187]\n"
"s_add_u32 s75, s86, 0\n"
"ds_read_b64_tr_b16 v[16:17], v105 offset:1024\n"
"buffer_load_dwordx4 v101, s[40:43], s75 offen offset:0 lds\n"
"v_mfma_f32_16x16x32_bf16 v[180:183], v[44:47], v[84:87], v[180:183]\n"
"ds_read_b64_tr_b16 v[18:19], v105 offset:1280\n"
"v_mfma_f32_16x16x32_bf16 v[176:179], v[44:47], v[80:83], v[176:179]\n"
"ds_read_b64_tr_b16 v[20:21], v106 offset:1024\n"
"v_mfma_f32_16x16x32_bf16 v[192:195], v[48:51], v[80:83], v[192:195]\n"
"ds_read_b64_tr_b16 v[22:23], v106 offset:1280\n"
"v_mfma_f32_16x16x32_bf16 v[196:199], v[48:51], v[84:87], v[196:199]\n"
"ds_read_b64_tr_b16 v[24:25], v105 offset:1536\n"
"v_mfma_f32_16x16x32_bf16 v[200:203], v[48:51], v[88:91], v[200:203]\n"
"s_add_u32 s75, s86, 15360\n"
"ds_read_b64_tr_b16 v[26:27], v105 offset:1792\n"
"buffer_load_dwordx4 v102, s[40:43], s75 offen offset:1024 lds\n"
"v_mfma_f32_16x16x32_bf16 v[204:207], v[48:51], v[92:95], v[204:207]\n"
"ds_read_b64_tr_b16 v[28:29], v106 offset:1536\n"
"v_mfma_f32_16x16x32_bf16 v[220:223], v[52:55], v[92:95], v[220:223]\n"
"ds_read_b64_tr_b16 v[30:31], v106 offset:1792\n"
"v_mfma_f32_16x16x32_bf16 v[216:219], v[52:55], v[88:91], v[216:219]\n"
"v_mfma_f32_16x16x32_bf16 v[212:215], v[52:55], v[84:87], v[212:215]\n"
"v_mfma_f32_16x16x32_bf16 v[208:211], v[52:55], v[80:83], v[208:211]\n"
"s_add_u32 s75, s86, 30720\n"
"buffer_load_dwordx4 v101, s[40:43], s75 offen offset:2048 lds\n"
"v_mfma_f32_16x16x32_bf16 v[224:227], v[56:59], v[80:83], v[224:227]\n"
"v_mfma_f32_16x16x32_bf16 v[228:231], v[56:59], v[84:87], v[228:231]\n"
"v_mfma_f32_16x16x32_bf16 v[232:235], v[56:59], v[88:91], v[232:235]\n"
"v_mfma_f32_16x16x32_bf16 v[236:239], v[56:59], v[92:95], v[236:239]\n"
"v_mfma_f32_16x16x32_bf16 v[252:255], v[60:63], v[92:95], v[252:255]\n"
"s_add_u32 s75, s86, 46080\n"
"buffer_load_dwordx4 v102, s[40:43], s75 offen offset:3072 lds\n"
"v_mfma_f32_16x16x32_bf16 v[248:251], v[60:63], v[88:91], v[248:251]\n"
"v_mfma_f32_16x16x32_bf16 v[244:247], v[60:63], v[84:87], v[244:247]\n"
"v_mfma_f32_16x16x32_bf16 v[240:243], v[60:63], v[80:83], v[240:243]\n"
"s_waitcnt lgkmcnt(0)\n"
"v_mfma_f32_16x16x32_bf16 v[128:131], v[0:3], v[64:67], v[128:131]\n"
"ds_read_b128 v[80:83], v100 offset:0\n"
"v_mfma_f32_16x16x32_bf16 v[132:135], v[0:3], v[68:71], v[132:135]\n"
"s_add_u32 m0, s69, 57344\n"
"ds_read_b128 v[84:87], v100 offset:2048\n"
"v_mfma_f32_16x16x32_bf16 v[136:139], v[0:3], v[72:75], v[136:139]\n"
"s_add_u32 s75, s87, 263168\n"
"ds_read_b128 v[88:91], v100 offset:4096\n"
"buffer_load_dwordx4 v104, s[44:47], s75 offen offset:0 lds\n"
"v_mfma_f32_16x16x32_bf16 v[140:143], v[0:3], v[76:79], v[140:143]\n"
"ds_read_b128 v[92:95], v100 offset:6144\n"
"v_mfma_f32_16x16x32_bf16 v[156:159], v[4:7], v[76:79], v[156:159]\n"
"ds_read_b64_tr_b16 v[32:33], v105 offset:8192\n"
"v_mfma_f32_16x16x32_bf16 v[152:155], v[4:7], v[72:75], v[152:155]\n"
"ds_read_b64_tr_b16 v[34:35], v105 offset:8448\n"
"v_mfma_f32_16x16x32_bf16 v[148:151], v[4:7], v[68:71], v[148:151]\n"
"ds_read_b64_tr_b16 v[36:37], v106 offset:8192\n"
"v_mfma_f32_16x16x32_bf16 v[144:147], v[4:7], v[64:67], v[144:147]\n"
"s_add_u32 s75, s87, 262272\n"
"ds_read_b64_tr_b16 v[38:39], v106 offset:8448\n"
"buffer_load_dwordx4 v104, s[44:47], s75 offen offset:1024 lds\n"
"v_mfma_f32_16x16x32_bf16 v[160:163], v[8:11], v[64:67], v[160:163]\n"
"ds_read_b64_tr_b16 v[40:41], v105 offset:8704\n"
"v_mfma_f32_16x16x32_bf16 v[164:167], v[8:11], v[68:71], v[164:167]\n"
"ds_read_b64_tr_b16 v[42:43], v105 offset:8960\n"
"v_mfma_f32_16x16x32_bf16 v[168:171], v[8:11], v[72:75], v[168:171]\n"
"ds_read_b64_tr_b16 v[44:45], v106 offset:8704\n"
"v_mfma_f32_16x16x32_bf16 v[172:175], v[8:11], v[76:79], v[172:175]\n"
"s_add_u32 m0, s70, 118784\n"
"ds_read_b64_tr_b16 v[46:47], v106 offset:8960\n"
"v_mfma_f32_16x16x32_bf16 v[188:191], v[12:15], v[76:79], v[188:191]\n"
"s_add_u32 s75, s86, 65536\n"
"ds_read_b64_tr_b16 v[48:49], v105 offset:9216\n"
"buffer_load_dwordx4 v101, s[40:43], s75 offen offset:0 lds\n"
"v_mfma_f32_16x16x32_bf16 v[184:187], v[12:15], v[72:75], v[184:187]\n"
"ds_read_b64_tr_b16 v[50:51], v105 offset:9472\n"
"v_mfma_f32_16x16x32_bf16 v[180:183], v[12:15], v[68:71], v[180:183]\n"
"ds_read_b64_tr_b16 v[52:53], v106 offset:9216\n"
"v_mfma_f32_16x16x32_bf16 v[176:179], v[12:15], v[64:67], v[176:179]\n"
"ds_read_b64_tr_b16 v[54:55], v106 offset:9472\n"
"v_mfma_f32_16x16x32_bf16 v[192:195], v[16:19], v[64:67], v[192:195]\n"
"ds_read_b64_tr_b16 v[56:57], v105 offset:9728\n"
"v_mfma_f32_16x16x32_bf16 v[196:199], v[16:19], v[68:71], v[196:199]\n"
"s_add_u32 s75, s86, 80896\n"
"ds_read_b64_tr_b16 v[58:59], v105 offset:9984\n"
"buffer_load_dwordx4 v102, s[40:43], s75 offen offset:1024 lds\n"
"v_mfma_f32_16x16x32_bf16 v[200:203], v[16:19], v[72:75], v[200:203]\n"
"ds_read_b64_tr_b16 v[60:61], v106 offset:9728\n"
"v_mfma_f32_16x16x32_bf16 v[204:207], v[16:19], v[76:79], v[204:207]\n"
"ds_read_b64_tr_b16 v[62:63], v106 offset:9984\n"
"v_mfma_f32_16x16x32_bf16 v[220:223], v[20:23], v[76:79], v[220:223]\n"
"v_mfma_f32_16x16x32_bf16 v[216:219], v[20:23], v[72:75], v[216:219]\n"
"v_mfma_f32_16x16x32_bf16 v[212:215], v[20:23], v[68:71], v[212:215]\n"
"s_add_u32 s75, s86, 96256\n"
"buffer_load_dwordx4 v101, s[40:43], s75 offen offset:2048 lds\n"
"v_mfma_f32_16x16x32_bf16 v[208:211], v[20:23], v[64:67], v[208:211]\n"
"v_mfma_f32_16x16x32_bf16 v[224:227], v[24:27], v[64:67], v[224:227]\n"
"v_mfma_f32_16x16x32_bf16 v[228:231], v[24:27], v[68:71], v[228:231]\n"
"v_mfma_f32_16x16x32_bf16 v[232:235], v[24:27], v[72:75], v[232:235]\n"
"v_mfma_f32_16x16x32_bf16 v[236:239], v[24:27], v[76:79], v[236:239]\n"
"s_add_u32 s75, s86, 111616\n"
"buffer_load_dwordx4 v102, s[40:43], s75 offen offset:3072 lds\n"
"v_mfma_f32_16x16x32_bf16 v[252:255], v[28:31], v[76:79], v[252:255]\n"
"s_bfe_u32 s85, s79, 0x40010\n"
"v_mfma_f32_16x16x32_bf16 v[248:251], v[28:31], v[72:75], v[248:251]\n"
"s_lshl_b32 s86, s85, 7\n"
"v_mfma_f32_16x16x32_bf16 v[244:247], v[28:31], v[68:71], v[244:247]\n"
"s_lshl_b32 s87, s85, 19\n"
"v_mfma_f32_16x16x32_bf16 v[240:243], v[28:31], v[64:67], v[240:243]\n"
"s_waitcnt vmcnt(12) lgkmcnt(0)\n"
"s_barrier\n"
"v_mfma_f32_16x16x32_bf16 v[128:131], v[32:35], v[80:83], v[128:131]\n"
"v_lshlrev_b32 v109, 5, v108\n"
"v_mfma_f32_16x16x32_bf16 v[132:135], v[32:35], v[84:87], v[132:135]\n"
"ds_read_b128 v[64:67], v96 offset:0\n"
"buffer_load_dwordx4 a[176:179], v109, s[60:63], 0 offen offset:0 sc1\n"
"v_mfma_f32_16x16x32_bf16 v[136:139], v[32:35], v[88:91], v[136:139]\n"
"s_add_u32 m0, s69, 65536\n"
"ds_read_b128 v[68:71], v96 offset:2048\n"
"buffer_load_dwordx4 a[180:183], v109, s[60:63], 0 offen offset:16 sc1\n"
"v_mfma_f32_16x16x32_bf16 v[140:143], v[32:35], v[92:95], v[140:143]\n"
"s_add_u32 s75, s87, 1024\n"
"ds_read_b128 v[72:75], v96 offset:4096\n"
"buffer_load_dwordx4 v104, s[44:47], s75 offen offset:0 lds\n"
"v_mfma_f32_16x16x32_bf16 v[156:159], v[36:39], v[92:95], v[156:159]\n"
"s_lshr_b32 s73, s93, 3\n"
"s_mul_i32 s73, s73, 640\n"
"ds_read_b128 v[76:79], v96 offset:6144\n"
"v_mfma_f32_16x16x32_bf16 v[152:155], v[36:39], v[88:91], v[152:155]\n"
"s_add_u32 s73, s73, 7168\n"
"ds_read_b64_tr_b16 v[0:1], v112 offset:0\n"
"v_lshrrev_b32 v109, 4, v124\n"
"v_mfma_f32_16x16x32_bf16 v[148:151], v[36:39], v[84:87], v[148:151]\n"
"ds_read_b64_tr_b16 v[2:3], v112 offset:256\n"
"v_min_u32 v109, 4, v109\n"
"v_mfma_f32_16x16x32_bf16 v[144:147], v[36:39], v[80:83], v[144:147]\n"
"ds_read_b64_tr_b16 v[4:5], v113 offset:0\n"
"v_lshlrev_b32 v109, 7, v109\n"
"v_mfma_f32_16x16x32_bf16 v[160:163], v[40:43], v[80:83], v[160:163]\n"
"s_add_u32 s75, s87, 128\n"
"ds_read_b64_tr_b16 v[6:7], v113 offset:256\n"
"buffer_load_dwordx4 v104, s[44:47], s75 offen offset:1024 lds\n"
"v_mfma_f32_16x16x32_bf16 v[164:167], v[40:43], v[84:87], v[164:167]\n"
"ds_read_b64_tr_b16 v[8:9], v112 offset:512\n"
"v_add_u32 v109, s73, v109\n"
"v_mfma_f32_16x16x32_bf16 v[168:171], v[40:43], v[88:91], v[168:171]\n"
"s_getpc_b64 s[88:89]\n"
"ds_read_b64_tr_b16 v[10:11], v112 offset:768\n"
"global_load_dword a252, v109, s[88:89]\n"
"v_mfma_f32_16x16x32_bf16 v[172:175], v[40:43], v[92:95], v[172:175]\n"
"ds_read_b64_tr_b16 v[12:13], v113 offset:512\n"
"v_mfma_f32_16x16x32_bf16 v[188:191], v[44:47], v[92:95], v[188:191]\n"
"s_add_u32 m0, s70, 81920\n"
"ds_read_b64_tr_b16 v[14:15], v113 offset:768\n"
"v_mfma_f32_16x16x32_bf16 v[184:187], v[44:47], v[88:91], v[184:187]\n"
"s_add_u32 s75, s86, 0\n"
"ds_read_b64_tr_b16 v[16:17], v112 offset:1024\n"
"buffer_load_dwordx4 v101, s[40:43], s75 offen offset:0 lds\n"
"v_mfma_f32_16x16x32_bf16 v[180:183], v[44:47], v[84:87], v[180:183]\n"
"ds_read_b64_tr_b16 v[18:19], v112 offset:1280\n"
"v_mfma_f32_16x16x32_bf16 v[176:179], v[44:47], v[80:83], v[176:179]\n"
"ds_read_b64_tr_b16 v[20:21], v113 offset:1024\n"
"v_mfma_f32_16x16x32_bf16 v[192:195], v[48:51], v[80:83], v[192:195]\n"
"ds_read_b64_tr_b16 v[22:23], v113 offset:1280\n"
"v_mfma_f32_16x16x32_bf16 v[196:199], v[48:51], v[84:87], v[196:199]\n"
"ds_read_b64_tr_b16 v[24:25], v112 offset:1536\n"
"v_mfma_f32_16x16x32_bf16 v[200:203], v[48:51], v[88:91], v[200:203]\n"
"s_add_u32 s75, s86, 15360\n"
"ds_read_b64_tr_b16 v[26:27], v112 offset:1792\n"
"buffer_load_dwordx4 v102, s[40:43], s75 offen offset:1024 lds\n"
"v_mfma_f32_16x16x32_bf16 v[204:207], v[48:51], v[92:95], v[204:207]\n"
"ds_read_b64_tr_b16 v[28:29], v113 offset:1536\n"
"v_mfma_f32_16x16x32_bf16 v[220:223], v[52:55], v[92:95], v[220:223]\n"
"ds_read_b64_tr_b16 v[30:31], v113 offset:1792\n"
"v_mfma_f32_16x16x32_bf16 v[216:219], v[52:55], v[88:91], v[216:219]\n"
"v_mfma_f32_16x16x32_bf16 v[212:215], v[52:55], v[84:87], v[212:215]\n"
"v_mfma_f32_16x16x32_bf16 v[208:211], v[52:55], v[80:83], v[208:211]\n"
"s_add_u32 s75, s86, 30720\n"
"buffer_load_dwordx4 v101, s[40:43], s75 offen offset:2048 lds\n"
"v_mfma_f32_16x16x32_bf16 v[224:227], v[56:59], v[80:83], v[224:227]\n"
"v_mfma_f32_16x16x32_bf16 v[228:231], v[56:59], v[84:87], v[228:231]\n"
"v_mfma_f32_16x16x32_bf16 v[232:235], v[56:59], v[88:91], v[232:235]\n"
"v_mfma_f32_16x16x32_bf16 v[236:239], v[56:59], v[92:95], v[236:239]\n"
"v_mfma_f32_16x16x32_bf16 v[252:255], v[60:63], v[92:95], v[252:255]\n"
"s_add_u32 s75, s86, 46080\n"
"buffer_load_dwordx4 v102, s[40:43], s75 offen offset:3072 lds\n"
"v_mfma_f32_16x16x32_bf16 v[248:251], v[60:63], v[88:91], v[248:251]\n"
"v_mfma_f32_16x16x32_bf16 v[244:247], v[60:63], v[84:87], v[244:247]\n"
"v_mfma_f32_16x16x32_bf16 v[240:243], v[60:63], v[80:83], v[240:243]\n"
"s_waitcnt lgkmcnt(0)\n"
"v_mfma_f32_16x16x32_bf16 v[128:131], v[0:3], v[64:67], v[128:131]\n"
"ds_read_b128 v[80:83], v97 offset:0\n"
"v_mfma_f32_16x16x32_bf16 v[132:135], v[0:3], v[68:71], v[132:135]\n"
"s_add_u32 m0, s69, 73728\n"
"ds_read_b128 v[84:87], v97 offset:2048\n"
"v_mfma_f32_16x16x32_bf16 v[136:139], v[0:3], v[72:75], v[136:139]\n"
"s_add_u32 s75, s87, 263168\n"
"ds_read_b128 v[88:91], v97 offset:4096\n"
"buffer_load_dwordx4 v104, s[44:47], s75 offen offset:0 lds\n"
"v_mfma_f32_16x16x32_bf16 v[140:143], v[0:3], v[76:79], v[140:143]\n"
"ds_read_b128 v[92:95], v97 offset:6144\n"
"v_mfma_f32_16x16x32_bf16 v[156:159], v[4:7], v[76:79], v[156:159]\n"
"ds_read_b64_tr_b16 v[32:33], v112 offset:8192\n"
"v_mfma_f32_16x16x32_bf16 v[152:155], v[4:7], v[72:75], v[152:155]\n"
"ds_read_b64_tr_b16 v[34:35], v112 offset:8448\n"
"v_mfma_f32_16x16x32_bf16 v[148:151], v[4:7], v[68:71], v[148:151]\n"
"ds_read_b64_tr_b16 v[36:37], v113 offset:8192\n"
"v_mfma_f32_16x16x32_bf16 v[144:147], v[4:7], v[64:67], v[144:147]\n"
"s_add_u32 s75, s87, 262272\n"
"ds_read_b64_tr_b16 v[38:39], v113 offset:8448\n"
"buffer_load_dwordx4 v104, s[44:47], s75 offen offset:1024 lds\n"
"v_mfma_f32_16x16x32_bf16 v[160:163], v[8:11], v[64:67], v[160:163]\n"
"ds_read_b64_tr_b16 v[40:41], v112 offset:8704\n"
"v_mfma_f32_16x16x32_bf16 v[164:167], v[8:11], v[68:71], v[164:167]\n"
"ds_read_b64_tr_b16 v[42:43], v112 offset:8960\n"
"v_mfma_f32_16x16x32_bf16 v[168:171], v[8:11], v[72:75], v[168:171]\n"
"ds_read_b64_tr_b16 v[44:45], v113 offset:8704\n"
"v_mfma_f32_16x16x32_bf16 v[172:175], v[8:11], v[76:79], v[172:175]\n"
"s_add_u32 m0, s70, 86016\n"
"ds_read_b64_tr_b16 v[46:47], v113 offset:8960\n"
"v_mfma_f32_16x16x32_bf16 v[188:191], v[12:15], v[76:79], v[188:191]\n"
"s_add_u32 s75, s86, 65536\n"
"ds_read_b64_tr_b16 v[48:49], v112 offset:9216\n"
"buffer_load_dwordx4 v101, s[40:43], s75 offen offset:0 lds\n"
"v_mfma_f32_16x16x32_bf16 v[184:187], v[12:15], v[72:75], v[184:187]\n"
"ds_read_b64_tr_b16 v[50:51], v112 offset:9472\n"
"v_mfma_f32_16x16x32_bf16 v[180:183], v[12:15], v[68:71], v[180:183]\n"
"ds_read_b64_tr_b16 v[52:53], v113 offset:9216\n"
"v_mfma_f32_16x16x32_bf16 v[176:179], v[12:15], v[64:67], v[176:179]\n"
"ds_read_b64_tr_b16 v[54:55], v113 offset:9472\n"
"v_mfma_f32_16x16x32_bf16 v[192:195], v[16:19], v[64:67], v[192:195]\n"
"ds_read_b64_tr_b16 v[56:57], v112 offset:9728\n"
"v_mfma_f32_16x16x32_bf16 v[196:199], v[16:19], v[68:71], v[196:199]\n"
"s_add_u32 s75, s86, 80896\n"
"ds_read_b64_tr_b16 v[58:59], v112 offset:9984\n"
"buffer_load_dwordx4 v102, s[40:43], s75 offen offset:1024 lds\n"
"v_mfma_f32_16x16x32_bf16 v[200:203], v[16:19], v[72:75], v[200:203]\n"
"ds_read_b64_tr_b16 v[60:61], v113 offset:9728\n"
"v_mfma_f32_16x16x32_bf16 v[204:207], v[16:19], v[76:79], v[204:207]\n"
"ds_read_b64_tr_b16 v[62:63], v113 offset:9984\n"
"v_mfma_f32_16x16x32_bf16 v[220:223], v[20:23], v[76:79], v[220:223]\n"
"v_mfma_f32_16x16x32_bf16 v[216:219], v[20:23], v[72:75], v[216:219]\n"
"v_mfma_f32_16x16x32_bf16 v[212:215], v[20:23], v[68:71], v[212:215]\n"
"s_add_u32 s75, s86, 96256\n"
"buffer_load_dwordx4 v101, s[40:43], s75 offen offset:2048 lds\n"
"v_mfma_f32_16x16x32_bf16 v[208:211], v[20:23], v[64:67], v[208:211]\n"
"v_mfma_f32_16x16x32_bf16 v[224:227], v[24:27], v[64:67], v[224:227]\n"
"v_mfma_f32_16x16x32_bf16 v[228:231], v[24:27], v[68:71], v[228:231]\n"
"v_mfma_f32_16x16x32_bf16 v[232:235], v[24:27], v[72:75], v[232:235]\n"
"v_mfma_f32_16x16x32_bf16 v[236:239], v[24:27], v[76:79], v[236:239]\n"
"s_add_u32 s75, s86, 111616\n"
"buffer_load_dwordx4 v102, s[40:43], s75 offen offset:3072 lds\n"
"v_mfma_f32_16x16x32_bf16 v[252:255], v[28:31], v[76:79], v[252:255]\n"
"s_bfe_u32 s85, s79, 0x40014\n"
"v_mfma_f32_16x16x32_bf16 v[248:251], v[28:31], v[72:75], v[248:251]\n"
"s_lshl_b32 s86, s85, 7\n"
"v_mfma_f32_16x16x32_bf16 v[244:247], v[28:31], v[68:71], v[244:247]\n"
"s_lshl_b32 s87, s85, 19\n"
"v_mfma_f32_16x16x32_bf16 v[240:243], v[28:31], v[64:67], v[240:243]\n"
"s_waitcnt vmcnt(15) lgkmcnt(0)\n"
"s_barrier\n"
"v_mfma_f32_16x16x32_bf16 v[128:131], v[32:35], v[80:83], v[128:131]\n"
"v_mfma_f32_16x16x32_bf16 v[132:135], v[32:35], v[84:87], v[132:135]\n"
"ds_read_b128 v[64:67], v98 offset:32768\n"
"v_mfma_f32_16x16x32_bf16 v[136:139], v[32:35], v[88:91], v[136:139]\n"
"s_add_u32 m0, s69, 0\n"
"ds_read_b128 v[68:71], v98 offset:34816\n"
"v_mfma_f32_16x16x32_bf16 v[140:143], v[32:35], v[92:95], v[140:143]\n"
"s_add_u32 s75, s87, 1024\n"
"ds_read_b128 v[72:75], v98 offset:36864\n"
"buffer_load_dwordx4 v104, s[44:47], s75 offen offset:0 lds\n"
"v_mfma_f32_16x16x32_bf16 v[156:159], v[36:39], v[92:95], v[156:159]\n"
"ds_read_b128 v[76:79], v98 offset:38912\n"
"v_mfma_f32_16x16x32_bf16 v[152:155], v[36:39], v[88:91], v[152:155]\n"
"ds_read_b64_tr_b16 v[0:1], v112 offset:49152\n"
"v_mfma_f32_16x16x32_bf16 v[148:151], v[36:39], v[84:87], v[148:151]\n"
"ds_read_b64_tr_b16 v[2:3], v112 offset:49408\n"
"v_mfma_f32_16x16x32_bf16 v[144:147], v[36:39], v[80:83], v[144:147]\n"
"ds_read_b64_tr_b16 v[4:5], v113 offset:49152\n"
"v_mfma_f32_16x16x32_bf16 v[160:163], v[40:43], v[80:83], v[160:163]\n"
"s_add_u32 s75, s87, 128\n"
"ds_read_b64_tr_b16 v[6:7], v113 offset:49408\n"
"buffer_load_dwordx4 v104, s[44:47], s75 offen offset:1024 lds\n"
"v_mfma_f32_16x16x32_bf16 v[164:167], v[40:43], v[84:87], v[164:167]\n"
"ds_read_b64_tr_b16 v[8:9], v112 offset:49664\n"
"v_mfma_f32_16x16x32_bf16 v[168:171], v[40:43], v[88:91], v[168:171]\n"
"ds_read_b64_tr_b16 v[10:11], v112 offset:49920\n"
"v_mfma_f32_16x16x32_bf16 v[172:175], v[40:43], v[92:95], v[172:175]\n"
"ds_read_b64_tr_b16 v[12:13], v113 offset:49664\n"
"v_mfma_f32_16x16x32_bf16 v[188:191], v[44:47], v[92:95], v[188:191]\n"
"s_add_u32 m0, s70, 16384\n"
"ds_read_b64_tr_b16 v[14:15], v113 offset:49920\n"
"v_mfma_f32_16x16x32_bf16 v[184:187], v[44:47], v[88:91], v[184:187]\n"
"s_add_u32 s75, s86, 0\n"
"ds_read_b64_tr_b16 v[16:17], v112 offset:50176\n"
"buffer_load_dwordx4 v101, s[40:43], s75 offen offset:0 lds\n"
"v_mfma_f32_16x16x32_bf16 v[180:183], v[44:47], v[84:87], v[180:183]\n"
"ds_read_b64_tr_b16 v[18:19], v112 offset:50432\n"
"v_mfma_f32_16x16x32_bf16 v[176:179], v[44:47], v[80:83], v[176:179]\n"
"ds_read_b64_tr_b16 v[20:21], v113 offset:50176\n"
"v_mfma_f32_16x16x32_bf16 v[192:195], v[48:51], v[80:83], v[192:195]\n"
"ds_read_b64_tr_b16 v[22:23], v113 offset:50432\n"
"v_mfma_f32_16x16x32_bf16 v[196:199], v[48:51], v[84:87], v[196:199]\n"
"ds_read_b64_tr_b16 v[24:25], v112 offset:50688\n"
"v_mfma_f32_16x16x32_bf16 v[200:203], v[48:51], v[88:91], v[200:203]\n"
"s_add_u32 s75, s86, 15360\n"
"ds_read_b64_tr_b16 v[26:27], v112 offset:50944\n"
"buffer_load_dwordx4 v102, s[40:43], s75 offen offset:1024 lds\n"
"v_mfma_f32_16x16x32_bf16 v[204:207], v[48:51], v[92:95], v[204:207]\n"
"ds_read_b64_tr_b16 v[28:29], v113 offset:50688\n"
"v_mfma_f32_16x16x32_bf16 v[220:223], v[52:55], v[92:95], v[220:223]\n"
"ds_read_b64_tr_b16 v[30:31], v113 offset:50944\n"
"v_mfma_f32_16x16x32_bf16 v[216:219], v[52:55], v[88:91], v[216:219]\n"
"v_mfma_f32_16x16x32_bf16 v[212:215], v[52:55], v[84:87], v[212:215]\n"
"v_mfma_f32_16x16x32_bf16 v[208:211], v[52:55], v[80:83], v[208:211]\n"
"s_add_u32 s75, s86, 30720\n"
"buffer_load_dwordx4 v101, s[40:43], s75 offen offset:2048 lds\n"
"v_mfma_f32_16x16x32_bf16 v[224:227], v[56:59], v[80:83], v[224:227]\n"
"v_mfma_f32_16x16x32_bf16 v[228:231], v[56:59], v[84:87], v[228:231]\n"
"v_mfma_f32_16x16x32_bf16 v[232:235], v[56:59], v[88:91], v[232:235]\n"
"v_mfma_f32_16x16x32_bf16 v[236:239], v[56:59], v[92:95], v[236:239]\n"
"v_mfma_f32_16x16x32_bf16 v[252:255], v[60:63], v[92:95], v[252:255]\n"
"s_add_u32 s75, s86, 46080\n"
"buffer_load_dwordx4 v102, s[40:43], s75 offen offset:3072 lds\n"
"v_mfma_f32_16x16x32_bf16 v[248:251], v[60:63], v[88:91], v[248:251]\n"
"v_mfma_f32_16x16x32_bf16 v[244:247], v[60:63], v[84:87], v[244:247]\n"
"v_mfma_f32_16x16x32_bf16 v[240:243], v[60:63], v[80:83], v[240:243]\n"
"s_waitcnt lgkmcnt(0)\n"
"v_mfma_f32_16x16x32_bf16 v[128:131], v[0:3], v[64:67], v[128:131]\n"
"ds_read_b128 v[80:83], v100 offset:32768\n"
"v_mfma_f32_16x16x32_bf16 v[132:135], v[0:3], v[68:71], v[132:135]\n"
"s_add_u32 m0, s69, 8192\n"
"ds_read_b128 v[84:87], v100 offset:34816\n"
"v_mfma_f32_16x16x32_bf16 v[136:139], v[0:3], v[72:75], v[136:139]\n"
"s_add_u32 s75, s87, 263168\n"
"ds_read_b128 v[88:91], v100 offset:36864\n"
"buffer_load_dwordx4 v104, s[44:47], s75 offen offset:0 lds\n"
"v_mfma_f32_16x16x32_bf16 v[140:143], v[0:3], v[76:79], v[140:143]\n"
"ds_read_b128 v[92:95], v100 offset:38912\n"
"v_mfma_f32_16x16x32_bf16 v[156:159], v[4:7], v[76:79], v[156:159]\n"
"ds_read_b64_tr_b16 v[32:33], v112 offset:57344\n"
"v_mfma_f32_16x16x32_bf16 v[152:155], v[4:7], v[72:75], v[152:155]\n"
"ds_read_b64_tr_b16 v[34:35], v112 offset:57600\n"
"v_mfma_f32_16x16x32_bf16 v[148:151], v[4:7], v[68:71], v[148:151]\n"
"ds_read_b64_tr_b16 v[36:37], v113 offset:57344\n"
"v_mfma_f32_16x16x32_bf16 v[144:147], v[4:7], v[64:67], v[144:147]\n"
"s_add_u32 s75, s87, 262272\n"
"ds_read_b64_tr_b16 v[38:39], v113 offset:57600\n"
"buffer_load_dwordx4 v104, s[44:47], s75 offen offset:1024 lds\n"
"v_mfma_f32_16x16x32_bf16 v[160:163], v[8:11], v[64:67], v[160:163]\n"
"ds_read_b64_tr_b16 v[40:41], v112 offset:57856\n"
"v_mfma_f32_16x16x32_bf16 v[164:167], v[8:11], v[68:71], v[164:167]\n"
"ds_read_b64_tr_b16 v[42:43], v112 offset:58112\n"
"v_mfma_f32_16x16x32_bf16 v[168:171], v[8:11], v[72:75], v[168:171]\n"
"ds_read_b64_tr_b16 v[44:45], v113 offset:57856\n"
"v_mfma_f32_16x16x32_bf16 v[172:175], v[8:11], v[76:79], v[172:175]\n"
"s_add_u32 m0, s70, 20480\n"
"ds_read_b64_tr_b16 v[46:47], v113 offset:58112\n"
"v_mfma_f32_16x16x32_bf16 v[188:191], v[12:15], v[76:79], v[188:191]\n"
"s_add_u32 s75, s86, 65536\n"
"ds_read_b64_tr_b16 v[48:49], v112 offset:58368\n"
"buffer_load_dwordx4 v101, s[40:43], s75 offen offset:0 lds\n"
"v_mfma_f32_16x16x32_bf16 v[184:187], v[12:15], v[72:75], v[184:187]\n"
"ds_read_b64_tr_b16 v[50:51], v112 offset:58624\n"
"v_mfma_f32_16x16x32_bf16 v[180:183], v[12:15], v[68:71], v[180:183]\n"
"ds_read_b64_tr_b16 v[52:53], v113 offset:58368\n"
"v_mfma_f32_16x16x32_bf16 v[176:179], v[12:15], v[64:67], v[176:179]\n"
"ds_read_b64_tr_b16 v[54:55], v113 offset:58624\n"
"v_mfma_f32_16x16x32_bf16 v[192:195], v[16:19], v[64:67], v[192:195]\n"
"ds_read_b64_tr_b16 v[56:57], v112 offset:58880\n"
"v_mfma_f32_16x16x32_bf16 v[196:199], v[16:19], v[68:71], v[196:199]\n"
"s_add_u32 s75, s86, 80896\n"
"ds_read_b64_tr_b16 v[58:59], v112 offset:59136\n"
"buffer_load_dwordx4 v102, s[40:43], s75 offen offset:1024 lds\n"
"v_mfma_f32_16x16x32_bf16 v[200:203], v[16:19], v[72:75], v[200:203]\n"
"ds_read_b64_tr_b16 v[60:61], v113 offset:58880\n"
"v_mfma_f32_16x16x32_bf16 v[204:207], v[16:19], v[76:79], v[204:207]\n"
"ds_read_b64_tr_b16 v[62:63], v113 offset:59136\n"
"v_mfma_f32_16x16x32_bf16 v[220:223], v[20:23], v[76:79], v[220:223]\n"
"v_mfma_f32_16x16x32_bf16 v[216:219], v[20:23], v[72:75], v[216:219]\n"
"v_mfma_f32_16x16x32_bf16 v[212:215], v[20:23], v[68:71], v[212:215]\n"
"s_add_u32 s75, s86, 96256\n"
"buffer_load_dwordx4 v101, s[40:43], s75 offen offset:2048 lds\n"
"v_mfma_f32_16x16x32_bf16 v[208:211], v[20:23], v[64:67], v[208:211]\n"
"v_mfma_f32_16x16x32_bf16 v[224:227], v[24:27], v[64:67], v[224:227]\n"
"v_mfma_f32_16x16x32_bf16 v[228:231], v[24:27], v[68:71], v[228:231]\n"
"v_mfma_f32_16x16x32_bf16 v[232:235], v[24:27], v[72:75], v[232:235]\n"
"v_mfma_f32_16x16x32_bf16 v[236:239], v[24:27], v[76:79], v[236:239]\n"
"s_add_u32 s75, s86, 111616\n"
"buffer_load_dwordx4 v102, s[40:43], s75 offen offset:3072 lds\n"
"v_mfma_f32_16x16x32_bf16 v[252:255], v[28:31], v[76:79], v[252:255]\n"
"s_bfe_u32 s85, s79, 0x40018\n"
"v_mfma_f32_16x16x32_bf16 v[248:251], v[28:31], v[72:75], v[248:251]\n"
"s_lshl_b32 s86, s85, 7\n"
"v_mfma_f32_16x16x32_bf16 v[244:247], v[28:31], v[68:71], v[244:247]\n"
"s_lshl_b32 s87, s85, 19\n"
"v_mfma_f32_16x16x32_bf16 v[240:243], v[28:31], v[64:67], v[240:243]\n"
"s_waitcnt vmcnt(12) lgkmcnt(0)\n"
"s_barrier\n"
"v_mfma_f32_16x16x32_bf16 v[128:131], v[32:35], v[80:83], v[128:131]\n"
"v_accvgpr_read_b32 v99, a177\n"
"v_accvgpr_read_b32 v103, a179\n"
"v_mfma_f32_16x16x32_bf16 v[132:135], v[32:35], v[84:87], v[132:135]\n"
"ds_read_b128 v[64:67], v98 offset:0\n"
"v_accvgpr_read_b32 v107, a181\n"
"v_mfma_f32_16x16x32_bf16 v[136:139], v[32:35], v[88:91], v[136:139]\n"
"s_add_u32 m0, s69, 49152\n"
"ds_read_b128 v[68:71], v98 offset:2048\n"
"v_accvgpr_read_b32 v111, a183\n"
"v_mfma_f32_16x16x32_bf16 v[140:143], v[32:35], v[92:95], v[140:143]\n"
"s_add_u32 s75, s87, 1024\n"
"ds_read_b128 v[72:75], v98 offset:4096\n"
"buffer_load_dwordx4 v104, s[44:47], s75 offen offset:0 lds\n"
"v_mfma_f32_16x16x32_bf16 v[156:159], v[36:39], v[92:95], v[156:159]\n"
"ds_read_b128 v[76:79], v98 offset:6144\n"
"v_xor_b32 v99, s76, v99\n"
"v_mfma_f32_16x16x32_bf16 v[152:155], v[36:39], v[88:91], v[152:155]\n"
"ds_read_b64_tr_b16 v[0:1], v105 offset:0\n"
"v_xor_b32 v103, s76, v103\n"
"v_mfma_f32_16x16x32_bf16 v[148:151], v[36:39], v[84:87], v[148:151]\n"
"ds_read_b64_tr_b16 v[2:3], v105 offset:256\n"
"v_xor_b32 v107, s76, v107\n"
"v_mfma_f32_16x16x32_bf16 v[144:147], v[36:39], v[80:83], v[144:147]\n"
"ds_read_b64_tr_b16 v[4:5], v106 offset:0\n"
"v_xor_b32 v111, s76, v111\n"
"v_mfma_f32_16x16x32_bf16 v[160:163], v[40:43], v[80:83], v[160:163]\n"
"s_add_u32 s75, s87, 128\n"
"ds_read_b64_tr_b16 v[6:7], v106 offset:256\n"
"buffer_load_dwordx4 v104, s[44:47], s75 offen offset:1024 lds\n"
"v_mfma_f32_16x16x32_bf16 v[164:167], v[40:43], v[84:87], v[164:167]\n"
"ds_read_b64_tr_b16 v[8:9], v105 offset:512\n"
"v_or3_b32 v99, v99, v103, v107\n"
"v_mfma_f32_16x16x32_bf16 v[168:171], v[40:43], v[88:91], v[168:171]\n"
"ds_read_b64_tr_b16 v[10:11], v105 offset:768\n"
"v_or_b32 v99, v99, v111\n"
"v_mfma_f32_16x16x32_bf16 v[172:175], v[40:43], v[92:95], v[172:175]\n"
"ds_read_b64_tr_b16 v[12:13], v106 offset:512\n"
"v_accvgpr_read_b32 v110, a176\n"
"v_mfma_f32_16x16x32_bf16 v[188:191], v[44:47], v[92:95], v[188:191]\n"
"s_add_u32 m0, s70, 114688\n"
"ds_read_b64_tr_b16 v[14:15], v106 offset:768\n"
"v_accvgpr_read_b32 v103, a178\n"
"v_mfma_f32_16x16x32_bf16 v[184:187], v[44:47], v[88:91], v[184:187]\n"
"s_add_u32 s75, s86, 0\n"
"ds_read_b64_tr_b16 v[16:17], v105 offset:1024\n"
"buffer_load_dwordx4 v101, s[40:43], s75 offen offset:0 lds\n"
"v_mfma_f32_16x16x32_bf16 v[180:183], v[44:47], v[84:87], v[180:183]\n"
"ds_read_b64_tr_b16 v[18:19], v105 offset:1280\n"
"v_accvgpr_read_b32 v107, a180\n"
"v_mfma_f32_16x16x32_bf16 v[176:179], v[44:47], v[80:83], v[176:179]\n"
"ds_read_b64_tr_b16 v[20:21], v106 offset:1024\n"
"v_accvgpr_read_b32 v111, a182\n"
"v_mfma_f32_16x16x32_bf16 v[192:195], v[48:51], v[80:83], v[192:195]\n"
"ds_read_b64_tr_b16 v[22:23], v106 offset:1280\n"
"v_max3_u32 v110, v110, v103, v107\n"
"v_mfma_f32_16x16x32_bf16 v[196:199], v[48:51], v[84:87], v[196:199]\n"
"ds_read_b64_tr_b16 v[24:25], v105 offset:1536\n"
"v_max_u32 v110, v110, v111\n"
"v_mfma_f32_16x16x32_bf16 v[200:203], v[48:51], v[88:91], v[200:203]\n"
"s_add_u32 s75, s86, 15360\n"
"ds_read_b64_tr_b16 v[26:27], v105 offset:1792\n"
"buffer_load_dwordx4 v102, s[40:43], s75 offen offset:1024 lds\n"
"v_mfma_f32_16x16x32_bf16 v[204:207], v[48:51], v[92:95], v[204:207]\n"
"ds_read_b64_tr_b16 v[28:29], v106 offset:1536\n"
"v_mfma_f32_16x16x32_bf16 v[220:223], v[52:55], v[92:95], v[220:223]\n"
"ds_read_b64_tr_b16 v[30:31], v106 offset:1792\n"
"v_mfma_f32_16x16x32_bf16 v[216:219], v[52:55], v[88:91], v[216:219]\n"
"v_mfma_f32_16x16x32_bf16 v[212:215], v[52:55], v[84:87], v[212:215]\n"
"v_mfma_f32_16x16x32_bf16 v[208:211], v[52:55], v[80:83], v[208:211]\n"
"s_add_u32 s75, s86, 30720\n"
"buffer_load_dwordx4 v101, s[40:43], s75 offen offset:2048 lds\n"
"v_mfma_f32_16x16x32_bf16 v[224:227], v[56:59], v[80:83], v[224:227]\n"
"v_mfma_f32_16x16x32_bf16 v[228:231], v[56:59], v[84:87], v[228:231]\n"
"v_mfma_f32_16x16x32_bf16 v[232:235], v[56:59], v[88:91], v[232:235]\n"
"v_mfma_f32_16x16x32_bf16 v[236:239], v[56:59], v[92:95], v[236:239]\n"
"v_mfma_f32_16x16x32_bf16 v[252:255], v[60:63], v[92:95], v[252:255]\n"
"s_add_u32 s75, s86, 46080\n"
"buffer_load_dwordx4 v102, s[40:43], s75 offen offset:3072 lds\n"
"v_mfma_f32_16x16x32_bf16 v[248:251], v[60:63], v[88:91], v[248:251]\n"
"v_mfma_f32_16x16x32_bf16 v[244:247], v[60:63], v[84:87], v[244:247]\n"
"v_mfma_f32_16x16x32_bf16 v[240:243], v[60:63], v[80:83], v[240:243]\n"
"s_waitcnt lgkmcnt(0)\n"
"v_cmp_ne_u32 vcc, 0, v99\n"
"s_nop 4\n"
"s_cbranch_vccnz LSPIN1_%=\n"
"LBACK1_%=:\n"
"v_mfma_f32_16x16x32_bf16 v[128:131], v[0:3], v[64:67], v[128:131]\n"
"ds_read_b128 v[80:83], v100 offset:0\n"
"v_max_u32_dpp v110, v110, v110 quad_perm:[1,0,3,2] row_mask:0xf bank_mask:0xf\n"
"v_mfma_f32_16x16x32_bf16 v[132:135], v[0:3], v[68:71], v[132:135]\n"
"s_add_u32 m0, s69, 57344\n"
"ds_read_b128 v[84:87], v100 offset:2048\n"
"v_max_u32_dpp v110, v110, v110 quad_perm:[2,3,0,1] row_mask:0xf bank_mask:0xf\n"
"v_mfma_f32_16x16x32_bf16 v[136:139], v[0:3], v[72:75], v[136:139]\n"
"s_add_u32 s75, s87, 263168\n"
"ds_read_b128 v[88:91], v100 offset:4096\n"
"buffer_load_dwordx4 v104, s[44:47], s75 offen offset:0 lds\n"
"v_mfma_f32_16x16x32_bf16 v[140:143], v[0:3], v[76:79], v[140:143]\n"
"ds_read_b128 v[92:95], v100 offset:6144\n"
"v_max_u32_dpp v110, v110, v110 row_half_mirror row_mask:0xf bank_mask:0xf\n"
"v_mfma_f32_16x16x32_bf16 v[156:159], v[4:7], v[76:79], v[156:159]\n"
"ds_read_b64_tr_b16 v[32:33], v105 offset:8192\n"
"v_max_u32_dpp v110, v110, v110 row_mirror row_mask:0xf bank_mask:0xf\n"
"v_mfma_f32_16x16x32_bf16 v[152:155], v[4:7], v[72:75], v[152:155]\n"
"ds_read_b64_tr_b16 v[34:35], v105 offset:8448\n"
"v_mov_b32 v109, v110\n"
"v_mfma_f32_16x16x32_bf16 v[148:151], v[4:7], v[68:71], v[148:151]\n"
"ds_read_b64_tr_b16 v[36:37], v106 offset:8192\n"
"v_mfma_f32_16x16x32_bf16 v[144:147], v[4:7], v[64:67], v[144:147]\n"
"s_add_u32 s75, s87, 262272\n"
"ds_read_b64_tr_b16 v[38:39], v106 offset:8448\n"
"buffer_load_dwordx4 v104, s[44:47], s75 offen offset:1024 lds\n"
"v_mfma_f32_16x16x32_bf16 v[160:163], v[8:11], v[64:67], v[160:163]\n"
"ds_read_b64_tr_b16 v[40:41], v105 offset:8704\n"
"v_mfma_f32_16x16x32_bf16 v[164:167], v[8:11], v[68:71], v[164:167]\n"
"ds_read_b64_tr_b16 v[42:43], v105 offset:8960\n"
"v_mfma_f32_16x16x32_bf16 v[168:171], v[8:11], v[72:75], v[168:171]\n"
"ds_read_b64_tr_b16 v[44:45], v106 offset:8704\n"
"v_mfma_f32_16x16x32_bf16 v[172:175], v[8:11], v[76:79], v[172:175]\n"
"s_add_u32 m0, s70, 118784\n"
"ds_read_b64_tr_b16 v[46:47], v106 offset:8960\n"
"v_mfma_f32_16x16x32_bf16 v[188:191], v[12:15], v[76:79], v[188:191]\n"
"s_add_u32 s75, s86, 65536\n"
"ds_read_b64_tr_b16 v[48:49], v105 offset:9216\n"
"buffer_load_dwordx4 v101, s[40:43], s75 offen offset:0 lds\n"
"v_mfma_f32_16x16x32_bf16 v[184:187], v[12:15], v[72:75], v[184:187]\n"
"ds_read_b64_tr_b16 v[50:51], v105 offset:9472\n"
"v_mfma_f32_16x16x32_bf16 v[180:183], v[12:15], v[68:71], v[180:183]\n"
"ds_read_b64_tr_b16 v[52:53], v106 offset:9216\n"
"v_mfma_f32_16x16x32_bf16 v[176:179], v[12:15], v[64:67], v[176:179]\n"
"ds_read_b64_tr_b16 v[54:55], v106 offset:9472\n"
"v_mfma_f32_16x16x32_bf16 v[192:195], v[16:19], v[64:67], v[192:195]\n"
"ds_read_b64_tr_b16 v[56:57], v105 offset:9728\n"
"v_mfma_f32_16x16x32_bf16 v[196:199], v[16:19], v[68:71], v[196:199]\n"
"s_add_u32 s75, s86, 80896\n"
"ds_read_b64_tr_b16 v[58:59], v105 offset:9984\n"
"buffer_load_dwordx4 v102, s[40:43], s75 offen offset:1024 lds\n"
"v_mfma_f32_16x16x32_bf16 v[200:203], v[16:19], v[72:75], v[200:203]\n"
"ds_read_b64_tr_b16 v[60:61], v106 offset:9728\n"
"v_mfma_f32_16x16x32_bf16 v[204:207], v[16:19], v[76:79], v[204:207]\n"
"ds_read_b64_tr_b16 v[62:63], v106 offset:9984\n"
"v_mfma_f32_16x16x32_bf16 v[220:223], v[20:23], v[76:79], v[220:223]\n"
"v_permlane16_swap_b32 v110, v109\n"
"v_mfma_f32_16x16x32_bf16 v[216:219], v[20:23], v[72:75], v[216:219]\n"
"v_max_u32 v110, v110, v109\n"
"v_mov_b32 v109, v110\n"
"v_mfma_f32_16x16x32_bf16 v[212:215], v[20:23], v[68:71], v[212:215]\n"
"s_add_u32 s75, s86, 96256\n"
"buffer_load_dwordx4 v101, s[40:43], s75 offen offset:2048 lds\n"
"v_mfma_f32_16x16x32_bf16 v[208:211], v[20:23], v[64:67], v[208:211]\n"
"v_permlane32_swap_b32 v110, v109\n"
"v_mfma_f32_16x16x32_bf16 v[224:227], v[24:27], v[64:67], v[224:227]\n"
"v_max_u32 v110, v110, v109\n"
"v_mfma_f32_16x16x32_bf16 v[228:231], v[24:27], v[68:71], v[228:231]\n"
"v_readfirstlane_b32 s94, v110\n"
"v_mfma_f32_16x16x32_bf16 v[232:235], v[24:27], v[72:75], v[232:235]\n"
"v_mfma_f32_16x16x32_bf16 v[236:239], v[24:27], v[76:79], v[236:239]\n"
"s_add_u32 s75, s86, 111616\n"
"buffer_load_dwordx4 v102, s[40:43], s75 offen offset:3072 lds\n"
"v_mfma_f32_16x16x32_bf16 v[252:255], v[28:31], v[76:79], v[252:255]\n"
"s_bfe_u32 s85, s79, 0x4001c\n"
"v_mfma_f32_16x16x32_bf16 v[248:251], v[28:31], v[72:75], v[248:251]\n"
"s_lshl_b32 s86, s85, 7\n"
"v_mfma_f32_16x16x32_bf16 v[244:247], v[28:31], v[68:71], v[244:247]\n"
"s_lshl_b32 s87, s85, 19\n"
"v_mfma_f32_16x16x32_bf16 v[240:243], v[28:31], v[64:67], v[240:243]\n"
"s_waitcnt vmcnt(12) lgkmcnt(0)\n"
"s_barrier\n"
"v_mfma_f32_16x16x32_bf16 v[128:131], v[32:35], v[80:83], v[128:131]\n"
"v_and_b32 v109, 15, v108\n"
"v_lshlrev_b32 v110, 2, v109\n"
"v_mfma_f32_16x16x32_bf16 v[132:135], v[32:35], v[84:87], v[132:135]\n"
"s_lshl_b32 s90, s92, 2\n"
"s_add_u32 s90, s90, s68\n"
"ds_read_b128 v[64:67], v96 offset:0\n"
"v_mfma_f32_16x16x32_bf16 v[136:139], v[32:35], v[88:91], v[136:139]\n"
"s_add_u32 m0, s69, 65536\n"
"s_lshl_b32 s90, s90, 8\n"
"ds_read_b128 v[68:71], v96 offset:2048\n"
"v_mfma_f32_16x16x32_bf16 v[140:143], v[32:35], v[92:95], v[140:143]\n"
"s_add_u32 s75, s87, 1024\n"
"ds_read_b128 v[72:75], v96 offset:4096\n"
"buffer_load_dwordx4 v104, s[44:47], s75 offen offset:0 lds\n"
"v_mfma_f32_16x16x32_bf16 v[156:159], v[36:39], v[92:95], v[156:159]\n"
"s_add_u32 s90, s90, 4096\n"
"ds_read_b128 v[76:79], v96 offset:6144\n"
"buffer_load_dword v99, v110, s[60:63], s90 offen offset:0 sc1\n"
"v_mfma_f32_16x16x32_bf16 v[152:155], v[36:39], v[88:91], v[152:155]\n"
"ds_read_b64_tr_b16 v[0:1], v112 offset:0\n"
"buffer_load_dword v103, v110, s[60:63], s90 offen offset:64 sc1\n"
"v_mfma_f32_16x16x32_bf16 v[148:151], v[36:39], v[84:87], v[148:151]\n"
"ds_read_b64_tr_b16 v[2:3], v112 offset:256\n"
"buffer_load_dword v107, v110, s[60:63], s90 offen offset:128 sc1\n"
"v_mfma_f32_16x16x32_bf16 v[144:147], v[36:39], v[80:83], v[144:147]\n"
"ds_read_b64_tr_b16 v[4:5], v113 offset:0\n"
"buffer_load_dword v111, v110, s[60:63], s90 offen offset:192 sc1\n"
"v_mfma_f32_16x16x32_bf16 v[160:163], v[40:43], v[80:83], v[160:163]\n"
"s_add_u32 s75, s87, 128\n"
"ds_read_b64_tr_b16 v[6:7], v113 offset:256\n"
"buffer_load_dwordx4 v104, s[44:47], s75 offen offset:1024 lds\n"
"v_mfma_f32_16x16x32_bf16 v[164:167], v[40:43], v[84:87], v[164:167]\n"
"ds_read_b64_tr_b16 v[8:9], v112 offset:512\n"
"v_mul_u32_u24 v109, 16, v109\n"
"v_mfma_f32_16x16x32_bf16 v[168:171], v[40:43], v[88:91], v[168:171]\n"
"s_lshl_b32 s90, s91, 12\n"
"s_add_u32 s90, s90, 16384\n"
"ds_read_b64_tr_b16 v[10:11], v112 offset:768\n"
"v_mfma_f32_16x16x32_bf16 v[172:175], v[40:43], v[92:95], v[172:175]\n"
"s_and_b32 s73, s92, 1\n"
"s_lshl_b32 s73, s73, 11\n"
"ds_read_b64_tr_b16 v[12:13], v113 offset:512\n"
"v_mfma_f32_16x16x32_bf16 v[188:191], v[44:47], v[92:95], v[188:191]\n"
"s_add_u32 m0, s70, 81920\n"
"s_add_u32 s90, s90, s73\n"
"ds_read_b64_tr_b16 v[14:15], v113 offset:768\n"
"v_mfma_f32_16x16x32_bf16 v[184:187], v[44:47], v[88:91], v[184:187]\n"
"s_add_u32 s75, s86, 0\n"
"ds_read_b64_tr_b16 v[16:17], v112 offset:1024\n"
"buffer_load_dwordx4 v101, s[40:43], s75 offen offset:0 lds\n"
"v_mfma_f32_16x16x32_bf16 v[180:183], v[44:47], v[84:87], v[180:183]\n"
"ds_read_b64_tr_b16 v[18:19], v112 offset:1280\n"
"buffer_load_dwordx4 a[128:131], v109, s[60:63], s90 offen offset:0 sc1\n"
"v_mfma_f32_16x16x32_bf16 v[176:179], v[44:47], v[80:83], v[176:179]\n"
"ds_read_b64_tr_b16 v[20:21], v113 offset:1024\n"
"buffer_load_dwordx4 a[132:135], v109, s[60:63], s90 offen offset:256 sc1\n"
"v_mfma_f32_16x16x32_bf16 v[192:195], v[48:51], v[80:83], v[192:195]\n"
"ds_read_b64_tr_b16 v[22:23], v113 offset:1280\n"
"buffer_load_dwordx4 a[136:139], v109, s[60:63], s90 offen offset:512 sc1\n"
"v_mfma_f32_16x16x32_bf16 v[196:199], v[48:51], v[84:87], v[196:199]\n"
"ds_read_b64_tr_b16 v[24:25], v112 offset:1536\n"
"buffer_load_dwordx4 a[140:143], v109, s[60:63], s90 offen offset:768 sc1\n"
"v_mfma_f32_16x16x32_bf16 v[200:203], v[48:51], v[88:91], v[200:203]\n"
"s_add_u32 s75, s86, 15360\n"
"ds_read_b64_tr_b16 v[26:27], v112 offset:1792\n"
"buffer_load_dwordx4 v102, s[40:43], s75 offen offset:1024 lds\n"
"v_mfma_f32_16x16x32_bf16 v[204:207], v[48:51], v[92:95], v[204:207]\n"
"ds_read_b64_tr_b16 v[28:29], v113 offset:1536\n"
"buffer_load_dwordx4 a[144:147], v109, s[60:63], s90 offen offset:1024 sc1\n"
"v_mfma_f32_16x16x32_bf16 v[220:223], v[52:55], v[92:95], v[220:223]\n"
"ds_read_b64_tr_b16 v[30:31], v113 offset:1792\n"
"buffer_load_dwordx4 a[148:151], v109, s[60:63], s90 offen offset:1280 sc1\n"
"v_mfma_f32_16x16x32_bf16 v[216:219], v[52:55], v[88:91], v[216:219]\n"
"buffer_load_dwordx4 a[152:155], v109, s[60:63], s90 offen offset:1536 sc1\n"
"buffer_load_dwordx4 a[156:159], v109, s[60:63], s90 offen offset:1792 sc1\n"
"v_mfma_f32_16x16x32_bf16 v[212:215], v[52:55], v[84:87], v[212:215]\n"
"v_mfma_f32_16x16x32_bf16 v[208:211], v[52:55], v[80:83], v[208:211]\n"
"s_add_u32 s75, s86, 30720\n"
"buffer_load_dwordx4 v101, s[40:43], s75 offen offset:2048 lds\n"
"v_mfma_f32_16x16x32_bf16 v[224:227], v[56:59], v[80:83], v[224:227]\n"
"v_mfma_f32_16x16x32_bf16 v[228:231], v[56:59], v[84:87], v[228:231]\n"
"v_mfma_f32_16x16x32_bf16 v[232:235], v[56:59], v[88:91], v[232:235]\n"
"v_mfma_f32_16x16x32_bf16 v[236:239], v[56:59], v[92:95], v[236:239]\n"
"v_mfma_f32_16x16x32_bf16 v[252:255], v[60:63], v[92:95], v[252:255]\n"
"s_add_u32 s75, s86, 46080\n"
"buffer_load_dwordx4 v102, s[40:43], s75 offen offset:3072 lds\n"
"v_mfma_f32_16x16x32_bf16 v[248:251], v[60:63], v[88:91], v[248:251]\n"
"v_mfma_f32_16x16x32_bf16 v[244:247], v[60:63], v[84:87], v[244:247]\n"
"v_mfma_f32_16x16x32_bf16 v[240:243], v[60:63], v[80:83], v[240:243]\n"
"s_waitcnt lgkmcnt(0)\n"
"v_mfma_f32_16x16x32_bf16 v[128:131], v[0:3], v[64:67], v[128:131]\n"
"s_and_b32 s73, s94, 2047\n"
"s_sub_u32 s73, 2047, s73\n"
"ds_read_b128 v[80:83], v97 offset:0\n"
"v_mfma_f32_16x16x32_bf16 v[132:135], v[0:3], v[68:71], v[132:135]\n"
"s_add_u32 m0, s69, 73728\n"
"s_lshl_b32 s73, s73, 11\n"
"ds_read_b128 v[84:87], v97 offset:2048\n"
"v_mfma_f32_16x16x32_bf16 v[136:139], v[0:3], v[72:75], v[136:139]\n"
"s_add_u32 s75, s87, 263168\n"
"ds_read_b128 v[88:91], v97 offset:4096\n"
"buffer_load_dwordx4 v104, s[44:47], s75 offen offset:0 lds\n"
"v_mfma_f32_16x16x32_bf16 v[140:143], v[0:3], v[76:79], v[140:143]\n"
"s_and_b32 s74, s93, 7\n"
"s_lshl_b32 s74, s74, 8\n"
"ds_read_b128 v[92:95], v97 offset:6144\n"
"v_mfma_f32_16x16x32_bf16 v[156:159], v[4:7], v[76:79], v[156:159]\n"
"s_add_u32 s90, s73, s74\n"
"ds_read_b64_tr_b16 v[32:33], v112 offset:8192\n"
"v_and_b32 v109, 48, v108\n"
"v_mfma_f32_16x16x32_bf16 v[152:155], v[4:7], v[72:75], v[152:155]\n"
"s_mov_b32 s88, 0xfffdfffd\n"
"s_mov_b32 s89, 0xfffdfffd\n"
"ds_read_b64_tr_b16 v[34:35], v112 offset:8448\n"
"v_mfma_f32_16x16x32_bf16 v[148:151], v[4:7], v[68:71], v[148:151]\n"
"s_lshr_b32 s73, s73, 9\n"
"s_add_u32 s73, s73, 4096\n"
"ds_read_b64_tr_b16 v[36:37], v113 offset:8192\n"
"v_mfma_f32_16x16x32_bf16 v[144:147], v[4:7], v[64:67], v[144:147]\n"
"s_add_u32 s75, s87, 262272\n"
"ds_read_b64_tr_b16 v[38:39], v113 offset:8448\n"
"buffer_load_dwordx4 v104, s[44:47], s75 offen offset:1024 lds\n"
"v_mfma_f32_16x16x32_bf16 v[160:163], v[8:11], v[64:67], v[160:163]\n"
"ds_read_b64_tr_b16 v[40:41], v112 offset:8704\n"
"v_mov_b32 v110, 0\n"
"v_mfma_f32_16x16x32_bf16 v[164:167], v[8:11], v[68:71], v[164:167]\n"
"ds_read_b64_tr_b16 v[42:43], v112 offset:8960\n"
"buffer_load_dword v110, v110, s[60:63], s73 offen offset:0 sc1\n"
"v_mfma_f32_16x16x32_bf16 v[168:171], v[8:11], v[72:75], v[168:171]\n"
"ds_read_b64_tr_b16 v[44:45], v113 offset:8704\n"
"v_mfma_f32_16x16x32_bf16 v[172:175], v[8:11], v[76:79], v[172:175]\n"
"s_add_u32 m0, s70, 86016\n"
"ds_read_b64_tr_b16 v[46:47], v113 offset:8960\n"
"v_mfma_f32_16x16x32_bf16 v[188:191], v[12:15], v[76:79], v[188:191]\n"
"s_add_u32 s75, s86, 65536\n"
"ds_read_b64_tr_b16 v[48:49], v112 offset:9216\n"
"buffer_load_dwordx4 v101, s[40:43], s75 offen offset:0 lds\n"
"v_mfma_f32_16x16x32_bf16 v[184:187], v[12:15], v[72:75], v[184:187]\n"
"ds_read_b64_tr_b16 v[50:51], v112 offset:9472\n"
"v_mfma_f32_16x16x32_bf16 v[180:183], v[12:15], v[68:71], v[180:183]\n"
"ds_read_b64_tr_b16 v[52:53], v113 offset:9216\n"
"v_mfma_f32_16x16x32_bf16 v[176:179], v[12:15], v[64:67], v[176:179]\n"
"ds_read_b64_tr_b16 v[54:55], v113 offset:9472\n"
"v_mfma_f32_16x16x32_bf16 v[192:195], v[16:19], v[64:67], v[192:195]\n"
"ds_read_b64_tr_b16 v[56:57], v112 offset:9728\n"
"v_mfma_f32_16x16x32_bf16 v[196:199], v[16:19], v[68:71], v[196:199]\n"
"s_add_u32 s75, s86, 80896\n"
"ds_read_b64_tr_b16 v[58:59], v112 offset:9984\n"
"buffer_load_dwordx4 v102, s[40:43], s75 offen offset:1024 lds\n"
"v_mfma_f32_16x16x32_bf16 v[200:203], v[16:19], v[72:75], v[200:203]\n"
"ds_read_b64_tr_b16 v[60:61], v113 offset:9728\n"
"v_mfma_f32_16x16x32_bf16 v[204:207], v[16:19], v[76:79], v[204:207]\n"
"ds_read_b64_tr_b16 v[62:63], v113 offset:9984\n"
"v_mfma_f32_16x16x32_bf16 v[220:223], v[20:23], v[76:79], v[220:223]\n"
"v_mfma_f32_16x16x32_bf16 v[216:219], v[20:23], v[72:75], v[216:219]\n"
"v_mfma_f32_16x16x32_bf16 v[212:215], v[20:23], v[68:71], v[212:215]\n"
"s_add_u32 s75, s86, 96256\n"
"buffer_load_dwordx4 v101, s[40:43], s75 offen offset:2048 lds\n"
"v_mfma_f32_16x16x32_bf16 v[208:211], v[20:23], v[64:67], v[208:211]\n"
"v_mfma_f32_16x16x32_bf16 v[224:227], v[24:27], v[64:67], v[224:227]\n"
"v_mfma_f32_16x16x32_bf16 v[228:231], v[24:27], v[68:71], v[228:231]\n"
"v_mfma_f32_16x16x32_bf16 v[232:235], v[24:27], v[72:75], v[232:235]\n"
"v_mfma_f32_16x16x32_bf16 v[236:239], v[24:27], v[76:79], v[236:239]\n"
"s_add_u32 s75, s86, 111616\n"
"buffer_load_dwordx4 v102, s[40:43], s75 offen offset:3072 lds\n"
"v_mfma_f32_16x16x32_bf16 v[252:255], v[28:31], v[76:79], v[252:255]\n"
"v_mfma_f32_16x16x32_bf16 v[248:251], v[28:31], v[72:75], v[248:251]\n"
"v_mfma_f32_16x16x32_bf16 v[244:247], v[28:31], v[68:71], v[244:247]\n"
"v_mfma_f32_16x16x32_bf16 v[240:243], v[28:31], v[64:67], v[240:243]\n"
"s_waitcnt vmcnt(25) lgkmcnt(0)\n"
"s_barrier\n"
"s_mov_b64 exec, s[88:89]\n"
"buffer_load_dwordx4 a[212:215], v109, s[96:99], s90 offen offset:0\n"
"buffer_load_dwordx4 a[216:219], v109, s[96:99], s90 offen offset:64\n"
"buffer_load_dwordx4 a[220:223], v109, s[96:99], s90 offen offset:128\n"
"buffer_load_dwordx4 a[224:227], v109, s[96:99], s90 offen offset:192\n"
"s_mov_b64 exec, -1\n"
"v_mfma_f32_16x16x32_bf16 v[128:131], v[32:35], v[80:83], v[128:131]\n"
"v_mfma_f32_16x16x32_bf16 v[132:135], v[32:35], v[84:87], v[132:135]\n"
"ds_read_b128 v[64:67], v98 offset:32768\n"
"v_mfma_f32_16x16x32_bf16 v[136:139], v[32:35], v[88:91], v[136:139]\n"
"s_add_u32 m0, s71, 0\n"
"ds_read_b128 v[68:71], v98 offset:34816\n"
"v_mfma_f32_16x16x32_bf16 v[140:143], v[32:35], v[92:95], v[140:143]\n"
"s_mov_b32 s75, 0\n"
"ds_read_b128 v[72:75], v98 offset:36864\n"
"buffer_load_dwordx4 v118, s[48:51], s75 offen offset:0 lds\n"
"v_mfma_f32_16x16x32_bf16 v[156:159], v[36:39], v[92:95], v[156:159]\n"
"ds_read_b128 v[76:79], v98 offset:38912\n"
"v_mfma_f32_16x16x32_bf16 v[152:155], v[36:39], v[88:91], v[152:155]\n"
"ds_read_b64_tr_b16 v[0:1], v112 offset:49152\n"
"v_mfma_f32_16x16x32_bf16 v[148:151], v[36:39], v[84:87], v[148:151]\n"
"ds_read_b64_tr_b16 v[2:3], v112 offset:49408\n"
"v_mfma_f32_16x16x32_bf16 v[144:147], v[36:39], v[80:83], v[144:147]\n"
"s_add_u32 m0, s71, 4096\n"
"ds_read_b64_tr_b16 v[4:5], v113 offset:49152\n"
"v_mfma_f32_16x16x32_bf16 v[160:163], v[40:43], v[80:83], v[160:163]\n"
"s_mov_b32 s75, 4096\n"
"ds_read_b64_tr_b16 v[6:7], v113 offset:49408\n"
"buffer_load_dwordx4 v118, s[48:51], s75 offen offset:0 lds\n"
"v_mfma_f32_16x16x32_bf16 v[164:167], v[40:43], v[84:87], v[164:167]\n"
"ds_read_b64_tr_b16 v[8:9], v112 offset:49664\n"
"v_mfma_f32_16x16x32_bf16 v[168:171], v[40:43], v[88:91], v[168:171]\n"
"ds_read_b64_tr_b16 v[10:11], v112 offset:49920\n"
"v_mfma_f32_16x16x32_bf16 v[172:175], v[40:43], v[92:95], v[172:175]\n"
"ds_read_b64_tr_b16 v[12:13], v113 offset:49664\n"
"v_mfma_f32_16x16x32_bf16 v[188:191], v[44:47], v[92:95], v[188:191]\n"
"s_add_u32 m0, s71, 8192\n"
"ds_read_b64_tr_b16 v[14:15], v113 offset:49920\n"
"v_mfma_f32_16x16x32_bf16 v[184:187], v[44:47], v[88:91], v[184:187]\n"
"s_mov_b32 s75, 8192\n"
"ds_read_b64_tr_b16 v[16:17], v112 offset:50176\n"
"buffer_load_dwordx4 v118, s[48:51], s75 offen offset:0 lds\n"
"v_mfma_f32_16x16x32_bf16 v[180:183], v[44:47], v[84:87], v[180:183]\n"
"ds_read_b64_tr_b16 v[18:19], v112 offset:50432\n"
"v_mfma_f32_16x16x32_bf16 v[176:179], v[44:47], v[80:83], v[176:179]\n"
"ds_read_b64_tr_b16 v[20:21], v113 offset:50176\n"
"v_mfma_f32_16x16x32_bf16 v[192:195], v[48:51], v[80:83], v[192:195]\n"
"ds_read_b64_tr_b16 v[22:23], v113 offset:50432\n"
"v_mfma_f32_16x16x32_bf16 v[196:199], v[48:51], v[84:87], v[196:199]\n"
"s_add_u32 m0, s71, 12288\n"
"ds_read_b64_tr_b16 v[24:25], v112 offset:50688\n"
"v_mfma_f32_16x16x32_bf16 v[200:203], v[48:51], v[88:91], v[200:203]\n"
"s_mov_b32 s75, 12288\n"
"ds_read_b64_tr_b16 v[26:27], v112 offset:50944\n"
"buffer_load_dwordx4 v118, s[48:51], s75 offen offset:0 lds\n"
"v_mfma_f32_16x16x32_bf16 v[204:207], v[48:51], v[92:95], v[204:207]\n"
"ds_read_b64_tr_b16 v[28:29], v113 offset:50688\n"
"v_mfma_f32_16x16x32_bf16 v[220:223], v[52:55], v[92:95], v[220:223]\n"
"ds_read_b64_tr_b16 v[30:31], v113 offset:50944\n"
"v_mfma_f32_16x16x32_bf16 v[216:219], v[52:55], v[88:91], v[216:219]\n"
"v_mfma_f32_16x16x32_bf16 v[212:215], v[52:55], v[84:87], v[212:215]\n"
"s_add_u32 m0, s71, 16384\n"
"v_mfma_f32_16x16x32_bf16 v[208:211], v[52:55], v[80:83], v[208:211]\n"
"s_mov_b32 s75, 16384\n"
"buffer_load_dwordx4 v118, s[48:51], s75 offen offset:0 lds\n"
"v_mfma_f32_16x16x32_bf16 v[224:227], v[56:59], v[80:83], v[224:227]\n"
"v_mfma_f32_16x16x32_bf16 v[228:231], v[56:59], v[84:87], v[228:231]\n"
"v_mfma_f32_16x16x32_bf16 v[232:235], v[56:59], v[88:91], v[232:235]\n"
"v_mfma_f32_16x16x32_bf16 v[236:239], v[56:59], v[92:95], v[236:239]\n"
"s_add_u32 m0, s71, 20480\n"
"v_mfma_f32_16x16x32_bf16 v[252:255], v[60:63], v[92:95], v[252:255]\n"
"s_mov_b32 s75, 20480\n"
"buffer_load_dwordx4 v118, s[48:51], s75 offen offset:0 lds\n"
"v_mfma_f32_16x16x32_bf16 v[248:251], v[60:63], v[88:91], v[248:251]\n"
"v_mfma_f32_16x16x32_bf16 v[244:247], v[60:63], v[84:87], v[244:247]\n"
"v_mfma_f32_16x16x32_bf16 v[240:243], v[60:63], v[80:83], v[240:243]\n"
"s_waitcnt lgkmcnt(0)\n"
"v_mfma_f32_16x16x32_bf16 v[128:131], v[0:3], v[64:67], v[128:131]\n"
"ds_read_b128 v[80:83], v100 offset:32768\n"
"v_mfma_f32_16x16x32_bf16 v[132:135], v[0:3], v[68:71], v[132:135]\n"
"s_add_u32 m0, s71, 24576\n"
"ds_read_b128 v[84:87], v100 offset:34816\n"
"v_mfma_f32_16x16x32_bf16 v[136:139], v[0:3], v[72:75], v[136:139]\n"
"s_mov_b32 s75, 24576\n"
"ds_read_b128 v[88:91], v100 offset:36864\n"
"buffer_load_dwordx4 v118, s[48:51], s75 offen offset:0 lds\n"
"v_mfma_f32_16x16x32_bf16 v[140:143], v[0:3], v[76:79], v[140:143]\n"
"ds_read_b128 v[92:95], v100 offset:38912\n"
"v_mfma_f32_16x16x32_bf16 v[156:159], v[4:7], v[76:79], v[156:159]\n"
"ds_read_b64_tr_b16 v[32:33], v112 offset:57344\n"
"v_mfma_f32_16x16x32_bf16 v[152:155], v[4:7], v[72:75], v[152:155]\n"
"ds_read_b64_tr_b16 v[34:35], v112 offset:57600\n"
"v_mfma_f32_16x16x32_bf16 v[148:151], v[4:7], v[68:71], v[148:151]\n"
"s_add_u32 m0, s71, 28672\n"
"ds_read_b64_tr_b16 v[36:37], v113 offset:57344\n"
"v_mfma_f32_16x16x32_bf16 v[144:147], v[4:7], v[64:67], v[144:147]\n"
"s_mov_b32 s75, 28672\n"
"ds_read_b64_tr_b16 v[38:39], v113 offset:57600\n"
"buffer_load_dwordx4 v118, s[48:51], s75 offen offset:0 lds\n"
"v_mfma_f32_16x16x32_bf16 v[160:163], v[8:11], v[64:67], v[160:163]\n"
"ds_read_b64_tr_b16 v[40:41], v112 offset:57856\n"
"v_mfma_f32_16x16x32_bf16 v[164:167], v[8:11], v[68:71], v[164:167]\n"
"ds_read_b64_tr_b16 v[42:43], v112 offset:58112\n"
"v_mfma_f32_16x16x32_bf16 v[168:171], v[8:11], v[72:75], v[168:171]\n"
"ds_read_b64_tr_b16 v[44:45], v113 offset:57856\n"
"v_mfma_f32_16x16x32_bf16 v[172:175], v[8:11], v[76:79], v[172:175]\n"
"s_add_u32 m0, s71, 32768\n"
"ds_read_b64_tr_b16 v[46:47], v113 offset:58112\n"
"v_mfma_f32_16x16x32_bf16 v[188:191], v[12:15], v[76:79], v[188:191]\n"
"s_mov_b32 s75, 32768\n"
"ds_read_b64_tr_b16 v[48:49], v112 offset:58368\n"
"buffer_load_dwordx4 v118, s[48:51], s75 offen offset:0 lds\n"
"v_mfma_f32_16x16x32_bf16 v[184:187], v[12:15], v[72:75], v[184:187]\n"
"ds_read_b64_tr_b16 v[50:51], v112 offset:58624\n"
"v_mfma_f32_16x16x32_bf16 v[180:183], v[12:15], v[68:71], v[180:183]\n"
"ds_read_b64_tr_b16 v[52:53], v113 offset:58368\n"
"v_mfma_f32_16x16x32_bf16 v[176:179], v[12:15], v[64:67], v[176:179]\n"
"ds_read_b64_tr_b16 v[54:55], v113 offset:58624\n"
"v_mfma_f32_16x16x32_bf16 v[192:195], v[16:19], v[64:67], v[192:195]\n"
"s_add_u32 m0, s71, 36864\n"
"ds_read_b64_tr_b16 v[56:57], v112 offset:58880\n"
"v_mfma_f32_16x16x32_bf16 v[196:199], v[16:19], v[68:71], v[196:199]\n"
"s_mov_b32 s75, 36864\n"
"ds_read_b64_tr_b16 v[58:59], v112 offset:59136\n"
"buffer_load_dwordx4 v118, s[48:51], s75 offen offset:0 lds\n"
"v_mfma_f32_16x16x32_bf16 v[200:203], v[16:19], v[72:75], v[200:203]\n"
"ds_read_b64_tr_b16 v[60:61], v113 offset:58880\n"
"v_mfma_f32_16x16x32_bf16 v[204:207], v[16:19], v[76:79], v[204:207]\n"
"ds_read_b64_tr_b16 v[62:63], v113 offset:59136\n"
"v_mfma_f32_16x16x32_bf16 v[220:223], v[20:23], v[76:79], v[220:223]\n"
"v_mfma_f32_16x16x32_bf16 v[216:219], v[20:23], v[72:75], v[216:219]\n"
"s_add_u32 m0, s71, 40960\n"
"v_mfma_f32_16x16x32_bf16 v[212:215], v[20:23], v[68:71], v[212:215]\n"
"s_mov_b32 s75, 40960\n"
"buffer_load_dwordx4 v118, s[48:51], s75 offen offset:0 lds\n"
"v_mfma_f32_16x16x32_bf16 v[208:211], v[20:23], v[64:67], v[208:211]\n"
"v_mfma_f32_16x16x32_bf16 v[224:227], v[24:27], v[64:67], v[224:227]\n"
"v_mfma_f32_16x16x32_bf16 v[228:231], v[24:27], v[68:71], v[228:231]\n"
"v_mfma_f32_16x16x32_bf16 v[232:235], v[24:27], v[72:75], v[232:235]\n"
"s_add_u32 m0, s71, 45056\n"
"v_mfma_f32_16x16x32_bf16 v[236:239], v[24:27], v[76:79], v[236:239]\n"
"s_mov_b32 s75, 45056\n"
"buffer_load_dwordx4 v118, s[48:51], s75 offen offset:0 lds\n"
"v_mfma_f32_16x16x32_bf16 v[252:255], v[28:31], v[76:79], v[252:255]\n"
"v_mfma_f32_16x16x32_bf16 v[248:251], v[28:31], v[72:75], v[248:251]\n"
"v_mfma_f32_16x16x32_bf16 v[244:247], v[28:31], v[68:71], v[244:247]\n"
"v_mfma_f32_16x16x32_bf16 v[240:243], v[28:31], v[64:67], v[240:243]\n"
"s_waitcnt vmcnt(16) lgkmcnt(0)\n"
"s_barrier\n"
"v_mfma_f32_16x16x32_bf16 v[128:131], v[32:35], v[80:83], v[128:131]\n"
"v_mfma_f32_16x16x32_bf16 v[132:135], v[32:35], v[84:87], v[132:135]\n"
"ds_read_b128 v[64:67], v98 offset:0\n"
"v_mfma_f32_16x16x32_bf16 v[136:139], v[32:35], v[88:91], v[136:139]\n"
"s_add_u32 m0, s72, 49152\n"
"ds_read_b128 v[68:71], v98 offset:2048\n"
"v_mfma_f32_16x16x32_bf16 v[140:143], v[32:35], v[92:95], v[140:143]\n"
"s_mov_b32 s75, 1024\n"
"ds_read_b128 v[72:75], v98 offset:4096\n"
"buffer_load_dwordx4 v122, s[52:55], s75 offen offset:0 lds\n"
"v_mfma_f32_16x16x32_bf16 v[156:159], v[36:39], v[92:95], v[156:159]\n"
"ds_read_b128 v[76:79], v98 offset:6144\n"
"v_mfma_f32_16x16x32_bf16 v[152:155], v[36:39], v[88:91], v[152:155]\n"
"ds_read_b64_tr_b16 v[0:1], v105 offset:0\n"
"v_mfma_f32_16x16x32_bf16 v[148:151], v[36:39], v[84:87], v[148:151]\n"
"ds_read_b64_tr_b16 v[2:3], v105 offset:256\n"
"v_mfma_f32_16x16x32_bf16 v[144:147], v[36:39], v[80:83], v[144:147]\n"
"ds_read_b64_tr_b16 v[4:5], v106 offset:0\n"
"v_mfma_f32_16x16x32_bf16 v[160:163], v[40:43], v[80:83], v[160:163]\n"
"s_mov_b32 s75, 128\n"
"ds_read_b64_tr_b16 v[6:7], v106 offset:256\n"
"buffer_load_dwordx4 v122, s[52:55], s75 offen offset:1024 lds\n"
"v_mfma_f32_16x16x32_bf16 v[164:167], v[40:43], v[84:87], v[164:167]\n"
"ds_read_b64_tr_b16 v[8:9], v105 offset:512\n"
"v_mfma_f32_16x16x32_bf16 v[168:171], v[40:43], v[88:91], v[168:171]\n"
"ds_read_b64_tr_b16 v[10:11], v105 offset:768\n"
"v_mfma_f32_16x16x32_bf16 v[172:175], v[40:43], v[92:95], v[172:175]\n"
"ds_read_b64_tr_b16 v[12:13], v106 offset:512\n"
"v_mfma_f32_16x16x32_bf16 v[188:191], v[44:47], v[92:95], v[188:191]\n"
"s_add_u32 m0, s72, 57344\n"
"ds_read_b64_tr_b16 v[14:15], v106 offset:768\n"
"v_mfma_f32_16x16x32_bf16 v[184:187], v[44:47], v[88:91], v[184:187]\n"
"s_mov_b32 s75, 9216\n"
"ds_read_b64_tr_b16 v[16:17], v105 offset:1024\n"
"buffer_load_dwordx4 v122, s[52:55], s75 offen offset:0 lds\n"
"v_mfma_f32_16x16x32_bf16 v[180:183], v[44:47], v[84:87], v[180:183]\n"
"ds_read_b64_tr_b16 v[18:19], v105 offset:1280\n"
"v_mfma_f32_16x16x32_bf16 v[176:179], v[44:47], v[80:83], v[176:179]\n"
"ds_read_b64_tr_b16 v[20:21], v106 offset:1024\n"
"v_mfma_f32_16x16x32_bf16 v[192:195], v[48:51], v[80:83], v[192:195]\n"
"ds_read_b64_tr_b16 v[22:23], v106 offset:1280\n"
"v_mfma_f32_16x16x32_bf16 v[196:199], v[48:51], v[84:87], v[196:199]\n"
"ds_read_b64_tr_b16 v[24:25], v105 offset:1536\n"
"v_mfma_f32_16x16x32_bf16 v[200:203], v[48:51], v[88:91], v[200:203]\n"
"s_mov_b32 s75, 8320\n"
"ds_read_b64_tr_b16 v[26:27], v105 offset:1792\n"
"buffer_load_dwordx4 v122, s[52:55], s75 offen offset:1024 lds\n"
"v_mfma_f32_16x16x32_bf16 v[204:207], v[48:51], v[92:95], v[204:207]\n"
"ds_read_b64_tr_b16 v[28:29], v106 offset:1536\n"
"v_mfma_f32_16x16x32_bf16 v[220:223], v[52:55], v[92:95], v[220:223]\n"
"s_lshr_b32 s73, s93, 3\n"
"s_lshl_b32 s73, s73, 12\n"
"ds_read_b64_tr_b16 v[30:31], v106 offset:1792\n"
"v_mfma_f32_16x16x32_bf16 v[216:219], v[52:55], v[88:91], v[216:219]\n"
"v_lshlrev_b32 v109, 6, v108\n"
"v_mfma_f32_16x16x32_bf16 v[212:215], v[52:55], v[84:87], v[212:215]\n"
"buffer_load_dword a252, v109, s[48:51], s73 offen offset:0\n"
"buffer_load_dword a252, v109, s[52:55], s73 offen offset:2048\n"
"v_mfma_f32_16x16x32_bf16 v[208:211], v[52:55], v[80:83], v[208:211]\n"
"v_mfma_f32_16x16x32_bf16 v[224:227], v[56:59], v[80:83], v[224:227]\n"
"v_mfma_f32_16x16x32_bf16 v[228:231], v[56:59], v[84:87], v[228:231]\n"
"v_mfma_f32_16x16x32_bf16 v[232:235], v[56:59], v[88:91], v[232:235]\n"
"v_mfma_f32_16x16x32_bf16 v[236:239], v[56:59], v[92:95], v[236:239]\n"
"v_mfma_f32_16x16x32_bf16 v[252:255], v[60:63], v[92:95], v[252:255]\n"
"v_mfma_f32_16x16x32_bf16 v[248:251], v[60:63], v[88:91], v[248:251]\n"
"v_mfma_f32_16x16x32_bf16 v[244:247], v[60:63], v[84:87], v[244:247]\n"
"v_mfma_f32_16x16x32_bf16 v[240:243], v[60:63], v[80:83], v[240:243]\n"
"s_waitcnt lgkmcnt(0)\n"
"v_mfma_f32_16x16x32_bf16 v[128:131], v[0:3], v[64:67], v[128:131]\n"
"s_mov_b32 s78, 131072\n"
"s_mov_b32 s79, 262144\n"
"ds_read_b128 v[80:83], v100 offset:0\n"
"v_mfma_f32_16x16x32_bf16 v[132:135], v[0:3], v[68:71], v[132:135]\n"
"s_mov_b32 s80, 393216\n"
"ds_read_b128 v[84:87], v100 offset:2048\n"
"v_readfirstlane_b32 s85, v110\n"
"v_mfma_f32_16x16x32_bf16 v[136:139], v[0:3], v[72:75], v[136:139]\n"
"s_mov_b32 s88, 0xffff\n"
"s_mov_b32 s89, 0\n"
"ds_read_b128 v[88:91], v100 offset:4096\n"
"v_mfma_f32_16x16x32_bf16 v[140:143], v[0:3], v[76:79], v[140:143]\n"
"ds_read_b128 v[92:95], v100 offset:6144\n"
"v_lshrrev_b32 v109, 16, v99\n"
"v_mfma_f32_16x16x32_bf16 v[144:147], v[4:7], v[64:67], v[144:147]\n"
"ds_read_b64_tr_b16 v[32:33], v105 offset:8192\n"
"v_xor_b32 v109, 0x8000, v109\n"
"v_mfma_f32_16x16x32_bf16 v[148:151], v[4:7], v[68:71], v[148:151]\n"
"ds_read_b64_tr_b16 v[34:35], v105 offset:8448\n"
"v_lshl_or_b32 v109, v109, 16, v109\n"
"v_mfma_f32_16x16x32_bf16 v[152:155], v[4:7], v[72:75], v[152:155]\n"
"ds_read_b64_tr_b16 v[36:37], v106 offset:8192\n"
"v_cndmask_b32 v109, 0, v109, s[88:89]\n"
"v_mfma_f32_16x16x32_bf16 v[156:159], v[4:7], v[76:79], v[156:159]\n"
"ds_read_b64_tr_b16 v[38:39], v106 offset:8448\n"
"v_accvgpr_write_b32 a160, v109\n"
"v_mfma_f32_16x16x32_bf16 v[160:163], v[8:11], v[64:67], v[160:163]\n"
"ds_read_b64_tr_b16 v[40:41], v105 offset:8704\n"
"v_accvgpr_write_b32 a161, v109\n"
"v_mfma_f32_16x16x32_bf16 v[164:167], v[8:11], v[68:71], v[164:167]\n"
"ds_read_b64_tr_b16 v[42:43], v105 offset:8960\n"
"v_accvgpr_write_b32 a162, v109\n"
"v_mfma_f32_16x16x32_bf16 v[168:171], v[8:11], v[72:75], v[168:171]\n"
"ds_read_b64_tr_b16 v[44:45], v106 offset:8704\n"
"v_accvgpr_write_b32 a163, v109\n"
"v_mfma_f32_16x16x32_bf16 v[172:175], v[8:11], v[76:79], v[172:175]\n"
"ds_read_b64_tr_b16 v[46:47], v106 offset:8960\n"
"v_lshlrev_b32 v99, 16, v99\n"
"v_mfma_f32_16x16x32_bf16 v[176:179], v[12:15], v[64:67], v[176:179]\n"
"ds_read_b64_tr_b16 v[48:49], v105 offset:9216\n"
"v_mfma_f32_16x16x32_bf16 v[180:183], v[12:15], v[68:71], v[180:183]\n"
"ds_read_b64_tr_b16 v[50:51], v105 offset:9472\n"
"v_mfma_f32_16x16x32_bf16 v[184:187], v[12:15], v[72:75], v[184:187]\n"
"ds_read_b64_tr_b16 v[52:53], v106 offset:9216\n"
"v_mfma_f32_16x16x32_bf16 v[188:191], v[12:15], v[76:79], v[188:191]\n"
"ds_read_b64_tr_b16 v[54:55], v106 offset:9472\n"
"v_mfma_f32_16x16x32_bf16 v[192:195], v[16:19], v[64:67], v[192:195]\n"
"ds_read_b64_tr_b16 v[56:57], v105 offset:9728\n"
"v_mfma_f32_16x16x32_bf16 v[196:199], v[16:19], v[68:71], v[196:199]\n"
"ds_read_b64_tr_b16 v[58:59], v105 offset:9984\n"
"v_mfma_f32_16x16x32_bf16 v[200:203], v[16:19], v[72:75], v[200:203]\n"
"ds_read_b64_tr_b16 v[60:61], v106 offset:9728\n"
"v_mfma_f32_16x16x32_bf16 v[204:207], v[16:19], v[76:79], v[204:207]\n"
"ds_read_b64_tr_b16 v[62:63], v106 offset:9984\n"
"v_mfma_f32_16x16x32_bf16 v[208:211], v[20:23], v[64:67], v[208:211]\n"
"v_rcp_f32 v99, v99\n"
"v_mfma_f32_16x16x32_bf16 v[212:215], v[20:23], v[68:71], v[212:215]\n"
"v_mul_f32 v99, s81, v99\n"
"v_lshrrev_b32 v109, 16, v103\n"
"v_mfma_f32_16x16x32_bf16 v[216:219], v[20:23], v[72:75], v[216:219]\n"
"v_xor_b32 v109, 0x8000, v109\n"
"v_lshl_or_b32 v109, v109, 16, v109\n"
"v_mfma_f32_16x16x32_bf16 v[220:223], v[20:23], v[76:79], v[220:223]\n"
"v_cndmask_b32 v109, 0, v109, s[88:89]\n"
"v_accvgpr_write_b32 a164, v109\n"
"v_mfma_f32_16x16x32_bf16 v[224:227], v[24:27], v[64:67], v[224:227]\n"
"v_accvgpr_write_b32 a165, v109\n"
"v_accvgpr_write_b32 a166, v109\n"
"v_mfma_f32_16x16x32_bf16 v[228:231], v[24:27], v[68:71], v[228:231]\n"
"v_accvgpr_write_b32 a167, v109\n"
"v_lshlrev_b32 v103, 16, v103\n"
"v_mfma_f32_16x16x32_bf16 v[232:235], v[24:27], v[72:75], v[232:235]\n"
"v_rcp_f32 v103, v103\n"
"v_mfma_f32_16x16x32_bf16 v[236:239], v[24:27], v[76:79], v[236:239]\n"
"v_mul_f32 v103, s81, v103\n"
"v_lshrrev_b32 v109, 16, v107\n"
"v_mfma_f32_16x16x32_bf16 v[240:243], v[28:31], v[64:67], v[240:243]\n"
"v_xor_b32 v109, 0x8000, v109\n"
"v_lshl_or_b32 v109, v109, 16, v109\n"
"v_mfma_f32_16x16x32_bf16 v[244:247], v[28:31], v[68:71], v[244:247]\n"
"v_cndmask_b32 v109, 0, v109, s[88:89]\n"
"v_accvgpr_write_b32 a168, v109\n"
"v_mfma_f32_16x16x32_bf16 v[248:251], v[28:31], v[72:75], v[248:251]\n"
"v_accvgpr_write_b32 a169, v109\n"
"v_accvgpr_write_b32 a170, v109\n"
"v_accvgpr_write_b32 a171, v109\n"
"v_lshlrev_b32 v107, 16, v107\n"
"v_rcp_f32 v107, v107\n"
"s_nop 0\n"
"v_mul_f32 v107, s81, v107\n"
"v_lshrrev_b32 v109, 16, v111\n"
"v_xor_b32 v109, 0x8000, v109\n"
"v_lshl_or_b32 v109, v109, 16, v109\n"
"v_cndmask_b32 v109, 0, v109, s[88:89]\n"
"v_accvgpr_write_b32 a172, v109\n"
"v_accvgpr_write_b32 a173, v109\n"
"v_accvgpr_write_b32 a174, v109\n"
"v_accvgpr_write_b32 a175, v109\n"
"v_lshlrev_b32 v111, 16, v111\n"
"v_rcp_f32 v111, v111\n"
"s_nop 0\n"
"v_mul_f32 v111, s81, v111\n"
"v_mfma_f32_16x16x32_bf16 v[252:255], v[28:31], v[76:79], v[252:255]\n"
"s_waitcnt vmcnt(6) lgkmcnt(0)\n"
"s_barrier\n"
"v_mfma_f32_16x16x32_bf16 v[128:131], v[32:35], v[80:83], v[128:131]\n"
"ds_read_b128 a[228:231], v119 offset:0\n"
"v_add_u32 v112, 49152, v112\n"
"v_mfma_f32_16x16x32_bf16 v[144:147], v[36:39], v[80:83], v[144:147]\n"
"s_add_u32 m0, s72, 65536\n"
"ds_read_b128 a[232:235], v119 offset:64\n"
"v_add_u32 v113, 49152, v113\n"
"v_mfma_f32_16x16x32_bf16 v[160:163], v[40:43], v[80:83], v[160:163]\n"
"s_mov_b32 s75, 17408\n"
"ds_read_b128 a[236:239], v119 offset:128\n"
"buffer_load_dwordx4 v122, s[52:55], s75 offen offset:0 lds\n"
"v_mfma_f32_16x16x32_bf16 v[176:179], v[44:47], v[80:83], v[176:179]\n"
"ds_read_b128 a[240:243], v119 offset:192\n"
"v_add_u32 v120, 4352, v125\n"
"v_mfma_f32_16x16x32_bf16 v[192:195], v[48:51], v[80:83], v[192:195]\n"
"ds_read_b128 a[64:67], v114 offset:0\n"
"v_mfma_f32_16x16x32_bf16 v[208:211], v[52:55], v[80:83], v[208:211]\n"
"s_mov_b32 s75, 16512\n"
"ds_read_b128 a[68:71], v115 offset:0\n"
"buffer_load_dwordx4 v122, s[52:55], s75 offen offset:1024 lds\n"
"v_mfma_f32_16x16x32_bf16 v[224:227], v[56:59], v[80:83], v[224:227]\n"
"ds_read_b128 a[72:75], v116 offset:0\n"
"v_mfma_f32_16x16x32_bf16 v[240:243], v[60:63], v[80:83], v[240:243]\n"
"s_add_u32 m0, s72, 73728\n"
"ds_read_b128 a[76:79], v117 offset:0\n"
"v_mfma_f32_16x16x32_bf16 v[128:131], a[128:131], a[160:163], v[128:131]\n"
"s_mov_b32 s75, 25600\n"
"ds_read_b128 a[80:83], v114 offset:4096\n"
"buffer_load_dwordx4 v122, s[52:55], s75 offen offset:0 lds\n"
"v_mfma_f32_16x16x32_bf16 v[144:147], a[132:135], a[160:163], v[144:147]\n"
"ds_read_b128 a[84:87], v115 offset:4096\n"
"v_mfma_f32_16x16x32_bf16 v[160:163], a[136:139], a[160:163], v[160:163]\n"
"ds_read_b128 a[88:91], v116 offset:4096\n"
"v_mfma_f32_16x16x32_bf16 v[176:179], a[140:143], a[160:163], v[176:179]\n"
"s_mov_b32 s75, 24704\n"
"ds_read_b128 a[92:95], v117 offset:4096\n"
"buffer_load_dwordx4 v122, s[52:55], s75 offen offset:1024 lds\n"
"v_mfma_f32_16x16x32_bf16 v[192:195], a[144:147], a[160:163], v[192:195]\n"
"ds_read_b128 a[96:99], v114 offset:8192\n"
"v_mfma_f32_16x16x32_bf16 v[208:211], a[148:151], a[160:163], v[208:211]\n"
"ds_read_b128 a[100:103], v115 offset:8192\n"
"v_mfma_f32_16x16x32_bf16 v[224:227], a[152:155], a[160:163], v[224:227]\n"
"ds_read_b128 a[104:107], v116 offset:8192\n"
"v_mfma_f32_16x16x32_bf16 v[240:243], a[156:159], a[160:163], v[240:243]\n"
"ds_read_b128 a[108:111], v117 offset:8192\n"
"v_mfma_f32_16x16x32_bf16 v[132:135], v[32:35], v[84:87], v[132:135]\n"
"ds_read_b128 a[112:115], v114 offset:12288\n"
"v_mfma_f32_16x16x32_bf16 v[148:151], v[36:39], v[84:87], v[148:151]\n"
"ds_read_b128 a[116:119], v115 offset:12288\n"
"v_mfma_f32_16x16x32_bf16 v[164:167], v[40:43], v[84:87], v[164:167]\n"
"ds_read_b128 a[120:123], v116 offset:12288\n"
"v_mul_f32 v128, v128, v99\n"
"v_mfma_f32_16x16x32_bf16 v[180:183], v[44:47], v[84:87], v[180:183]\n"
"ds_read_b128 a[124:127], v117 offset:12288\n"
"v_mul_f32 v129, v129, v99\n"
"v_mfma_f32_16x16x32_bf16 v[196:199], v[48:51], v[84:87], v[196:199]\n"
"v_mul_f32 v130, v130, v99\n"
"v_mul_f32 v131, v131, v99\n"
"v_mfma_f32_16x16x32_bf16 v[212:215], v[52:55], v[84:87], v[212:215]\n"
"v_cvt_pk_bf16_f32 v128, v128, v129\n"
"v_cvt_pk_bf16_f32 v129, v130, v131\n"
"v_mfma_f32_16x16x32_bf16 v[228:231], v[56:59], v[84:87], v[228:231]\n"
"v_mul_f32 v144, v144, v99\n"
"v_mul_f32 v145, v145, v99\n"
"v_mfma_f32_16x16x32_bf16 v[244:247], v[60:63], v[84:87], v[244:247]\n"
"v_mul_f32 v146, v146, v99\n"
"v_mul_f32 v147, v147, v99\n"
"v_mfma_f32_16x16x32_bf16 v[132:135], a[128:131], a[164:167], v[132:135]\n"
"v_cvt_pk_bf16_f32 v144, v144, v145\n"
"v_cvt_pk_bf16_f32 v145, v146, v147\n"
"v_mfma_f32_16x16x32_bf16 v[148:151], a[132:135], a[164:167], v[148:151]\n"
"v_mul_f32 v160, v160, v99\n"
"v_mfma_f32_16x16x32_bf16 v[164:167], a[136:139], a[164:167], v[164:167]\n"
"ds_write2_b64 v125, v[128:129], v[144:145] offset0:0 offset1:4\n"
"v_mul_f32 v161, v161, v99\n"
"v_mfma_f32_16x16x32_bf16 v[180:183], a[140:143], a[164:167], v[180:183]\n"
"v_mul_f32 v162, v162, v99\n"
"v_mul_f32 v163, v163, v99\n"
"v_mfma_f32_16x16x32_bf16 v[196:199], a[144:147], a[164:167], v[196:199]\n"
"v_cvt_pk_bf16_f32 v160, v160, v161\n"
"v_cvt_pk_bf16_f32 v161, v162, v163\n"
"v_mfma_f32_16x16x32_bf16 v[212:215], a[148:151], a[164:167], v[212:215]\n"
"v_mul_f32 v176, v176, v99\n"
"v_mul_f32 v177, v177, v99\n"
"v_mfma_f32_16x16x32_bf16 v[228:231], a[152:155], a[164:167], v[228:231]\n"
"v_mul_f32 v178, v178, v99\n"
"v_mul_f32 v179, v179, v99\n"
"v_mfma_f32_16x16x32_bf16 v[244:247], a[156:159], a[164:167], v[244:247]\n"
"v_cvt_pk_bf16_f32 v176, v176, v177\n"
"v_cvt_pk_bf16_f32 v177, v178, v179\n"
"v_mfma_f32_16x16x32_bf16 v[136:139], v[32:35], v[88:91], v[136:139]\n"
"v_mul_f32 v192, v192, v99\n"
"v_mfma_f32_16x16x32_bf16 v[152:155], v[36:39], v[88:91], v[152:155]\n"
"ds_write2_b64 v125, v[160:161], v[176:177] offset0:8 offset1:12\n"
"v_mul_f32 v193, v193, v99\n"
"v_mfma_f32_16x16x32_bf16 v[168:171], v[40:43], v[88:91], v[168:171]\n"
"v_mul_f32 v194, v194, v99\n"
"v_mul_f32 v195, v195, v99\n"
"v_mfma_f32_16x16x32_bf16 v[184:187], v[44:47], v[88:91], v[184:187]\n"
"v_cvt_pk_bf16_f32 v192, v192, v193\n"
"v_cvt_pk_bf16_f32 v193, v194, v195\n"
"v_mfma_f32_16x16x32_bf16 v[200:203], v[48:51], v[88:91], v[200:203]\n"
"v_mul_f32 v208, v208, v99\n"
"v_mul_f32 v209, v209, v99\n"
"v_mfma_f32_16x16x32_bf16 v[216:219], v[52:55], v[88:91], v[216:219]\n"
"v_mul_f32 v210, v210, v99\n"
"v_mul_f32 v211, v211, v99\n"
"v_mfma_f32_16x16x32_bf16 v[232:235], v[56:59], v[88:91], v[232:235]\n"
"v_cvt_pk_bf16_f32 v208, v208, v209\n"
"v_cvt_pk_bf16_f32 v209, v210, v211\n"
"v_mfma_f32_16x16x32_bf16 v[248:251], v[60:63], v[88:91], v[248:251]\n"
"v_mul_f32 v224, v224, v99\n"
"v_mfma_f32_16x16x32_bf16 v[136:139], a[128:131], a[168:171], v[136:139]\n"
"ds_write2_b64 v125, v[192:193], v[208:209] offset0:16 offset1:20\n"
"v_mul_f32 v225, v225, v99\n"
"v_mfma_f32_16x16x32_bf16 v[152:155], a[132:135], a[168:171], v[152:155]\n"
"v_mul_f32 v226, v226, v99\n"
"v_mul_f32 v227, v227, v99\n"
"v_mfma_f32_16x16x32_bf16 v[168:171], a[136:139], a[168:171], v[168:171]\n"
"v_cvt_pk_bf16_f32 v224, v224, v225\n"
"v_cvt_pk_bf16_f32 v225, v226, v227\n"
"v_mfma_f32_16x16x32_bf16 v[184:187], a[140:143], a[168:171], v[184:187]\n"
"v_mul_f32 v240, v240, v99\n"
"v_mul_f32 v241, v241, v99\n"
"v_mfma_f32_16x16x32_bf16 v[200:203], a[144:147], a[168:171], v[200:203]\n"
"v_mul_f32 v242, v242, v99\n"
"v_mul_f32 v243, v243, v99\n"
"v_mfma_f32_16x16x32_bf16 v[216:219], a[148:151], a[168:171], v[216:219]\n"
"v_cvt_pk_bf16_f32 v240, v240, v241\n"
"v_cvt_pk_bf16_f32 v241, v242, v243\n"
"v_mfma_f32_16x16x32_bf16 v[232:235], a[152:155], a[168:171], v[232:235]\n"
"ds_write2_b64 v125, v[224:225], v[240:241] offset0:24 offset1:28\n"
"ds_read_b128 a[0:3], v126 offset:0\n"
"v_mfma_f32_16x16x32_bf16 v[248:251], a[156:159], a[168:171], v[248:251]\n"
"ds_read_b128 a[4:7], v126 offset:64\n"
"ds_read_b128 a[8:11], v126 offset:128\n"
"v_mfma_f32_16x16x32_bf16 v[140:143], v[32:35], v[92:95], v[140:143]\n"
"ds_read_b128 a[12:15], v126 offset:192\n"
"v_mul_f32 v132, v132, v103\n"
"v_mfma_f32_16x16x32_bf16 v[156:159], v[36:39], v[92:95], v[156:159]\n"
"v_mul_f32 v133, v133, v103\n"
"v_mul_f32 v134, v134, v103\n"
"v_mfma_f32_16x16x32_bf16 v[172:175], v[40:43], v[92:95], v[172:175]\n"
"v_mul_f32 v135, v135, v103\n"
"v_cvt_pk_bf16_f32 v132, v132, v133\n"
"v_mfma_f32_16x16x32_bf16 v[188:191], v[44:47], v[92:95], v[188:191]\n"
"v_cvt_pk_bf16_f32 v133, v134, v135\n"
"v_mul_f32 v148, v148, v103\n"
"v_mfma_f32_16x16x32_bf16 v[204:207], v[48:51], v[92:95], v[204:207]\n"
"v_mul_f32 v149, v149, v103\n"
"v_mul_f32 v150, v150, v103\n"
"v_mfma_f32_16x16x32_bf16 v[220:223], v[52:55], v[92:95], v[220:223]\n"
"v_mul_f32 v151, v151, v103\n"
"v_cvt_pk_bf16_f32 v148, v148, v149\n"
"v_mfma_f32_16x16x32_bf16 v[236:239], v[56:59], v[92:95], v[236:239]\n"
"v_cvt_pk_bf16_f32 v149, v150, v151\n"
"v_mul_f32 v164, v164, v103\n"
"v_mfma_f32_16x16x32_bf16 v[252:255], v[60:63], v[92:95], v[252:255]\n"
"ds_write2_b64 v120, v[132:133], v[148:149] offset0:0 offset1:4\n"
"v_mul_f32 v165, v165, v103\n"
"v_mfma_f32_16x16x32_bf16 v[140:143], a[128:131], a[172:175], v[140:143]\n"
"v_mul_f32 v166, v166, v103\n"
"v_mul_f32 v167, v167, v103\n"
"v_mfma_f32_16x16x32_bf16 v[156:159], a[132:135], a[172:175], v[156:159]\n"
"v_cvt_pk_bf16_f32 v164, v164, v165\n"
"v_cvt_pk_bf16_f32 v165, v166, v167\n"
"v_mfma_f32_16x16x32_bf16 v[172:175], a[136:139], a[172:175], v[172:175]\n"
"v_mul_f32 v180, v180, v103\n"
"v_mul_f32 v181, v181, v103\n"
"v_mfma_f32_16x16x32_bf16 v[188:191], a[140:143], a[172:175], v[188:191]\n"
"v_mul_f32 v182, v182, v103\n"
"v_mul_f32 v183, v183, v103\n"
"v_mfma_f32_16x16x32_bf16 v[204:207], a[144:147], a[172:175], v[204:207]\n"
"v_cvt_pk_bf16_f32 v180, v180, v181\n"
"v_cvt_pk_bf16_f32 v181, v182, v183\n"
"v_mfma_f32_16x16x32_bf16 v[220:223], a[148:151], a[172:175], v[220:223]\n"
"v_mul_f32 v196, v196, v103\n"
"v_mfma_f32_16x16x32_bf16 v[236:239], a[152:155], a[172:175], v[236:239]\n"
"ds_write2_b64 v120, v[164:165], v[180:181] offset0:8 offset1:12\n"
"v_mul_f32 v197, v197, v103\n"
"v_mfma_f32_16x16x32_bf16 v[252:255], a[156:159], a[172:175], v[252:255]\n"
"v_mul_f32 v198, v198, v103\n"
"v_mul_f32 v199, v199, v103\n"
"v_cvt_pk_bf16_f32 v196, v196, v197\n"
"v_cvt_pk_bf16_f32 v197, v198, v199\n"
"v_mul_f32 v212, v212, v103\n"
"v_mul_f32 v213, v213, v103\n"
"v_mul_f32 v214, v214, v103\n"
"v_mul_f32 v215, v215, v103\n"
"v_cvt_pk_bf16_f32 v212, v212, v213\n"
"v_cvt_pk_bf16_f32 v213, v214, v215\n"
"v_mul_f32 v228, v228, v103\n"
"ds_write2_b64 v120, v[196:197], v[212:213] offset0:16 offset1:20\n"
"v_mul_f32 v229, v229, v103\n"
"v_mul_f32 v230, v230, v103\n"
"v_mul_f32 v231, v231, v103\n"
"v_cvt_pk_bf16_f32 v228, v228, v229\n"
"v_cvt_pk_bf16_f32 v229, v230, v231\n"
"v_mul_f32 v244, v244, v103\n"
"v_mul_f32 v245, v245, v103\n"
"v_mul_f32 v246, v246, v103\n"
"v_mul_f32 v247, v247, v103\n"
"v_cvt_pk_bf16_f32 v244, v244, v245\n"
"v_cvt_pk_bf16_f32 v245, v246, v247\n"
"ds_write2_b64 v120, v[228:229], v[244:245] offset0:24 offset1:28\n"
"ds_read_b128 a[16:19], v126 offset:4352\n"
"ds_read_b128 a[20:23], v126 offset:4416\n"
"ds_read_b128 a[24:27], v126 offset:4480\n"
"ds_read_b128 a[28:31], v126 offset:4544\n"
"s_waitcnt lgkmcnt(8)\n"
"v_mfma_f32_16x16x32_bf16 v[96:99], a[228:231], a[0:3], 0\n"
"v_mul_f32 v136, v136, v107\n"
"v_mul_f32 v137, v137, v107\n"
"v_mfma_f32_16x16x32_bf16 v[96:99], a[232:235], a[4:7], v[96:99]\n"
"v_mul_f32 v138, v138, v107\n"
"v_mul_f32 v139, v139, v107\n"
"v_mfma_f32_16x16x32_bf16 v[96:99], a[236:239], a[8:11], v[96:99]\n"
"v_cvt_pk_bf16_f32 v136, v136, v137\n"
"v_cvt_pk_bf16_f32 v137, v138, v139\n"
"v_mfma_f32_16x16x32_bf16 v[96:99], a[240:243], a[12:15], v[96:99]\n"
"v_mul_f32 v152, v152, v107\n"
"v_mul_f32 v153, v153, v107\n"
"v_mul_f32 v154, v154, v107\n"
"v_mul_f32 v155, v155, v107\n"
"v_cvt_pk_bf16_f32 v152, v152, v153\n"
"v_cvt_pk_bf16_f32 v153, v154, v155\n"
"v_mul_f32 v168, v168, v107\n"
"ds_write2_b64 v125, v[136:137], v[152:153] offset0:0 offset1:4\n"
"v_mul_f32 v169, v169, v107\n"
"v_mul_f32 v170, v170, v107\n"
"s_waitcnt lgkmcnt(1)\n"
"v_sub_f32 v96, 0, v96\n"
"v_sub_f32 v97, 0, v97\n"
"v_sub_f32 v98, 0, v98\n"
"v_sub_f32 v99, 0, v99\n"
"v_mfma_f32_16x16x32_bf16 v[100:103], a[228:231], a[16:19], 0\n"
"v_mul_f32 v171, v171, v107\n"
"v_cvt_pk_bf16_f32 v168, v168, v169\n"
"v_mfma_f32_16x16x32_bf16 v[100:103], a[232:235], a[20:23], v[100:103]\n"
"v_cvt_pk_bf16_f32 v169, v170, v171\n"
"v_mul_f32 v184, v184, v107\n"
"v_mfma_f32_16x16x32_bf16 v[100:103], a[236:239], a[24:27], v[100:103]\n"
"v_mul_f32 v185, v185, v107\n"
"v_mul_f32 v186, v186, v107\n"
"v_mfma_f32_16x16x32_bf16 v[100:103], a[240:243], a[28:31], v[100:103]\n"
"v_mul_f32 v187, v187, v107\n"
"v_cvt_pk_bf16_f32 v184, v184, v185\n"
"v_mfma_f32_16x16x32_bf16 v[0:3], a[64:67], a[0:3], v[96:99]\n"
"v_cvt_pk_bf16_f32 v185, v186, v187\n"
"v_mul_f32 v200, v200, v107\n"
"v_mfma_f32_16x16x32_bf16 v[16:19], a[80:83], a[0:3], v[96:99]\n"
"ds_write2_b64 v125, v[168:169], v[184:185] offset0:8 offset1:12\n"
"v_mul_f32 v201, v201, v107\n"
"v_mfma_f32_16x16x32_bf16 v[0:3], a[68:71], a[4:7], v[0:3]\n"
"v_mul_f32 v202, v202, v107\n"
"v_mul_f32 v203, v203, v107\n"
"v_mfma_f32_16x16x32_bf16 v[16:19], a[84:87], a[4:7], v[16:19]\n"
"v_cvt_pk_bf16_f32 v200, v200, v201\n"
"v_cvt_pk_bf16_f32 v201, v202, v203\n"
"v_mfma_f32_16x16x32_bf16 v[0:3], a[72:75], a[8:11], v[0:3]\n"
"v_mul_f32 v216, v216, v107\n"
"v_mul_f32 v217, v217, v107\n"
"v_mfma_f32_16x16x32_bf16 v[16:19], a[88:91], a[8:11], v[16:19]\n"
"v_mul_f32 v218, v218, v107\n"
"v_mul_f32 v219, v219, v107\n"
"v_mfma_f32_16x16x32_bf16 v[0:3], a[76:79], a[12:15], v[0:3]\n"
"v_cvt_pk_bf16_f32 v216, v216, v217\n"
"v_cvt_pk_bf16_f32 v217, v218, v219\n"
"v_mfma_f32_16x16x32_bf16 v[16:19], a[92:95], a[12:15], v[16:19]\n"
"v_mul_f32 v232, v232, v107\n"
"v_mfma_f32_16x16x32_bf16 v[32:35], a[96:99], a[0:3], v[96:99]\n"
"ds_write2_b64 v125, v[200:201], v[216:217] offset0:16 offset1:20\n"
"v_mul_f32 v233, v233, v107\n"
"v_mfma_f32_16x16x32_bf16 v[48:51], a[112:115], a[0:3], v[96:99]\n"
"v_mul_f32 v234, v234, v107\n"
"v_mul_f32 v235, v235, v107\n"
"v_mfma_f32_16x16x32_bf16 v[32:35], a[100:103], a[4:7], v[32:35]\n"
"v_cvt_pk_bf16_f32 v232, v232, v233\n"
"v_cvt_pk_bf16_f32 v233, v234, v235\n"
"v_mfma_f32_16x16x32_bf16 v[48:51], a[116:119], a[4:7], v[48:51]\n"
"v_mul_f32 v248, v248, v107\n"
"v_mul_f32 v249, v249, v107\n"
"v_mfma_f32_16x16x32_bf16 v[32:35], a[104:107], a[8:11], v[32:35]\n"
"v_mul_f32 v250, v250, v107\n"
"v_mul_f32 v251, v251, v107\n"
"v_mfma_f32_16x16x32_bf16 v[48:51], a[120:123], a[8:11], v[48:51]\n"
"v_cvt_pk_bf16_f32 v248, v248, v249\n"
"v_cvt_pk_bf16_f32 v249, v250, v251\n"
"v_mfma_f32_16x16x32_bf16 v[32:35], a[108:111], a[12:15], v[32:35]\n"
"ds_write2_b64 v125, v[232:233], v[248:249] offset0:24 offset1:28\n"
"ds_read_b128 a[32:35], v126 offset:0\n"
"v_mfma_f32_16x16x32_bf16 v[48:51], a[124:127], a[12:15], v[48:51]\n"
"ds_read_b128 a[36:39], v126 offset:64\n"
"ds_read_b128 a[40:43], v126 offset:128\n"
"ds_read_b128 a[44:47], v126 offset:192\n"
"v_sub_f32 v100, 0, v100\n"
"v_sub_f32 v101, 0, v101\n"
"v_sub_f32 v102, 0, v102\n"
"v_sub_f32 v103, 0, v103\n"
"v_mul_f32 v140, v140, v111\n"
"v_mul_f32 v141, v141, v111\n"
"v_mul_f32 v142, v142, v111\n"
"v_mul_f32 v143, v143, v111\n"
"v_cvt_pk_bf16_f32 v140, v140, v141\n"
"v_cvt_pk_bf16_f32 v141, v142, v143\n"
"v_mul_f32 v156, v156, v111\n"
"v_mul_f32 v157, v157, v111\n"
"v_mul_f32 v158, v158, v111\n"
"v_mul_f32 v159, v159, v111\n"
"v_cvt_pk_bf16_f32 v156, v156, v157\n"
"v_cvt_pk_bf16_f32 v157, v158, v159\n"
"v_mul_f32 v172, v172, v111\n"
"ds_write2_b64 v120, v[140:141], v[156:157] offset0:0 offset1:4\n"
"v_mul_f32 v173, v173, v111\n"
"v_mul_f32 v174, v174, v111\n"
"s_waitcnt vmcnt(4) lgkmcnt(1)\n"
"s_barrier\n"
"v_mfma_f32_16x16x32_bf16 v[104:107], a[228:231], a[32:35], 0\n"
"v_mul_f32 v175, v175, v111\n"
"v_cvt_pk_bf16_f32 v172, v172, v173\n"
"v_mfma_f32_16x16x32_bf16 v[104:107], a[232:235], a[36:39], v[104:107]\n"
"v_cvt_pk_bf16_f32 v173, v174, v175\n"
"v_mul_f32 v188, v188, v111\n"
"v_mfma_f32_16x16x32_bf16 v[104:107], a[236:239], a[40:43], v[104:107]\n"
"v_mul_f32 v189, v189, v111\n"
"v_mul_f32 v190, v190, v111\n"
"v_mfma_f32_16x16x32_bf16 v[104:107], a[240:243], a[44:47], v[104:107]\n"
"v_mul_f32 v191, v191, v111\n"
"v_cvt_pk_bf16_f32 v188, v188, v189\n"
"v_mfma_f32_16x16x32_bf16 v[4:7], a[64:67], a[16:19], v[100:103]\n"
"v_cvt_pk_bf16_f32 v189, v190, v191\n"
"v_mul_f32 v204, v204, v111\n"
"v_mfma_f32_16x16x32_bf16 v[20:23], a[80:83], a[16:19], v[100:103]\n"
"ds_write2_b64 v120, v[172:173], v[188:189] offset0:8 offset1:12\n"
"v_mul_f32 v205, v205, v111\n"
"v_mfma_f32_16x16x32_bf16 v[4:7], a[68:71], a[20:23], v[4:7]\n"
"v_mul_f32 v206, v206, v111\n"
"v_mul_f32 v207, v207, v111\n"
"v_mfma_f32_16x16x32_bf16 v[20:23], a[84:87], a[20:23], v[20:23]\n"
"v_cvt_pk_bf16_f32 v204, v204, v205\n"
"v_cvt_pk_bf16_f32 v205, v206, v207\n"
"v_mfma_f32_16x16x32_bf16 v[4:7], a[72:75], a[24:27], v[4:7]\n"
"v_mul_f32 v220, v220, v111\n"
"v_mul_f32 v221, v221, v111\n"
"v_mfma_f32_16x16x32_bf16 v[20:23], a[88:91], a[24:27], v[20:23]\n"
"v_mul_f32 v222, v222, v111\n"
"v_mul_f32 v223, v223, v111\n"
"v_mfma_f32_16x16x32_bf16 v[4:7], a[76:79], a[28:31], v[4:7]\n"
"v_cvt_pk_bf16_f32 v220, v220, v221\n"
"v_cvt_pk_bf16_f32 v221, v222, v223\n"
"v_mfma_f32_16x16x32_bf16 v[20:23], a[92:95], a[28:31], v[20:23]\n"
"v_mul_f32 v236, v236, v111\n"
"v_mfma_f32_16x16x32_bf16 v[36:39], a[96:99], a[16:19], v[100:103]\n"
"ds_write2_b64 v120, v[204:205], v[220:221] offset0:16 offset1:20\n"
"v_mul_f32 v237, v237, v111\n"
"v_mfma_f32_16x16x32_bf16 v[52:55], a[112:115], a[16:19], v[100:103]\n"
"v_mul_f32 v238, v238, v111\n"
"v_mul_f32 v239, v239, v111\n"
"v_mfma_f32_16x16x32_bf16 v[36:39], a[100:103], a[20:23], v[36:39]\n"
"v_cvt_pk_bf16_f32 v236, v236, v237\n"
"v_cvt_pk_bf16_f32 v237, v238, v239\n"
"v_mfma_f32_16x16x32_bf16 v[52:55], a[116:119], a[20:23], v[52:55]\n"
"v_mul_f32 v252, v252, v111\n"
"v_mul_f32 v253, v253, v111\n"
"v_mfma_f32_16x16x32_bf16 v[36:39], a[104:107], a[24:27], v[36:39]\n"
"v_mul_f32 v254, v254, v111\n"
"v_mul_f32 v255, v255, v111\n"
"v_mfma_f32_16x16x32_bf16 v[52:55], a[120:123], a[24:27], v[52:55]\n"
"v_cvt_pk_bf16_f32 v252, v252, v253\n"
"v_cvt_pk_bf16_f32 v253, v254, v255\n"
"v_mfma_f32_16x16x32_bf16 v[36:39], a[108:111], a[28:31], v[36:39]\n"
"ds_write2_b64 v120, v[236:237], v[252:253] offset0:24 offset1:28\n"
"ds_read_b128 a[48:51], v126 offset:4352\n"
"v_mfma_f32_16x16x32_bf16 v[52:55], a[124:127], a[28:31], v[52:55]\n"
"ds_read_b128 a[52:55], v126 offset:4416\n"
"ds_read_b128 a[56:59], v126 offset:4480\n"
"ds_read_b128 a[60:63], v126 offset:4544\n"
"v_sub_f32 v104, 0, v104\n"
"v_sub_f32 v105, 0, v105\n"
"v_sub_f32 v106, 0, v106\n"
"v_sub_f32 v107, 0, v107\n"
"v_exp_f32 v0, v0\n"
"v_exp_f32 v1, v1\n"
"v_exp_f32 v2, v2\n"
"v_exp_f32 v3, v3\n"
"v_cvt_pk_bf16_f32 v64, v0, v1\n"
"v_cvt_pk_bf16_f32 v65, v2, v3\n"
"v_exp_f32 v16, v16\n"
"v_exp_f32 v17, v17\n"
"v_exp_f32 v18, v18\n"
"v_exp_f32 v19, v19\n"
"v_cvt_pk_bf16_f32 v66, v16, v17\n"
"v_cvt_pk_bf16_f32 v67, v18, v19\n"
"s_waitcnt lgkmcnt(0)\n"
"v_mfma_f32_16x16x32_bf16 v[108:111], a[228:231], a[48:51], 0\n"
"v_exp_f32 v4, v4\n"
"v_mfma_f32_16x16x32_bf16 v[108:111], a[232:235], a[52:55], v[108:111]\n"
"ds_read_b64_tr_b16 a[128:129], v112 offset:0\n"
"ds_read_b64_tr_b16 a[130:131], v112 offset:256\n"
"v_mfma_f32_16x16x32_bf16 v[108:111], a[236:239], a[56:59], v[108:111]\n"
"v_exp_f32 v5, v5\n"
"v_mfma_f32_16x16x32_bf16 v[108:111], a[240:243], a[60:63], v[108:111]\n"
"ds_read_b64_tr_b16 a[132:133], v113 offset:0\n"
"ds_read_b64_tr_b16 a[134:135], v113 offset:256\n"
"v_mfma_f32_16x16x32_bf16 v[8:11], a[64:67], a[32:35], v[104:107]\n"
"v_exp_f32 v6, v6\n"
"v_mfma_f32_16x16x32_bf16 v[24:27], a[80:83], a[32:35], v[104:107]\n"
"ds_read_b64_tr_b16 a[136:137], v112 offset:512\n"
"ds_read_b64_tr_b16 a[138:139], v112 offset:768\n"
"v_mfma_f32_16x16x32_bf16 v[8:11], a[68:71], a[36:39], v[8:11]\n"
"v_exp_f32 v7, v7\n"
"v_mfma_f32_16x16x32_bf16 v[24:27], a[84:87], a[36:39], v[24:27]\n"
"ds_read_b64_tr_b16 a[140:141], v113 offset:512\n"
"ds_read_b64_tr_b16 a[142:143], v113 offset:768\n"
"v_mfma_f32_16x16x32_bf16 v[8:11], a[72:75], a[40:43], v[8:11]\n"
"v_cvt_pk_bf16_f32 v72, v4, v5\n"
"v_cvt_pk_bf16_f32 v73, v6, v7\n"
"v_mfma_f32_16x16x32_bf16 v[24:27], a[88:91], a[40:43], v[24:27]\n"
"ds_read_b64_tr_b16 a[144:145], v112 offset:1024\n"
"ds_read_b64_tr_b16 a[146:147], v112 offset:1280\n"
"v_mfma_f32_16x16x32_bf16 v[8:11], a[76:79], a[44:47], v[8:11]\n"
"v_exp_f32 v20, v20\n"
"v_mfma_f32_16x16x32_bf16 v[24:27], a[92:95], a[44:47], v[24:27]\n"
"ds_read_b64_tr_b16 a[148:149], v113 offset:1024\n"
"ds_read_b64_tr_b16 a[150:151], v113 offset:1280\n"
"v_mfma_f32_16x16x32_bf16 v[40:43], a[96:99], a[32:35], v[104:107]\n"
"v_exp_f32 v21, v21\n"
"v_mfma_f32_16x16x32_bf16 v[56:59], a[112:115], a[32:35], v[104:107]\n"
"ds_read_b64_tr_b16 a[152:153], v112 offset:1536\n"
"ds_read_b64_tr_b16 a[154:155], v112 offset:1792\n"
"v_mfma_f32_16x16x32_bf16 v[40:43], a[100:103], a[36:39], v[40:43]\n"
"v_exp_f32 v22, v22\n"
"v_mfma_f32_16x16x32_bf16 v[56:59], a[116:119], a[36:39], v[56:59]\n"
"ds_read_b64_tr_b16 a[156:157], v113 offset:1536\n"
"ds_read_b64_tr_b16 a[158:159], v113 offset:1792\n"
"v_mfma_f32_16x16x32_bf16 v[40:43], a[104:107], a[40:43], v[40:43]\n"
"v_exp_f32 v23, v23\n"
"v_mfma_f32_16x16x32_bf16 v[56:59], a[120:123], a[40:43], v[56:59]\n"
"v_cvt_pk_bf16_f32 v74, v20, v21\n"
"v_cvt_pk_bf16_f32 v75, v22, v23\n"
"v_mfma_f32_16x16x32_bf16 v[40:43], a[108:111], a[44:47], v[40:43]\n"
"v_sub_f32 v108, 0, v108\n"
"v_sub_f32 v109, 0, v109\n"
"v_mfma_f32_16x16x32_bf16 v[56:59], a[124:127], a[44:47], v[56:59]\n"
"v_sub_f32 v110, 0, v110\n"
"v_sub_f32 v111, 0, v111\n"
"s_nop 0\n"
"s_nop 0\n"
"v_mfma_f32_16x16x32_bf16 v[12:15], a[64:67], a[48:51], v[108:111]\n"
"v_exp_f32 v8, v8\n"
"v_mfma_f32_16x16x32_bf16 v[28:31], a[80:83], a[48:51], v[108:111]\n"
"v_exp_f32 v9, v9\n"
"v_mfma_f32_16x16x32_bf16 v[12:15], a[68:71], a[52:55], v[12:15]\n"
"v_exp_f32 v10, v10\n"
"v_mfma_f32_16x16x32_bf16 v[28:31], a[84:87], a[52:55], v[28:31]\n"
"v_exp_f32 v11, v11\n"
"v_mfma_f32_16x16x32_bf16 v[12:15], a[72:75], a[56:59], v[12:15]\n"
"v_cvt_pk_bf16_f32 v80, v8, v9\n"
"v_cvt_pk_bf16_f32 v81, v10, v11\n"
"v_mfma_f32_16x16x32_bf16 v[28:31], a[88:91], a[56:59], v[28:31]\n"
"v_exp_f32 v24, v24\n"
"v_mfma_f32_16x16x32_bf16 v[12:15], a[76:79], a[60:63], v[12:15]\n"
"v_exp_f32 v25, v25\n"
"v_mfma_f32_16x16x32_bf16 v[28:31], a[92:95], a[60:63], v[28:31]\n"
"v_exp_f32 v26, v26\n"
"v_mfma_f32_16x16x32_bf16 v[44:47], a[96:99], a[48:51], v[108:111]\n"
"ds_read_b128 a[64:67], v114 offset:16384\n"
"v_mfma_f32_16x16x32_bf16 v[60:63], a[112:115], a[48:51], v[108:111]\n"
"ds_read_b128 a[68:71], v115 offset:16384\n"
"v_mfma_f32_16x16x32_bf16 v[44:47], a[100:103], a[52:55], v[44:47]\n"
"ds_read_b128 a[72:75], v116 offset:16384\n"
"v_mfma_f32_16x16x32_bf16 v[60:63], a[116:119], a[52:55], v[60:63]\n"
"ds_read_b128 a[76:79], v117 offset:16384\n"
"v_mfma_f32_16x16x32_bf16 v[44:47], a[104:107], a[56:59], v[44:47]\n"
"ds_read_b128 a[80:83], v114 offset:20480\n"
"v_mfma_f32_16x16x32_bf16 v[60:63], a[120:123], a[56:59], v[60:63]\n"
"ds_read_b128 a[84:87], v115 offset:20480\n"
"v_mfma_f32_16x16x32_bf16 v[44:47], a[108:111], a[60:63], v[44:47]\n"
"ds_read_b128 a[88:91], v116 offset:20480\n"
"v_mfma_f32_16x16x32_bf16 v[60:63], a[124:127], a[60:63], v[60:63]\n"
"ds_read_b128 a[92:95], v117 offset:20480\n"
"v_exp_f32 v27, v27\n"
"v_cvt_pk_bf16_f32 v82, v24, v25\n"
"v_cvt_pk_bf16_f32 v83, v26, v27\n"
"v_exp_f32 v12, v12\n"
"v_exp_f32 v13, v13\n"
"v_exp_f32 v14, v14\n"
"v_exp_f32 v15, v15\n"
"v_cvt_pk_bf16_f32 v88, v12, v13\n"
"v_cvt_pk_bf16_f32 v89, v14, v15\n"
"v_exp_f32 v28, v28\n"
"v_exp_f32 v29, v29\n"
"v_exp_f32 v30, v30\n"
"v_exp_f32 v31, v31\n"
"v_cvt_pk_bf16_f32 v90, v28, v29\n"
"v_cvt_pk_bf16_f32 v91, v30, v31\n"
"s_waitcnt lgkmcnt(0)\n"
"s_barrier\n"
"v_mfma_f32_16x16x32_bf16 v[0:3], a[64:67], a[0:3], v[96:99]\n"
"s_add_u32 m0, s71, 0\n"
"v_exp_f32 v32, v32\n"
"v_mfma_f32_16x16x32_bf16 v[4:7], a[64:67], a[16:19], v[100:103]\n"
"s_mov_b32 s75, 49152\n"
"buffer_load_dwordx4 v118, s[48:51], s75 offen offset:0 lds\n"
"ds_read_b128 a[96:99], v114 offset:24576\n"
"v_mfma_f32_16x16x32_bf16 v[8:11], a[64:67], a[32:35], v[104:107]\n"
"v_exp_f32 v33, v33\n"
"v_mfma_f32_16x16x32_bf16 v[12:15], a[64:67], a[48:51], v[108:111]\n"
"ds_read_b128 a[100:103], v115 offset:24576\n"
"v_cvt_pk_bf16_f32 v68, v32, v33\n"
"v_mfma_f32_16x16x32_bf16 v[128:131], a[128:131], v[64:67], 0\n"
"s_add_u32 m0, s71, 4096\n"
"v_exp_f32 v34, v34\n"
"v_mfma_f32_16x16x32_bf16 v[132:135], a[128:131], v[72:75], 0\n"
"s_mov_b32 s75, 53248\n"
"buffer_load_dwordx4 v118, s[48:51], s75 offen offset:0 lds\n"
"ds_read_b128 a[104:107], v116 offset:24576\n"
"v_mfma_f32_16x16x32_bf16 v[136:139], a[128:131], v[80:83], 0\n"
"v_exp_f32 v35, v35\n"
"v_mfma_f32_16x16x32_bf16 v[140:143], a[128:131], v[88:91], 0\n"
"ds_read_b128 a[108:111], v117 offset:24576\n"
"v_cvt_pk_bf16_f32 v69, v34, v35\n"
"v_mfma_f32_16x16x32_bf16 v[0:3], a[68:71], a[4:7], v[0:3]\n"
"s_add_u32 m0, s71, 8192\n"
"v_exp_f32 v36, v36\n"
"v_mfma_f32_16x16x32_bf16 v[4:7], a[68:71], a[20:23], v[4:7]\n"
"s_mov_b32 s75, 57344\n"
"buffer_load_dwordx4 v118, s[48:51], s75 offen offset:0 lds\n"
"ds_read_b128 a[112:115], v114 offset:28672\n"
"v_mfma_f32_16x16x32_bf16 v[8:11], a[68:71], a[36:39], v[8:11]\n"
"v_exp_f32 v37, v37\n"
"v_mfma_f32_16x16x32_bf16 v[12:15], a[68:71], a[52:55], v[12:15]\n"
"ds_read_b128 a[116:119], v115 offset:28672\n"
"v_cvt_pk_bf16_f32 v76, v36, v37\n"
"v_mfma_f32_16x16x32_bf16 v[144:147], a[132:135], v[64:67], 0\n"
"s_add_u32 m0, s71, 12288\n"
"v_exp_f32 v38, v38\n"
"v_mfma_f32_16x16x32_bf16 v[148:151], a[132:135], v[72:75], 0\n"
"s_mov_b32 s75, 61440\n"
"buffer_load_dwordx4 v118, s[48:51], s75 offen offset:0 lds\n"
"ds_read_b128 a[120:123], v116 offset:28672\n"
"v_mfma_f32_16x16x32_bf16 v[152:155], a[132:135], v[80:83], 0\n"
"v_exp_f32 v39, v39\n"
"v_mfma_f32_16x16x32_bf16 v[156:159], a[132:135], v[88:91], 0\n"
"ds_read_b128 a[124:127], v117 offset:28672\n"
"v_cvt_pk_bf16_f32 v77, v38, v39\n"
"v_mfma_f32_16x16x32_bf16 v[0:3], a[72:75], a[8:11], v[0:3]\n"
"s_add_u32 m0, s72, 81920\n"
"v_exp_f32 v40, v40\n"
"v_mfma_f32_16x16x32_bf16 v[4:7], a[72:75], a[24:27], v[4:7]\n"
"s_mov_b32 s75, 33792\n"
"buffer_load_dwordx4 v122, s[52:55], s75 offen offset:0 lds\n"
"ds_read_b64_tr_b16 a[160:161], v112 offset:8192\n"
"v_mfma_f32_16x16x32_bf16 v[8:11], a[72:75], a[40:43], v[8:11]\n"
"v_exp_f32 v41, v41\n"
"v_mfma_f32_16x16x32_bf16 v[12:15], a[72:75], a[56:59], v[12:15]\n"
"ds_read_b64_tr_b16 a[162:163], v112 offset:8448\n"
"v_cvt_pk_bf16_f32 v84, v40, v41\n"
"v_mfma_f32_16x16x32_bf16 v[160:163], a[136:139], v[64:67], 0\n"
"v_exp_f32 v42, v42\n"
"v_mfma_f32_16x16x32_bf16 v[164:167], a[136:139], v[72:75], 0\n"
"s_mov_b32 s75, 32896\n"
"buffer_load_dwordx4 v122, s[52:55], s75 offen offset:1024 lds\n"
"ds_read_b64_tr_b16 a[164:165], v113 offset:8192\n"
"v_mfma_f32_16x16x32_bf16 v[168:171], a[136:139], v[80:83], 0\n"
"v_exp_f32 v43, v43\n"
"v_mfma_f32_16x16x32_bf16 v[172:175], a[136:139], v[88:91], 0\n"
"ds_read_b64_tr_b16 a[166:167], v113 offset:8448\n"
"v_cvt_pk_bf16_f32 v85, v42, v43\n"
"v_mfma_f32_16x16x32_bf16 v[0:3], a[76:79], a[12:15], v[0:3]\n"
"s_add_u32 m0, s72, 90112\n"
"v_exp_f32 v44, v44\n"
"v_mfma_f32_16x16x32_bf16 v[4:7], a[76:79], a[28:31], v[4:7]\n"
"s_mov_b32 s75, 41984\n"
"buffer_load_dwordx4 v122, s[52:55], s75 offen offset:0 lds\n"
"ds_read_b64_tr_b16 a[168:169], v112 offset:8704\n"
"v_mfma_f32_16x16x32_bf16 v[8:11], a[76:79], a[44:47], v[8:11]\n"
"v_exp_f32 v45, v45\n"
"v_mfma_f32_16x16x32_bf16 v[12:15], a[76:79], a[60:63], v[12:15]\n"
"ds_read_b64_tr_b16 a[170:171], v112 offset:8960\n"
"v_cvt_pk_bf16_f32 v92, v44, v45\n"
"v_mfma_f32_16x16x32_bf16 v[176:179], a[140:143], v[64:67], 0\n"
"v_exp_f32 v46, v46\n"
"v_mfma_f32_16x16x32_bf16 v[180:183], a[140:143], v[72:75], 0\n"
"s_mov_b32 s75, 41088\n"
"buffer_load_dwordx4 v122, s[52:55], s75 offen offset:1024 lds\n"
"ds_read_b64_tr_b16 a[172:173], v113 offset:8704\n"
"v_mfma_f32_16x16x32_bf16 v[184:187], a[140:143], v[80:83], 0\n"
"v_exp_f32 v47, v47\n"
"v_mfma_f32_16x16x32_bf16 v[188:191], a[140:143], v[88:91], 0\n"
"ds_read_b64_tr_b16 a[174:175], v113 offset:8960\n"
"v_cvt_pk_bf16_f32 v93, v46, v47\n"
"v_mfma_f32_16x16x32_bf16 v[16:19], a[80:83], a[0:3], v[96:99]\n"
"s_mov_b32 s77, 127\n"
"v_exp_f32 v48, v48\n"
"v_mfma_f32_16x16x32_bf16 v[20:23], a[80:83], a[16:19], v[100:103]\n"
"ds_read_b64_tr_b16 a[176:177], v112 offset:9216\n"
"v_lshrrev_b32 v126, 6, v124\n"
"v_mfma_f32_16x16x32_bf16 v[24:27], a[80:83], a[32:35], v[104:107]\n"
"v_exp_f32 v49, v49\n"
"v_mfma_f32_16x16x32_bf16 v[28:31], a[80:83], a[48:51], v[108:111]\n"
"ds_read_b64_tr_b16 a[178:179], v112 offset:9472\n"
"v_cvt_pk_bf16_f32 v70, v48, v49\n"
"v_mfma_f32_16x16x32_bf16 v[192:195], a[144:147], v[64:67], 0\n"
"v_exp_f32 v50, v50\n"
"v_mfma_f32_16x16x32_bf16 v[196:199], a[144:147], v[72:75], 0\n"
"ds_read_b64_tr_b16 a[180:181], v113 offset:9216\n"
"v_and_b32 v126, 7, v126\n"
"v_mfma_f32_16x16x32_bf16 v[200:203], a[144:147], v[80:83], 0\n"
"s_lshl_b32 s73, s68, 3\n"
"v_exp_f32 v51, v51\n"
"v_mfma_f32_16x16x32_bf16 v[204:207], a[144:147], v[88:91], 0\n"
"ds_read_b64_tr_b16 a[182:183], v113 offset:9472\n"
"v_cvt_pk_bf16_f32 v71, v50, v51\n"
"v_mfma_f32_16x16x32_bf16 v[16:19], a[84:87], a[4:7], v[16:19]\n"
"v_exp_f32 v52, v52\n"
"v_mfma_f32_16x16x32_bf16 v[20:23], a[84:87], a[20:23], v[20:23]\n"
"ds_read_b64_tr_b16 a[184:185], v112 offset:9728\n"
"v_add_u32 v126, s73, v126\n"
"v_mfma_f32_16x16x32_bf16 v[24:27], a[84:87], a[36:39], v[24:27]\n"
"v_exp_f32 v53, v53\n"
"v_mfma_f32_16x16x32_bf16 v[28:31], a[84:87], a[52:55], v[28:31]\n"
"ds_read_b64_tr_b16 a[186:187], v112 offset:9984\n"
"v_cvt_pk_bf16_f32 v78, v52, v53\n"
"v_mfma_f32_16x16x32_bf16 v[208:211], a[148:151], v[64:67], 0\n"
"v_exp_f32 v54, v54\n"
"v_mfma_f32_16x16x32_bf16 v[212:215], a[148:151], v[72:75], 0\n"
"ds_read_b64_tr_b16 a[188:189], v113 offset:9728\n"
"v_mul_u32_u24 v125, 7168, v126\n"
"v_mfma_f32_16x16x32_bf16 v[216:219], a[148:151], v[80:83], 0\n"
"v_exp_f32 v55, v55\n"
"v_mfma_f32_16x16x32_bf16 v[220:223], a[148:151], v[88:91], 0\n"
"ds_read_b64_tr_b16 a[190:191], v113 offset:9984\n"
"v_cvt_pk_bf16_f32 v79, v54, v55\n"
"v_mfma_f32_16x16x32_bf16 v[16:19], a[88:91], a[8:11], v[16:19]\n"
"v_exp_f32 v56, v56\n"
"v_mfma_f32_16x16x32_bf16 v[20:23], a[88:91], a[24:27], v[20:23]\n"
"v_lshlrev_b32 v126, 11, v126\n"
"v_and_or_b32 v126, v122, s77, v126\n"
"v_mfma_f32_16x16x32_bf16 v[24:27], a[88:91], a[40:43], v[24:27]\n"
"v_exp_f32 v57, v57\n"
"v_mfma_f32_16x16x32_bf16 v[28:31], a[88:91], a[56:59], v[28:31]\n"
"v_cvt_pk_bf16_f32 v86, v56, v57\n"
"v_and_or_b32 v125, v122, s77, v125\n"
"v_mfma_f32_16x16x32_bf16 v[224:227], a[152:155], v[64:67], 0\n"
"s_add_u32 m0, s69, 131072\n"
"v_exp_f32 v58, v58\n"
"v_mfma_f32_16x16x32_bf16 v[228:231], a[152:155], v[72:75], 0\n"
"s_mov_b32 s75, 1024\n"
"buffer_load_dwordx4 v125, s[24:27], s75 offen offset:0 nt lds\n"
"v_mfma_f32_16x16x32_bf16 v[232:235], a[152:155], v[80:83], 0\n"
"s_mov_b32 s75, 128\n"
"v_exp_f32 v59, v59\n"
"v_mfma_f32_16x16x32_bf16 v[236:239], a[152:155], v[88:91], 0\n"
"buffer_load_dwordx4 v125, s[24:27], s75 offen offset:1024 nt lds\n"
"v_cvt_pk_bf16_f32 v87, v58, v59\n"
"v_mfma_f32_16x16x32_bf16 v[16:19], a[92:95], a[12:15], v[16:19]\n"
"s_add_u32 m0, s69, 139264\n"
"v_exp_f32 v60, v60\n"
"v_mfma_f32_16x16x32_bf16 v[20:23], a[92:95], a[28:31], v[20:23]\n"
"s_mov_b32 s75, 230400\n"
"buffer_load_dwordx4 v125, s[24:27], s75 offen offset:0 nt lds\n"
"v_mfma_f32_16x16x32_bf16 v[24:27], a[92:95], a[44:47], v[24:27]\n"
"s_mov_b32 s75, 229504\n"
"v_exp_f32 v61, v61\n"
"v_mfma_f32_16x16x32_bf16 v[28:31], a[92:95], a[60:63], v[28:31]\n"
"buffer_load_dwordx4 v125, s[24:27], s75 offen offset:1024 nt lds\n"
"v_cvt_pk_bf16_f32 v94, v60, v61\n"
"v_mfma_f32_16x16x32_bf16 v[240:243], a[156:159], v[64:67], 0\n"
"v_exp_f32 v62, v62\n"
"v_mfma_f32_16x16x32_bf16 v[244:247], a[156:159], v[72:75], 0\n"
"v_mfma_f32_16x16x32_bf16 v[248:251], a[156:159], v[80:83], 0\n"
"v_exp_f32 v63, v63\n"
"v_mfma_f32_16x16x32_bf16 v[252:255], a[156:159], v[88:91], 0\n"
"v_cvt_pk_bf16_f32 v95, v62, v63\n"
"v_mfma_f32_16x16x32_bf16 a[196:199], a[192:195], v[64:67], 0\n"
"v_mfma_f32_16x16x32_bf16 a[200:203], a[192:195], v[72:75], 0\n"
"v_mfma_f32_16x16x32_bf16 a[204:207], a[192:195], v[80:83], 0\n"
"v_mfma_f32_16x16x32_bf16 a[208:211], a[192:195], v[88:91], 0\n"
"s_waitcnt vmcnt(12) lgkmcnt(0)\n"
"s_barrier\n"
"v_mfma_f32_16x16x32_bf16 v[32:35], a[96:99], a[0:3], v[96:99]\n"
"s_add_u32 m0, s71, 16384\n"
"v_exp_f32 v0, v0\n"
"v_mfma_f32_16x16x32_bf16 v[36:39], a[96:99], a[16:19], v[100:103]\n"
"s_mov_b32 s75, 65536\n"
"buffer_load_dwordx4 v118, s[48:51], s75 offen offset:0 lds\n"
"ds_read_b128 a[64:67], v114 offset:32768\n"
"v_mfma_f32_16x16x32_bf16 v[40:43], a[96:99], a[32:35], v[104:107]\n"
"v_exp_f32 v1, v1\n"
"v_mfma_f32_16x16x32_bf16 v[44:47], a[96:99], a[48:51], v[108:111]\n"
"ds_read_b128 a[68:71], v115 offset:32768\n"
"v_cvt_pk_bf16_f32 v64, v0, v1\n"
"v_mfma_f32_16x16x32_bf16 v[128:131], a[160:163], v[68:71], v[128:131]\n"
"s_add_u32 m0, s71, 20480\n"
"v_exp_f32 v2, v2\n"
"v_mfma_f32_16x16x32_bf16 v[132:135], a[160:163], v[76:79], v[132:135]\n"
"s_mov_b32 s75, 69632\n"
"buffer_load_dwordx4 v118, s[48:51], s75 offen offset:0 lds\n"
"ds_read_b128 a[72:75], v116 offset:32768\n"
"v_mfma_f32_16x16x32_bf16 v[136:139], a[160:163], v[84:87], v[136:139]\n"
"v_exp_f32 v3, v3\n"
"v_mfma_f32_16x16x32_bf16 v[140:143], a[160:163], v[92:95], v[140:143]\n"
"ds_read_b128 a[76:79], v117 offset:32768\n"
"v_cvt_pk_bf16_f32 v65, v2, v3\n"
"v_mfma_f32_16x16x32_bf16 v[32:35], a[100:103], a[4:7], v[32:35]\n"
"s_add_u32 m0, s71, 24576\n"
"v_exp_f32 v4, v4\n"
"v_mfma_f32_16x16x32_bf16 v[36:39], a[100:103], a[20:23], v[36:39]\n"
"s_mov_b32 s75, 73728\n"
"buffer_load_dwordx4 v118, s[48:51], s75 offen offset:0 lds\n"
"ds_read_b128 a[80:83], v114 offset:36864\n"
"v_mfma_f32_16x16x32_bf16 v[40:43], a[100:103], a[36:39], v[40:43]\n"
"v_exp_f32 v5, v5\n"
"v_mfma_f32_16x16x32_bf16 v[44:47], a[100:103], a[52:55], v[44:47]\n"
"ds_read_b128 a[84:87], v115 offset:36864\n"
"v_cvt_pk_bf16_f32 v72, v4, v5\n"
"v_mfma_f32_16x16x32_bf16 v[144:147], a[164:167], v[68:71], v[144:147]\n"
"s_add_u32 m0, s71, 28672\n"
"v_exp_f32 v6, v6\n"
"v_mfma_f32_16x16x32_bf16 v[148:151], a[164:167], v[76:79], v[148:151]\n"
"s_mov_b32 s75, 77824\n"
"buffer_load_dwordx4 v118, s[48:51], s75 offen offset:0 lds\n"
"ds_read_b128 a[88:91], v116 offset:36864\n"
"v_mfma_f32_16x16x32_bf16 v[152:155], a[164:167], v[84:87], v[152:155]\n"
"v_exp_f32 v7, v7\n"
"v_mfma_f32_16x16x32_bf16 v[156:159], a[164:167], v[92:95], v[156:159]\n"
"ds_read_b128 a[92:95], v117 offset:36864\n"
"v_cvt_pk_bf16_f32 v73, v6, v7\n"
"v_mfma_f32_16x16x32_bf16 v[32:35], a[104:107], a[8:11], v[32:35]\n"
"s_add_u32 m0, s72, 49152\n"
"v_exp_f32 v8, v8\n"
"v_mfma_f32_16x16x32_bf16 v[36:39], a[104:107], a[24:27], v[36:39]\n"
"s_mov_b32 s75, 50176\n"
"buffer_load_dwordx4 v122, s[52:55], s75 offen offset:0 lds\n"
"ds_read_b64_tr_b16 a[128:129], v112 offset:16384\n"
"v_mfma_f32_16x16x32_bf16 v[40:43], a[104:107], a[40:43], v[40:43]\n"
"v_exp_f32 v9, v9\n"
"v_mfma_f32_16x16x32_bf16 v[44:47], a[104:107], a[56:59], v[44:47]\n"
"ds_read_b64_tr_b16 a[130:131], v112 offset:16640\n"
"v_cvt_pk_bf16_f32 v80, v8, v9\n"
"v_mfma_f32_16x16x32_bf16 v[160:163], a[168:171], v[68:71], v[160:163]\n"
"v_exp_f32 v10, v10\n"
"v_mfma_f32_16x16x32_bf16 v[164:167], a[168:171], v[76:79], v[164:167]\n"
"s_mov_b32 s75, 49280\n"
"buffer_load_dwordx4 v122, s[52:55], s75 offen offset:1024 lds\n"
"ds_read_b64_tr_b16 a[132:133], v113 offset:16384\n"
"v_mfma_f32_16x16x32_bf16 v[168:171], a[168:171], v[84:87], v[168:171]\n"
"v_exp_f32 v11, v11\n"
"v_mfma_f32_16x16x32_bf16 v[172:175], a[168:171], v[92:95], v[172:175]\n"
"ds_read_b64_tr_b16 a[134:135], v113 offset:16640\n"
"v_cvt_pk_bf16_f32 v81, v10, v11\n"
"v_mfma_f32_16x16x32_bf16 v[32:35], a[108:111], a[12:15], v[32:35]\n"
"s_add_u32 m0, s72, 57344\n"
"v_exp_f32 v12, v12\n"
"v_mfma_f32_16x16x32_bf16 v[36:39], a[108:111], a[28:31], v[36:39]\n"
"s_mov_b32 s75, 58368\n"
"buffer_load_dwordx4 v122, s[52:55], s75 offen offset:0 lds\n"
"ds_read_b64_tr_b16 a[136:137], v112 offset:16896\n"
"v_mfma_f32_16x16x32_bf16 v[40:43], a[108:111], a[44:47], v[40:43]\n"
"v_exp_f32 v13, v13\n"
"v_mfma_f32_16x16x32_bf16 v[44:47], a[108:111], a[60:63], v[44:47]\n"
"ds_read_b64_tr_b16 a[138:139], v112 offset:17152\n"
"v_cvt_pk_bf16_f32 v88, v12, v13\n"
"v_mfma_f32_16x16x32_bf16 v[176:179], a[172:175], v[68:71], v[176:179]\n"
"v_exp_f32 v14, v14\n"
"v_mfma_f32_16x16x32_bf16 v[180:183], a[172:175], v[76:79], v[180:183]\n"
"s_mov_b32 s75, 57472\n"
"buffer_load_dwordx4 v122, s[52:55], s75 offen offset:1024 lds\n"
"ds_read_b64_tr_b16 a[140:141], v113 offset:16896\n"
"v_mfma_f32_16x16x32_bf16 v[184:187], a[172:175], v[84:87], v[184:187]\n"
"v_exp_f32 v15, v15\n"
"v_mfma_f32_16x16x32_bf16 v[188:191], a[172:175], v[92:95], v[188:191]\n"
"ds_read_b64_tr_b16 a[142:143], v113 offset:17152\n"
"v_cvt_pk_bf16_f32 v89, v14, v15\n"
"v_mfma_f32_16x16x32_bf16 v[48:51], a[112:115], a[0:3], v[96:99]\n"
"s_add_u32 m0, s69, 147456\n"
"v_exp_f32 v16, v16\n"
"v_mfma_f32_16x16x32_bf16 v[52:55], a[112:115], a[16:19], v[100:103]\n"
"s_mov_b32 s75, 459776\n"
"ds_read_b64_tr_b16 a[144:145], v112 offset:17408\n"
"buffer_load_dwordx4 v125, s[24:27], s75 offen offset:0 nt lds\n"
"v_mfma_f32_16x16x32_bf16 v[56:59], a[112:115], a[32:35], v[104:107]\n"
"s_mov_b32 s75, 458880\n"
"v_exp_f32 v17, v17\n"
"v_mfma_f32_16x16x32_bf16 v[60:63], a[112:115], a[48:51], v[108:111]\n"
"ds_read_b64_tr_b16 a[146:147], v112 offset:17664\n"
"v_cvt_pk_bf16_f32 v66, v16, v17\n"
"v_mfma_f32_16x16x32_bf16 v[192:195], a[176:179], v[68:71], v[192:195]\n"
"v_exp_f32 v18, v18\n"
"v_mfma_f32_16x16x32_bf16 v[196:199], a[176:179], v[76:79], v[196:199]\n"
"ds_read_b64_tr_b16 a[148:149], v113 offset:17408\n"
"buffer_load_dwordx4 v125, s[24:27], s75 offen offset:1024 nt lds\n"
"v_mfma_f32_16x16x32_bf16 v[200:203], a[176:179], v[84:87], v[200:203]\n"
"s_add_u32 m0, s69, 155648\n"
"v_exp_f32 v19, v19\n"
"v_mfma_f32_16x16x32_bf16 v[204:207], a[176:179], v[92:95], v[204:207]\n"
"s_mov_b32 s75, 689152\n"
"ds_read_b64_tr_b16 a[150:151], v113 offset:17664\n"
"v_cvt_pk_bf16_f32 v67, v18, v19\n"
"v_mfma_f32_16x16x32_bf16 v[48:51], a[116:119], a[4:7], v[48:51]\n"
"v_exp_f32 v20, v20\n"
"v_mfma_f32_16x16x32_bf16 v[52:55], a[116:119], a[20:23], v[52:55]\n"
"ds_read_b64_tr_b16 a[152:153], v112 offset:17920\n"
"buffer_load_dwordx4 v125, s[24:27], s75 offen offset:0 nt lds\n"
"v_mfma_f32_16x16x32_bf16 v[56:59], a[116:119], a[36:39], v[56:59]\n"
"s_mov_b32 s75, 688256\n"
"v_exp_f32 v21, v21\n"
"v_mfma_f32_16x16x32_bf16 v[60:63], a[116:119], a[52:55], v[60:63]\n"
"ds_read_b64_tr_b16 a[154:155], v112 offset:18176\n"
"v_cvt_pk_bf16_f32 v74, v20, v21\n"
"v_mfma_f32_16x16x32_bf16 v[208:211], a[180:183], v[68:71], v[208:211]\n"
"v_exp_f32 v22, v22\n"
"v_mfma_f32_16x16x32_bf16 v[212:215], a[180:183], v[76:79], v[212:215]\n"
"ds_read_b64_tr_b16 a[156:157], v113 offset:17920\n"
"buffer_load_dwordx4 v125, s[24:27], s75 offen offset:1024 nt lds\n"
"v_mfma_f32_16x16x32_bf16 v[216:219], a[180:183], v[84:87], v[216:219]\n"
"v_exp_f32 v23, v23\n"
"v_mfma_f32_16x16x32_bf16 v[220:223], a[180:183], v[92:95], v[220:223]\n"
"ds_read_b64_tr_b16 a[158:159], v113 offset:18176\n"
"v_cvt_pk_bf16_f32 v75, v22, v23\n"
"v_mfma_f32_16x16x32_bf16 v[48:51], a[120:123], a[8:11], v[48:51]\n"
"v_exp_f32 v24, v24\n"
"v_mfma_f32_16x16x32_bf16 v[52:55], a[120:123], a[24:27], v[52:55]\n"
"v_bfe_u32 v119, v124, 4, 1\n"
"v_mul_u32_u24 v119, 14336, v119\n"
"v_mfma_f32_16x16x32_bf16 v[56:59], a[120:123], a[40:43], v[56:59]\n"
"v_exp_f32 v25, v25\n"
"v_mfma_f32_16x16x32_bf16 v[60:63], a[120:123], a[56:59], v[60:63]\n"
"v_cvt_pk_bf16_f32 v82, v24, v25\n"
"v_lshrrev_b32 v120, 8, v124\n"
"v_mfma_f32_16x16x32_bf16 v[224:227], a[184:187], v[68:71], v[224:227]\n"
"v_exp_f32 v26, v26\n"
"v_mfma_f32_16x16x32_bf16 v[228:231], a[184:187], v[76:79], v[228:231]\n"
"v_lshl_add_u32 v119, v120, 4, v119\n"
"v_bfe_u32 v120, v124, 5, 3\n"
"v_mfma_f32_16x16x32_bf16 v[232:235], a[184:187], v[84:87], v[232:235]\n"
"v_exp_f32 v27, v27\n"
"v_mfma_f32_16x16x32_bf16 v[236:239], a[184:187], v[92:95], v[236:239]\n"
"v_cvt_pk_bf16_f32 v83, v26, v27\n"
"v_lshl_add_u32 v119, v120, 24, v119\n"
"v_mfma_f32_16x16x32_bf16 v[48:51], a[124:127], a[12:15], v[48:51]\n"
"s_andn2_b32 s73, 16777216, s95\n"
"v_exp_f32 v28, v28\n"
"v_mfma_f32_16x16x32_bf16 v[52:55], a[124:127], a[28:31], v[52:55]\n"
"v_or_b32 v119, s73, v119\n"
"v_mfma_f32_16x16x32_bf16 v[56:59], a[124:127], a[44:47], v[56:59]\n"
"v_exp_f32 v29, v29\n"
"v_mfma_f32_16x16x32_bf16 v[60:63], a[124:127], a[60:63], v[60:63]\n"
"v_cvt_pk_bf16_f32 v90, v28, v29\n"
"v_mfma_f32_16x16x32_bf16 v[240:243], a[188:191], v[68:71], v[240:243]\n"
"v_exp_f32 v30, v30\n"
"v_mfma_f32_16x16x32_bf16 v[244:247], a[188:191], v[76:79], v[244:247]\n"
"v_mfma_f32_16x16x32_bf16 v[248:251], a[188:191], v[84:87], v[248:251]\n"
"v_exp_f32 v31, v31\n"
"v_mfma_f32_16x16x32_bf16 v[252:255], a[188:191], v[92:95], v[252:255]\n"
"v_cvt_pk_bf16_f32 v91, v30, v31\n"
"v_mfma_f32_16x16x32_bf16 a[196:199], a[192:195], v[68:71], a[196:199]\n"
"v_mfma_f32_16x16x32_bf16 a[200:203], a[192:195], v[76:79], a[200:203]\n"
"v_mfma_f32_16x16x32_bf16 a[204:207], a[192:195], v[84:87], a[204:207]\n"
"v_mfma_f32_16x16x32_bf16 a[208:211], a[192:195], v[92:95], a[208:211]\n"
"s_waitcnt lgkmcnt(0)\n"
"v_mfma_f32_16x16x32_bf16 v[0:3], a[64:67], a[0:3], v[96:99]\n"
"s_add_u32 m0, s69, 98304\n"
"v_exp_f32 v32, v32\n"
"v_mfma_f32_16x16x32_bf16 v[4:7], a[64:67], a[16:19], v[100:103]\n"
"ds_read_b128 a[96:99], v114 offset:40960\n"
"v_mfma_f32_16x16x32_bf16 v[8:11], a[64:67], a[32:35], v[104:107]\n"
"v_exp_f32 v33, v33\n"
"v_mfma_f32_16x16x32_bf16 v[12:15], a[64:67], a[48:51], v[108:111]\n"
"ds_read_b128 a[100:103], v115 offset:40960\n"
"v_cvt_pk_bf16_f32 v68, v32, v33\n"
"v_mfma_f32_16x16x32_bf16 v[128:131], a[128:131], v[64:67], v[128:131]\n"
"s_mov_b32 s75, 1024\n"
"v_exp_f32 v34, v34\n"
"v_mfma_f32_16x16x32_bf16 v[132:135], a[128:131], v[72:75], v[132:135]\n"
"ds_read_b128 a[104:107], v116 offset:40960\n"
"buffer_load_dwordx4 v125, s[28:31], s75 offen offset:0 nt lds\n"
"v_mfma_f32_16x16x32_bf16 v[136:139], a[128:131], v[80:83], v[136:139]\n"
"v_exp_f32 v35, v35\n"
"v_mfma_f32_16x16x32_bf16 v[140:143], a[128:131], v[88:91], v[140:143]\n"
"ds_read_b128 a[108:111], v117 offset:40960\n"
"v_cvt_pk_bf16_f32 v69, v34, v35\n"
"v_mfma_f32_16x16x32_bf16 v[0:3], a[68:71], a[4:7], v[0:3]\n"
"v_exp_f32 v36, v36\n"
"v_mfma_f32_16x16x32_bf16 v[4:7], a[68:71], a[20:23], v[4:7]\n"
"s_mov_b32 s75, 128\n"
"ds_read_b128 a[112:115], v114 offset:45056\n"
"buffer_load_dwordx4 v125, s[28:31], s75 offen offset:1024 nt lds\n"
"v_mfma_f32_16x16x32_bf16 v[8:11], a[68:71], a[36:39], v[8:11]\n"
"v_exp_f32 v37, v37\n"
"v_mfma_f32_16x16x32_bf16 v[12:15], a[68:71], a[52:55], v[12:15]\n"
"ds_read_b128 a[116:119], v115 offset:45056\n"
"v_cvt_pk_bf16_f32 v76, v36, v37\n"
"v_mfma_f32_16x16x32_bf16 v[144:147], a[132:135], v[64:67], v[144:147]\n"
"v_exp_f32 v38, v38\n"
"v_mfma_f32_16x16x32_bf16 v[148:151], a[132:135], v[72:75], v[148:151]\n"
"s_add_u32 m0, s69, 106496\n"
"ds_read_b128 a[120:123], v116 offset:45056\n"
"v_mfma_f32_16x16x32_bf16 v[152:155], a[132:135], v[80:83], v[152:155]\n"
"v_exp_f32 v39, v39\n"
"v_mfma_f32_16x16x32_bf16 v[156:159], a[132:135], v[88:91], v[156:159]\n"
"ds_read_b128 a[124:127], v117 offset:45056\n"
"v_cvt_pk_bf16_f32 v77, v38, v39\n"
"v_mfma_f32_16x16x32_bf16 v[0:3], a[72:75], a[8:11], v[0:3]\n"
"v_exp_f32 v40, v40\n"
"v_mfma_f32_16x16x32_bf16 v[4:7], a[72:75], a[24:27], v[4:7]\n"
"s_mov_b32 s75, 230400\n"
"ds_read_b64_tr_b16 a[160:161], v112 offset:24576\n"
"buffer_load_dwordx4 v125, s[28:31], s75 offen offset:0 nt lds\n"
"v_mfma_f32_16x16x32_bf16 v[8:11], a[72:75], a[40:43], v[8:11]\n"
"v_exp_f32 v41, v41\n"
"v_mfma_f32_16x16x32_bf16 v[12:15], a[72:75], a[56:59], v[12:15]\n"
"ds_read_b64_tr_b16 a[162:163], v112 offset:24832\n"
"v_cvt_pk_bf16_f32 v84, v40, v41\n"
"v_mfma_f32_16x16x32_bf16 v[160:163], a[136:139], v[64:67], v[160:163]\n"
"v_exp_f32 v42, v42\n"
"v_mfma_f32_16x16x32_bf16 v[164:167], a[136:139], v[72:75], v[164:167]\n"
"s_mov_b32 s75, 229504\n"
"ds_read_b64_tr_b16 a[164:165], v113 offset:24576\n"
"buffer_load_dwordx4 v125, s[28:31], s75 offen offset:1024 nt lds\n"
"v_mfma_f32_16x16x32_bf16 v[168:171], a[136:139], v[80:83], v[168:171]\n"
"v_exp_f32 v43, v43\n"
"v_mfma_f32_16x16x32_bf16 v[172:175], a[136:139], v[88:91], v[172:175]\n"
"ds_read_b64_tr_b16 a[166:167], v113 offset:24832\n"
"v_cvt_pk_bf16_f32 v85, v42, v43\n"
"v_mfma_f32_16x16x32_bf16 v[0:3], a[76:79], a[12:15], v[0:3]\n"
"v_exp_f32 v44, v44\n"
"v_mfma_f32_16x16x32_bf16 v[4:7], a[76:79], a[28:31], v[4:7]\n"
"s_add_u32 m0, s69, 114688\n"
"ds_read_b64_tr_b16 a[168:169], v112 offset:25088\n"
"v_mfma_f32_16x16x32_bf16 v[8:11], a[76:79], a[44:47], v[8:11]\n"
"v_exp_f32 v45, v45\n"
"v_mfma_f32_16x16x32_bf16 v[12:15], a[76:79], a[60:63], v[12:15]\n"
"ds_read_b64_tr_b16 a[170:171], v112 offset:25344\n"
"v_cvt_pk_bf16_f32 v92, v44, v45\n"
"v_mfma_f32_16x16x32_bf16 v[176:179], a[140:143], v[64:67], v[176:179]\n"
"v_exp_f32 v46, v46\n"
"v_mfma_f32_16x16x32_bf16 v[180:183], a[140:143], v[72:75], v[180:183]\n"
"s_mov_b32 s75, 459776\n"
"ds_read_b64_tr_b16 a[172:173], v113 offset:25088\n"
"buffer_load_dwordx4 v125, s[28:31], s75 offen offset:0 nt lds\n"
"v_mfma_f32_16x16x32_bf16 v[184:187], a[140:143], v[80:83], v[184:187]\n"
"v_exp_f32 v47, v47\n"
"v_mfma_f32_16x16x32_bf16 v[188:191], a[140:143], v[88:91], v[188:191]\n"
"ds_read_b64_tr_b16 a[174:175], v113 offset:25344\n"
"v_cvt_pk_bf16_f32 v93, v46, v47\n"
"v_mfma_f32_16x16x32_bf16 v[16:19], a[80:83], a[0:3], v[96:99]\n"
"v_exp_f32 v48, v48\n"
"v_mfma_f32_16x16x32_bf16 v[20:23], a[80:83], a[16:19], v[100:103]\n"
"s_mov_b32 s75, 458880\n"
"ds_read_b64_tr_b16 a[176:177], v112 offset:25600\n"
"buffer_load_dwordx4 v125, s[28:31], s75 offen offset:1024 nt lds\n"
"v_mfma_f32_16x16x32_bf16 v[24:27], a[80:83], a[32:35], v[104:107]\n"
"v_exp_f32 v49, v49\n"
"v_mfma_f32_16x16x32_bf16 v[28:31], a[80:83], a[48:51], v[108:111]\n"
"ds_read_b64_tr_b16 a[178:179], v112 offset:25856\n"
"v_cvt_pk_bf16_f32 v70, v48, v49\n"
"v_mfma_f32_16x16x32_bf16 v[192:195], a[144:147], v[64:67], v[192:195]\n"
"v_exp_f32 v50, v50\n"
"v_mfma_f32_16x16x32_bf16 v[196:199], a[144:147], v[72:75], v[196:199]\n"
"ds_read_b64_tr_b16 a[180:181], v113 offset:25600\n"
"v_mfma_f32_16x16x32_bf16 v[200:203], a[144:147], v[80:83], v[200:203]\n"
"s_lshl_b32 s73, s68, 9\n"
"v_exp_f32 v51, v51\n"
"v_mfma_f32_16x16x32_bf16 v[204:207], a[144:147], v[88:91], v[204:207]\n"
"s_add_u32 s73, s73, 49152\n"
"ds_read_b64_tr_b16 a[182:183], v113 offset:25856\n"
"v_cvt_pk_bf16_f32 v71, v50, v51\n"
"v_mfma_f32_16x16x32_bf16 v[16:19], a[84:87], a[4:7], v[16:19]\n"
"v_exp_f32 v52, v52\n"
"v_mfma_f32_16x16x32_bf16 v[20:23], a[84:87], a[20:23], v[20:23]\n"
"ds_read_b64_tr_b16 a[184:185], v112 offset:26112\n"
"v_add_u32 v120, s73, v112\n"
"v_mfma_f32_16x16x32_bf16 v[24:27], a[84:87], a[36:39], v[24:27]\n"
"v_exp_f32 v53, v53\n"
"v_mfma_f32_16x16x32_bf16 v[28:31], a[84:87], a[52:55], v[28:31]\n"
"ds_read_b64_tr_b16 a[186:187], v112 offset:26368\n"
"v_cvt_pk_bf16_f32 v78, v52, v53\n"
"v_mfma_f32_16x16x32_bf16 v[208:211], a[148:151], v[64:67], v[208:211]\n"
"v_exp_f32 v54, v54\n"
"v_mfma_f32_16x16x32_bf16 v[212:215], a[148:151], v[72:75], v[212:215]\n"
"ds_read_b64_tr_b16 a[188:189], v113 offset:26112\n"
"v_add_u32 v121, s73, v113\n"
"v_mfma_f32_16x16x32_bf16 v[216:219], a[148:151], v[80:83], v[216:219]\n"
"v_exp_f32 v55, v55\n"
"v_mfma_f32_16x16x32_bf16 v[220:223], a[148:151], v[88:91], v[220:223]\n"
"ds_read_b64_tr_b16 a[190:191], v113 offset:26368\n"
"v_cvt_pk_bf16_f32 v79, v54, v55\n"
"v_mfma_f32_16x16x32_bf16 v[16:19], a[88:91], a[8:11], v[16:19]\n"
"v_exp_f32 v56, v56\n"
"v_mfma_f32_16x16x32_bf16 v[20:23], a[88:91], a[24:27], v[20:23]\n"
"v_mfma_f32_16x16x32_bf16 v[24:27], a[88:91], a[40:43], v[24:27]\n"
"v_exp_f32 v57, v57\n"
"v_mfma_f32_16x16x32_bf16 v[28:31], a[88:91], a[56:59], v[28:31]\n"
"v_cvt_pk_bf16_f32 v86, v56, v57\n"
"v_mfma_f32_16x16x32_bf16 v[224:227], a[152:155], v[64:67], v[224:227]\n"
"v_exp_f32 v58, v58\n"
"v_mfma_f32_16x16x32_bf16 v[228:231], a[152:155], v[72:75], v[228:231]\n"
"v_mfma_f32_16x16x32_bf16 v[232:235], a[152:155], v[80:83], v[232:235]\n"
"v_exp_f32 v59, v59\n"
"v_mfma_f32_16x16x32_bf16 v[236:239], a[152:155], v[88:91], v[236:239]\n"
"v_cvt_pk_bf16_f32 v87, v58, v59\n"
"v_mfma_f32_16x16x32_bf16 v[16:19], a[92:95], a[12:15], v[16:19]\n"
"v_exp_f32 v60, v60\n"
"v_mfma_f32_16x16x32_bf16 v[20:23], a[92:95], a[28:31], v[20:23]\n"
"v_mfma_f32_16x16x32_bf16 v[24:27], a[92:95], a[44:47], v[24:27]\n"
"v_exp_f32 v61, v61\n"
"v_mfma_f32_16x16x32_bf16 v[28:31], a[92:95], a[60:63], v[28:31]\n"
"v_cvt_pk_bf16_f32 v94, v60, v61\n"
"v_mfma_f32_16x16x32_bf16 v[240:243], a[156:159], v[64:67], v[240:243]\n"
"v_exp_f32 v62, v62\n"
"v_mfma_f32_16x16x32_bf16 v[244:247], a[156:159], v[72:75], v[244:247]\n"
"v_mfma_f32_16x16x32_bf16 v[248:251], a[156:159], v[80:83], v[248:251]\n"
"v_exp_f32 v63, v63\n"
"v_mfma_f32_16x16x32_bf16 v[252:255], a[156:159], v[88:91], v[252:255]\n"
"v_cvt_pk_bf16_f32 v95, v62, v63\n"
"v_mfma_f32_16x16x32_bf16 a[196:199], a[192:195], v[64:67], a[196:199]\n"
"v_mfma_f32_16x16x32_bf16 a[200:203], a[192:195], v[72:75], a[200:203]\n"
"v_mfma_f32_16x16x32_bf16 a[204:207], a[192:195], v[80:83], a[204:207]\n"
"v_mfma_f32_16x16x32_bf16 a[208:211], a[192:195], v[88:91], a[208:211]\n"
"s_waitcnt vmcnt(6) lgkmcnt(0)\n"
"s_barrier\n"
"v_mfma_f32_16x16x32_bf16 v[32:35], a[96:99], a[0:3], v[96:99]\n"
"s_add_u32 m0, s71, 32768\n"
"v_exp_f32 v0, v0\n"
"v_mfma_f32_16x16x32_bf16 v[36:39], a[96:99], a[16:19], v[100:103]\n"
"s_mov_b32 s75, 81920\n"
"buffer_load_dwordx4 v118, s[48:51], s75 offen offset:0 lds\n"
"ds_read_b128 a[64:67], v114 offset:0\n"
"v_mfma_f32_16x16x32_bf16 v[40:43], a[96:99], a[32:35], v[104:107]\n"
"v_exp_f32 v1, v1\n"
"v_mfma_f32_16x16x32_bf16 v[44:47], a[96:99], a[48:51], v[108:111]\n"
"ds_read_b128 a[68:71], v115 offset:0\n"
"v_cvt_pk_bf16_f32 v64, v0, v1\n"
"v_mfma_f32_16x16x32_bf16 v[128:131], a[160:163], v[68:71], v[128:131]\n"
"s_add_u32 m0, s71, 36864\n"
"v_exp_f32 v2, v2\n"
"v_mfma_f32_16x16x32_bf16 v[132:135], a[160:163], v[76:79], v[132:135]\n"
"s_mov_b32 s75, 86016\n"
"buffer_load_dwordx4 v118, s[48:51], s75 offen offset:0 lds\n"
"ds_read_b128 a[72:75], v116 offset:0\n"
"v_mfma_f32_16x16x32_bf16 v[136:139], a[160:163], v[84:87], v[136:139]\n"
"v_exp_f32 v3, v3\n"
"v_mfma_f32_16x16x32_bf16 v[140:143], a[160:163], v[92:95], v[140:143]\n"
"ds_read_b128 a[76:79], v117 offset:0\n"
"v_cvt_pk_bf16_f32 v65, v2, v3\n"
"v_mfma_f32_16x16x32_bf16 v[32:35], a[100:103], a[4:7], v[32:35]\n"
"s_add_u32 m0, s71, 40960\n"
"v_exp_f32 v4, v4\n"
"v_mfma_f32_16x16x32_bf16 v[36:39], a[100:103], a[20:23], v[36:39]\n"
"s_mov_b32 s75, 90112\n"
"buffer_load_dwordx4 v118, s[48:51], s75 offen offset:0 lds\n"
"ds_read_b128 a[80:83], v114 offset:4096\n"
"v_mfma_f32_16x16x32_bf16 v[40:43], a[100:103], a[36:39], v[40:43]\n"
"v_exp_f32 v5, v5\n"
"v_mfma_f32_16x16x32_bf16 v[44:47], a[100:103], a[52:55], v[44:47]\n"
"ds_read_b128 a[84:87], v115 offset:4096\n"
"v_cvt_pk_bf16_f32 v72, v4, v5\n"
"v_mfma_f32_16x16x32_bf16 v[144:147], a[164:167], v[68:71], v[144:147]\n"
"s_add_u32 m0, s71, 45056\n"
"v_exp_f32 v6, v6\n"
"v_mfma_f32_16x16x32_bf16 v[148:151], a[164:167], v[76:79], v[148:151]\n"
"s_mov_b32 s75, 94208\n"
"buffer_load_dwordx4 v118, s[48:51], s75 offen offset:0 lds\n"
"ds_read_b128 a[88:91], v116 offset:4096\n"
"v_mfma_f32_16x16x32_bf16 v[152:155], a[164:167], v[84:87], v[152:155]\n"
"v_exp_f32 v7, v7\n"
"v_mfma_f32_16x16x32_bf16 v[156:159], a[164:167], v[92:95], v[156:159]\n"
"ds_read_b128 a[92:95], v117 offset:4096\n"
"v_cvt_pk_bf16_f32 v73, v6, v7\n"
"v_mfma_f32_16x16x32_bf16 v[32:35], a[104:107], a[8:11], v[32:35]\n"
"s_add_u32 m0, s72, 65536\n"
"v_exp_f32 v8, v8\n"
"v_mfma_f32_16x16x32_bf16 v[36:39], a[104:107], a[24:27], v[36:39]\n"
"s_mov_b32 s75, 66560\n"
"buffer_load_dwordx4 v122, s[52:55], s75 offen offset:0 lds\n"
"ds_read_b64_tr_b16 a[128:129], v112 offset:32768\n"
"v_mfma_f32_16x16x32_bf16 v[40:43], a[104:107], a[40:43], v[40:43]\n"
"v_exp_f32 v9, v9\n"
"v_mfma_f32_16x16x32_bf16 v[44:47], a[104:107], a[56:59], v[44:47]\n"
"ds_read_b64_tr_b16 a[130:131], v112 offset:33024\n"
"v_cvt_pk_bf16_f32 v80, v8, v9\n"
"v_mfma_f32_16x16x32_bf16 v[160:163], a[168:171], v[68:71], v[160:163]\n"
"v_exp_f32 v10, v10\n"
"v_mfma_f32_16x16x32_bf16 v[164:167], a[168:171], v[76:79], v[164:167]\n"
"s_mov_b32 s75, 65664\n"
"buffer_load_dwordx4 v122, s[52:55], s75 offen offset:1024 lds\n"
"ds_read_b64_tr_b16 a[132:133], v113 offset:32768\n"
"v_mfma_f32_16x16x32_bf16 v[168:171], a[168:171], v[84:87], v[168:171]\n"
"v_exp_f32 v11, v11\n"
"v_mfma_f32_16x16x32_bf16 v[172:175], a[168:171], v[92:95], v[172:175]\n"
"ds_read_b64_tr_b16 a[134:135], v113 offset:33024\n"
"v_cvt_pk_bf16_f32 v81, v10, v11\n"
"v_mfma_f32_16x16x32_bf16 v[32:35], a[108:111], a[12:15], v[32:35]\n"
"s_add_u32 m0, s72, 73728\n"
"v_exp_f32 v12, v12\n"
"v_mfma_f32_16x16x32_bf16 v[36:39], a[108:111], a[28:31], v[36:39]\n"
"s_mov_b32 s75, 74752\n"
"buffer_load_dwordx4 v122, s[52:55], s75 offen offset:0 lds\n"
"ds_read_b64_tr_b16 a[136:137], v112 offset:33280\n"
"v_mfma_f32_16x16x32_bf16 v[40:43], a[108:111], a[44:47], v[40:43]\n"
"v_exp_f32 v13, v13\n"
"v_mfma_f32_16x16x32_bf16 v[44:47], a[108:111], a[60:63], v[44:47]\n"
"ds_read_b64_tr_b16 a[138:139], v112 offset:33536\n"
"v_cvt_pk_bf16_f32 v88, v12, v13\n"
"v_mfma_f32_16x16x32_bf16 v[176:179], a[172:175], v[68:71], v[176:179]\n"
"v_exp_f32 v14, v14\n"
"v_mfma_f32_16x16x32_bf16 v[180:183], a[172:175], v[76:79], v[180:183]\n"
"s_mov_b32 s75, 73856\n"
"buffer_load_dwordx4 v122, s[52:55], s75 offen offset:1024 lds\n"
"ds_read_b64_tr_b16 a[140:141], v113 offset:33280\n"
"v_mfma_f32_16x16x32_bf16 v[184:187], a[172:175], v[84:87], v[184:187]\n"
"v_exp_f32 v15, v15\n"
"v_mfma_f32_16x16x32_bf16 v[188:191], a[172:175], v[92:95], v[188:191]\n"
"ds_read_b64_tr_b16 a[142:143], v113 offset:33536\n"
"v_cvt_pk_bf16_f32 v89, v14, v15\n"
"v_mfma_f32_16x16x32_bf16 v[48:51], a[112:115], a[0:3], v[96:99]\n"
"s_add_u32 m0, s69, 122880\n"
"v_exp_f32 v16, v16\n"
"v_mfma_f32_16x16x32_bf16 v[52:55], a[112:115], a[16:19], v[100:103]\n"
"s_mov_b32 s75, 689152\n"
"ds_read_b64_tr_b16 a[144:145], v112 offset:33792\n"
"buffer_load_dwordx4 v125, s[28:31], s75 offen offset:0 nt lds\n"
"v_mfma_f32_16x16x32_bf16 v[56:59], a[112:115], a[32:35], v[104:107]\n"
"s_mov_b32 s75, 688256\n"
"v_exp_f32 v17, v17\n"
"v_mfma_f32_16x16x32_bf16 v[60:63], a[112:115], a[48:51], v[108:111]\n"
"ds_read_b64_tr_b16 a[146:147], v112 offset:34048\n"
"v_cvt_pk_bf16_f32 v66, v16, v17\n"
"v_mfma_f32_16x16x32_bf16 v[192:195], a[176:179], v[68:71], v[192:195]\n"
"v_exp_f32 v18, v18\n"
"v_mfma_f32_16x16x32_bf16 v[196:199], a[176:179], v[76:79], v[196:199]\n"
"ds_read_b64_tr_b16 a[148:149], v113 offset:33792\n"
"buffer_load_dwordx4 v125, s[28:31], s75 offen offset:1024 nt lds\n"
"v_mfma_f32_16x16x32_bf16 v[200:203], a[176:179], v[84:87], v[200:203]\n"
"v_exp_f32 v19, v19\n"
"v_mfma_f32_16x16x32_bf16 v[204:207], a[176:179], v[92:95], v[204:207]\n"
"ds_read_b64_tr_b16 a[150:151], v113 offset:34048\n"
"v_cvt_pk_bf16_f32 v67, v18, v19\n"
"v_mfma_f32_16x16x32_bf16 v[48:51], a[116:119], a[4:7], v[48:51]\n"
"v_exp_f32 v20, v20\n"
"v_mfma_f32_16x16x32_bf16 v[52:55], a[116:119], a[20:23], v[52:55]\n"
"ds_read_b64_tr_b16 a[152:153], v112 offset:34304\n"
"ds_read_b64_tr_b16 a[228:229], v120 offset:32768\n"
"v_mfma_f32_16x16x32_bf16 v[56:59], a[116:119], a[36:39], v[56:59]\n"
"v_exp_f32 v21, v21\n"
"v_mfma_f32_16x16x32_bf16 v[60:63], a[116:119], a[52:55], v[60:63]\n"
"ds_read_b64_tr_b16 a[154:155], v112 offset:34560\n"
"v_cvt_pk_bf16_f32 v74, v20, v21\n"
"v_mfma_f32_16x16x32_bf16 v[208:211], a[180:183], v[68:71], v[208:211]\n"
"v_exp_f32 v22, v22\n"
"v_mfma_f32_16x16x32_bf16 v[212:215], a[180:183], v[76:79], v[212:215]\n"
"ds_read_b64_tr_b16 a[156:157], v113 offset:34304\n"
"ds_read_b64_tr_b16 a[230:231], v120 offset:33024\n"
"v_mfma_f32_16x16x32_bf16 v[216:219], a[180:183], v[84:87], v[216:219]\n"
"v_exp_f32 v23, v23\n"
"v_mfma_f32_16x16x32_bf16 v[220:223], a[180:183], v[92:95], v[220:223]\n"
"ds_read_b64_tr_b16 a[158:159], v113 offset:34560\n"
"v_cvt_pk_bf16_f32 v75, v22, v23\n"
"v_mfma_f32_16x16x32_bf16 v[48:51], a[120:123], a[8:11], v[48:51]\n"
"v_exp_f32 v24, v24\n"
"v_mfma_f32_16x16x32_bf16 v[52:55], a[120:123], a[24:27], v[52:55]\n"
"ds_read_b64_tr_b16 a[232:233], v120 offset:40960\n"
"ds_read_b64_tr_b16 a[234:235], v120 offset:41216\n"
"v_mfma_f32_16x16x32_bf16 v[56:59], a[120:123], a[40:43], v[56:59]\n"
"v_exp_f32 v25, v25\n"
"v_mfma_f32_16x16x32_bf16 v[60:63], a[120:123], a[56:59], v[60:63]\n"
"ds_read_b64_tr_b16 a[236:237], v120 offset:49152\n"
"v_cvt_pk_bf16_f32 v82, v24, v25\n"
"v_mfma_f32_16x16x32_bf16 v[224:227], a[184:187], v[68:71], v[224:227]\n"
"v_exp_f32 v26, v26\n"
"v_mfma_f32_16x16x32_bf16 v[228:231], a[184:187], v[76:79], v[228:231]\n"
"ds_read_b64_tr_b16 a[238:239], v120 offset:49408\n"
"ds_read_b64_tr_b16 a[240:241], v120 offset:57344\n"
"v_mfma_f32_16x16x32_bf16 v[232:235], a[184:187], v[84:87], v[232:235]\n"
"v_exp_f32 v27, v27\n"
"v_mfma_f32_16x16x32_bf16 v[236:239], a[184:187], v[92:95], v[236:239]\n"
"ds_read_b64_tr_b16 a[242:243], v120 offset:57600\n"
"v_cvt_pk_bf16_f32 v83, v26, v27\n"
"v_mfma_f32_16x16x32_bf16 v[48:51], a[124:127], a[12:15], v[48:51]\n"
"v_exp_f32 v28, v28\n"
"v_mfma_f32_16x16x32_bf16 v[52:55], a[124:127], a[28:31], v[52:55]\n"
"v_mfma_f32_16x16x32_bf16 v[56:59], a[124:127], a[44:47], v[56:59]\n"
"v_exp_f32 v29, v29\n"
"v_mfma_f32_16x16x32_bf16 v[60:63], a[124:127], a[60:63], v[60:63]\n"
"v_cvt_pk_bf16_f32 v90, v28, v29\n"
"v_mfma_f32_16x16x32_bf16 v[240:243], a[188:191], v[68:71], v[240:243]\n"
"v_exp_f32 v30, v30\n"
"v_mfma_f32_16x16x32_bf16 v[244:247], a[188:191], v[76:79], v[244:247]\n"
"v_mfma_f32_16x16x32_bf16 v[248:251], a[188:191], v[84:87], v[248:251]\n"
"v_exp_f32 v31, v31\n"
"v_mfma_f32_16x16x32_bf16 v[252:255], a[188:191], v[92:95], v[252:255]\n"
"v_cvt_pk_bf16_f32 v91, v30, v31\n"
"v_mfma_f32_16x16x32_bf16 a[196:199], a[192:195], v[68:71], a[196:199]\n"
"v_mfma_f32_16x16x32_bf16 a[200:203], a[192:195], v[76:79], a[200:203]\n"
"v_mfma_f32_16x16x32_bf16 a[204:207], a[192:195], v[84:87], a[204:207]\n"
"v_mfma_f32_16x16x32_bf16 a[208:211], a[192:195], v[92:95], a[208:211]\n"
"s_waitcnt lgkmcnt(0)\n"
"v_mfma_f32_16x16x32_bf16 a[244:247], a[228:231], a[212:215], 0\n"
"v_exp_f32 v32, v32\n"
"v_mfma_f32_16x16x32_bf16 a[244:247], a[232:235], a[216:219], a[244:247]\n"
"ds_read_b128 a[96:99], v114 offset:8192\n"
"v_mfma_f32_16x16x32_bf16 a[244:247], a[236:239], a[220:223], a[244:247]\n"
"v_exp_f32 v33, v33\n"
"v_mfma_f32_16x16x32_bf16 a[244:247], a[240:243], a[224:227], a[244:247]\n"
"ds_read_b128 a[100:103], v115 offset:8192\n"
"v_cvt_pk_bf16_f32 v68, v32, v33\n"
"v_mfma_f32_16x16x32_bf16 v[0:3], a[64:67], a[0:3], v[96:99]\n"
"v_exp_f32 v34, v34\n"
"v_mfma_f32_16x16x32_bf16 v[4:7], a[64:67], a[16:19], v[100:103]\n"
"ds_read_b128 a[104:107], v116 offset:8192\n"
"v_mfma_f32_16x16x32_bf16 v[8:11], a[64:67], a[32:35], v[104:107]\n"
"v_exp_f32 v35, v35\n"
"v_mfma_f32_16x16x32_bf16 v[12:15], a[64:67], a[48:51], v[108:111]\n"
"ds_read_b128 a[108:111], v117 offset:8192\n"
"v_cvt_pk_bf16_f32 v69, v34, v35\n"
"v_mfma_f32_16x16x32_bf16 v[128:131], a[128:131], v[64:67], v[128:131]\n"
"v_exp_f32 v36, v36\n"
"v_mfma_f32_16x16x32_bf16 v[132:135], a[128:131], v[72:75], v[132:135]\n"
"ds_read_b128 a[112:115], v114 offset:12288\n"
"ds_read_b64_tr_b16 a[228:229], v121 offset:32768\n"
"v_mfma_f32_16x16x32_bf16 v[136:139], a[128:131], v[80:83], v[136:139]\n"
"v_exp_f32 v37, v37\n"
"v_mfma_f32_16x16x32_bf16 v[140:143], a[128:131], v[88:91], v[140:143]\n"
"ds_read_b128 a[116:119], v115 offset:12288\n"
"v_cvt_pk_bf16_f32 v76, v36, v37\n"
"v_mfma_f32_16x16x32_bf16 v[0:3], a[68:71], a[4:7], v[0:3]\n"
"v_exp_f32 v38, v38\n"
"v_mfma_f32_16x16x32_bf16 v[4:7], a[68:71], a[20:23], v[4:7]\n"
"ds_read_b128 a[120:123], v116 offset:12288\n"
"ds_read_b64_tr_b16 a[230:231], v121 offset:33024\n"
"v_mfma_f32_16x16x32_bf16 v[8:11], a[68:71], a[36:39], v[8:11]\n"
"v_exp_f32 v39, v39\n"
"v_mfma_f32_16x16x32_bf16 v[12:15], a[68:71], a[52:55], v[12:15]\n"
"ds_read_b128 a[124:127], v117 offset:12288\n"
"v_cvt_pk_bf16_f32 v77, v38, v39\n"
"v_mfma_f32_16x16x32_bf16 v[144:147], a[132:135], v[64:67], v[144:147]\n"
"v_exp_f32 v40, v40\n"
"v_mfma_f32_16x16x32_bf16 v[148:151], a[132:135], v[72:75], v[148:151]\n"
"ds_read_b64_tr_b16 a[160:161], v112 offset:40960\n"
"ds_read_b64_tr_b16 a[232:233], v121 offset:40960\n"
"v_mfma_f32_16x16x32_bf16 v[152:155], a[132:135], v[80:83], v[152:155]\n"
"v_exp_f32 v41, v41\n"
"v_mfma_f32_16x16x32_bf16 v[156:159], a[132:135], v[88:91], v[156:159]\n"
"ds_read_b64_tr_b16 a[162:163], v112 offset:41216\n"
"v_cvt_pk_bf16_f32 v84, v40, v41\n"
"v_mfma_f32_16x16x32_bf16 v[0:3], a[72:75], a[8:11], v[0:3]\n"
"v_exp_f32 v42, v42\n"
"v_mfma_f32_16x16x32_bf16 v[4:7], a[72:75], a[24:27], v[4:7]\n"
"ds_read_b64_tr_b16 a[164:165], v113 offset:40960\n"
"ds_read_b64_tr_b16 a[234:235], v121 offset:41216\n"
"v_mfma_f32_16x16x32_bf16 v[8:11], a[72:75], a[40:43], v[8:11]\n"
"v_exp_f32 v43, v43\n"
"v_mfma_f32_16x16x32_bf16 v[12:15], a[72:75], a[56:59], v[12:15]\n"
"ds_read_b64_tr_b16 a[166:167], v113 offset:41216\n"
"v_cvt_pk_bf16_f32 v85, v42, v43\n"
"v_mfma_f32_16x16x32_bf16 v[160:163], a[136:139], v[64:67], v[160:163]\n"
"v_exp_f32 v44, v44\n"
"v_mfma_f32_16x16x32_bf16 v[164:167], a[136:139], v[72:75], v[164:167]\n"
"ds_read_b64_tr_b16 a[168:169], v112 offset:41472\n"
"ds_read_b64_tr_b16 a[236:237], v121 offset:49152\n"
"v_mfma_f32_16x16x32_bf16 v[168:171], a[136:139], v[80:83], v[168:171]\n"
"v_exp_f32 v45, v45\n"
"v_mfma_f32_16x16x32_bf16 v[172:175], a[136:139], v[88:91], v[172:175]\n"
"ds_read_b64_tr_b16 a[170:171], v112 offset:41728\n"
"v_cvt_pk_bf16_f32 v92, v44, v45\n"
"v_mfma_f32_16x16x32_bf16 v[0:3], a[76:79], a[12:15], v[0:3]\n"
"v_exp_f32 v46, v46\n"
"v_mfma_f32_16x16x32_bf16 v[4:7], a[76:79], a[28:31], v[4:7]\n"
"ds_read_b64_tr_b16 a[172:173], v113 offset:41472\n"
"ds_read_b64_tr_b16 a[238:239], v121 offset:49408\n"
"v_mfma_f32_16x16x32_bf16 v[8:11], a[76:79], a[44:47], v[8:11]\n"
"v_exp_f32 v47, v47\n"
"v_mfma_f32_16x16x32_bf16 v[12:15], a[76:79], a[60:63], v[12:15]\n"
"ds_read_b64_tr_b16 a[174:175], v113 offset:41728\n"
"v_cvt_pk_bf16_f32 v93, v46, v47\n"
"v_mfma_f32_16x16x32_bf16 v[176:179], a[140:143], v[64:67], v[176:179]\n"
"v_exp_f32 v48, v48\n"
"v_mfma_f32_16x16x32_bf16 v[180:183], a[140:143], v[72:75], v[180:183]\n"
"ds_read_b64_tr_b16 a[176:177], v112 offset:41984\n"
"ds_read_b64_tr_b16 a[240:241], v121 offset:57344\n"
"v_mfma_f32_16x16x32_bf16 v[184:187], a[140:143], v[80:83], v[184:187]\n"
"v_exp_f32 v49, v49\n"
"v_mfma_f32_16x16x32_bf16 v[188:191], a[140:143], v[88:91], v[188:191]\n"
"ds_read_b64_tr_b16 a[178:179], v112 offset:42240\n"
"v_cvt_pk_bf16_f32 v70, v48, v49\n"
"v_mfma_f32_16x16x32_bf16 v[16:19], a[80:83], a[0:3], v[96:99]\n"
"v_exp_f32 v50, v50\n"
"v_mfma_f32_16x16x32_bf16 v[20:23], a[80:83], a[16:19], v[100:103]\n"
"ds_read_b64_tr_b16 a[180:181], v113 offset:41984\n"
"ds_read_b64_tr_b16 a[242:243], v121 offset:57600\n"
"v_mfma_f32_16x16x32_bf16 v[24:27], a[80:83], a[32:35], v[104:107]\n"
"s_and_b32 s73, s93, 7\n"
"v_exp_f32 v51, v51\n"
"v_mfma_f32_16x16x32_bf16 v[28:31], a[80:83], a[48:51], v[108:111]\n"
"s_mul_i32 s73, s73, 57344\n"
"ds_read_b64_tr_b16 a[182:183], v113 offset:42240\n"
"v_cvt_pk_bf16_f32 v71, v50, v51\n"
"v_mfma_f32_16x16x32_bf16 v[192:195], a[144:147], v[64:67], v[192:195]\n"
"s_lshr_b32 s74, s93, 3\n"
"v_exp_f32 v52, v52\n"
"v_mfma_f32_16x16x32_bf16 v[196:199], a[144:147], v[72:75], v[196:199]\n"
"s_lshl_b32 s74, s74, 2\n"
"s_add_u32 s74, s74, s68\n"
"ds_read_b64_tr_b16 a[184:185], v112 offset:42496\n"
"v_mfma_f32_16x16x32_bf16 v[200:203], a[144:147], v[80:83], v[200:203]\n"
"s_lshl_b32 s74, s74, 7\n"
"v_exp_f32 v53, v53\n"
"v_mfma_f32_16x16x32_bf16 v[204:207], a[144:147], v[88:91], v[204:207]\n"
"s_add_u32 s90, s73, s74\n"
"ds_read_b64_tr_b16 a[186:187], v112 offset:42752\n"
"v_cvt_pk_bf16_f32 v78, v52, v53\n"
"v_mfma_f32_16x16x32_bf16 v[16:19], a[84:87], a[4:7], v[16:19]\n"
"s_add_u32 s90, s90, 163840\n"
"v_exp_f32 v54, v54\n"
"v_mfma_f32_16x16x32_bf16 v[20:23], a[84:87], a[20:23], v[20:23]\n"
"ds_read_b64_tr_b16 a[188:189], v113 offset:42496\n"
"buffer_store_dwordx4 a[244:247], v119, s[60:63], s90 offen offset:0 sc1\n"
"v_mfma_f32_16x16x32_bf16 v[24:27], a[84:87], a[36:39], v[24:27]\n"
"v_exp_f32 v55, v55\n"
"v_mfma_f32_16x16x32_bf16 v[28:31], a[84:87], a[52:55], v[28:31]\n"
"ds_read_b64_tr_b16 a[190:191], v113 offset:42752\n"
"v_cvt_pk_bf16_f32 v79, v54, v55\n"
"v_mfma_f32_16x16x32_bf16 v[208:211], a[148:151], v[64:67], v[208:211]\n"
"v_exp_f32 v56, v56\n"
"v_mfma_f32_16x16x32_bf16 v[212:215], a[148:151], v[72:75], v[212:215]\n"
"v_lshlrev_b32 v125, 1, v124\n"
"v_mfma_f32_16x16x32_bf16 v[216:219], a[148:151], v[80:83], v[216:219]\n"
"s_lshr_b32 s73, s93, 3\n"
"v_exp_f32 v57, v57\n"
"v_mfma_f32_16x16x32_bf16 v[220:223], a[148:151], v[88:91], v[220:223]\n"
"s_lshl_b32 s73, s73, 2\n"
"s_add_u32 s73, s73, s68\n"
"v_cvt_pk_bf16_f32 v86, v56, v57\n"
"v_mfma_f32_16x16x32_bf16 v[16:19], a[88:91], a[8:11], v[16:19]\n"
"s_lshl_b32 s73, s73, 14\n"
"v_exp_f32 v58, v58\n"
"v_mfma_f32_16x16x32_bf16 v[20:23], a[88:91], a[24:27], v[20:23]\n"
"s_add_u32 s90, s73, 1024\n"
"buffer_load_dword a252, v125, s[36:39], s90 offen offset:0\n"
"v_mfma_f32_16x16x32_bf16 v[24:27], a[88:91], a[40:43], v[24:27]\n"
"v_exp_f32 v59, v59\n"
"v_mfma_f32_16x16x32_bf16 v[28:31], a[88:91], a[56:59], v[28:31]\n"
"v_cvt_pk_bf16_f32 v87, v58, v59\n"
"v_mfma_f32_16x16x32_bf16 v[224:227], a[152:155], v[64:67], v[224:227]\n"
"v_exp_f32 v60, v60\n"
"v_mfma_f32_16x16x32_bf16 v[228:231], a[152:155], v[72:75], v[228:231]\n"
"v_mfma_f32_16x16x32_bf16 v[232:235], a[152:155], v[80:83], v[232:235]\n"
"v_exp_f32 v61, v61\n"
"v_mfma_f32_16x16x32_bf16 v[236:239], a[152:155], v[88:91], v[236:239]\n"
"v_cvt_pk_bf16_f32 v94, v60, v61\n"
"v_mfma_f32_16x16x32_bf16 v[16:19], a[92:95], a[12:15], v[16:19]\n"
"v_exp_f32 v62, v62\n"
"v_mfma_f32_16x16x32_bf16 v[20:23], a[92:95], a[28:31], v[20:23]\n"
"s_add_u32 s90, s73, 3072\n"
"buffer_load_dword a252, v125, s[36:39], s90 offen offset:0\n"
"v_mfma_f32_16x16x32_bf16 v[24:27], a[92:95], a[44:47], v[24:27]\n"
"v_exp_f32 v63, v63\n"
"v_mfma_f32_16x16x32_bf16 v[28:31], a[92:95], a[60:63], v[28:31]\n"
"v_cvt_pk_bf16_f32 v95, v62, v63\n"
"v_mfma_f32_16x16x32_bf16 v[240:243], a[156:159], v[64:67], v[240:243]\n"
"v_mfma_f32_16x16x32_bf16 v[244:247], a[156:159], v[72:75], v[244:247]\n"
"v_mfma_f32_16x16x32_bf16 v[248:251], a[156:159], v[80:83], v[248:251]\n"
"v_mfma_f32_16x16x32_bf16 v[252:255], a[156:159], v[88:91], v[252:255]\n"
"v_mfma_f32_16x16x32_bf16 a[196:199], a[192:195], v[64:67], a[196:199]\n"
"v_mfma_f32_16x16x32_bf16 a[200:203], a[192:195], v[72:75], a[200:203]\n"
"v_mfma_f32_16x16x32_bf16 a[204:207], a[192:195], v[80:83], a[204:207]\n"
"v_mfma_f32_16x16x32_bf16 a[208:211], a[192:195], v[88:91], a[208:211]\n"
"s_waitcnt vmcnt(3) lgkmcnt(0)\n"
"s_barrier\n"
"v_mfma_f32_16x16x32_bf16 a[248:251], a[228:231], a[212:215], 0\n"
"s_add_u32 m0, s71, 0\n"
"v_exp_f32 v0, v0\n"
"v_mfma_f32_16x16x32_bf16 a[248:251], a[232:235], a[216:219], a[248:251]\n"
"s_mov_b32 s75, 98304\n"
"buffer_load_dwordx4 v118, s[48:51], s75 offen offset:0 lds\n"
"ds_read_b128 a[64:67], v114 offset:16384\n"
"v_mfma_f32_16x16x32_bf16 a[248:251], a[236:239], a[220:223], a[248:251]\n"
"v_exp_f32 v1, v1\n"
"v_mfma_f32_16x16x32_bf16 a[248:251], a[240:243], a[224:227], a[248:251]\n"
"ds_read_b128 a[68:71], v115 offset:16384\n"
"v_cvt_pk_bf16_f32 v64, v0, v1\n"
"v_mfma_f32_16x16x32_bf16 v[32:35], a[96:99], a[0:3], v[96:99]\n"
"s_add_u32 m0, s71, 4096\n"
"v_exp_f32 v2, v2\n"
"v_mfma_f32_16x16x32_bf16 v[36:39], a[96:99], a[16:19], v[100:103]\n"
"s_mov_b32 s75, 102400\n"
"buffer_load_dwordx4 v118, s[48:51], s75 offen offset:0 lds\n"
"ds_read_b128 a[72:75], v116 offset:16384\n"
"v_mfma_f32_16x16x32_bf16 v[40:43], a[96:99], a[32:35], v[104:107]\n"
"v_exp_f32 v3, v3\n"
"v_mfma_f32_16x16x32_bf16 v[44:47], a[96:99], a[48:51], v[108:111]\n"
"ds_read_b128 a[76:79], v117 offset:16384\n"
"v_cvt_pk_bf16_f32 v65, v2, v3\n"
"v_mfma_f32_16x16x32_bf16 v[128:131], a[160:163], v[68:71], v[128:131]\n"
"s_add_u32 m0, s71, 8192\n"
"v_exp_f32 v4, v4\n"
"v_mfma_f32_16x16x32_bf16 v[132:135], a[160:163], v[76:79], v[132:135]\n"
"s_mov_b32 s75, 106496\n"
"buffer_load_dwordx4 v118, s[48:51], s75 offen offset:0 lds\n"
"ds_read_b128 a[80:83], v114 offset:20480\n"
"v_mfma_f32_16x16x32_bf16 v[136:139], a[160:163], v[84:87], v[136:139]\n"
"v_exp_f32 v5, v5\n"
"v_mfma_f32_16x16x32_bf16 v[140:143], a[160:163], v[92:95], v[140:143]\n"
"ds_read_b128 a[84:87], v115 offset:20480\n"
"v_cvt_pk_bf16_f32 v72, v4, v5\n"
"v_mfma_f32_16x16x32_bf16 v[32:35], a[100:103], a[4:7], v[32:35]\n"
"s_add_u32 m0, s71, 12288\n"
"v_exp_f32 v6, v6\n"
"v_mfma_f32_16x16x32_bf16 v[36:39], a[100:103], a[20:23], v[36:39]\n"
"s_mov_b32 s75, 110592\n"
"buffer_load_dwordx4 v118, s[48:51], s75 offen offset:0 lds\n"
"ds_read_b128 a[88:91], v116 offset:20480\n"
"v_mfma_f32_16x16x32_bf16 v[40:43], a[100:103], a[36:39], v[40:43]\n"
"v_exp_f32 v7, v7\n"
"v_mfma_f32_16x16x32_bf16 v[44:47], a[100:103], a[52:55], v[44:47]\n"
"ds_read_b128 a[92:95], v117 offset:20480\n"
"v_cvt_pk_bf16_f32 v73, v6, v7\n"
"v_mfma_f32_16x16x32_bf16 v[144:147], a[164:167], v[68:71], v[144:147]\n"
"s_add_u32 m0, s72, 81920\n"
"v_exp_f32 v8, v8\n"
"v_mfma_f32_16x16x32_bf16 v[148:151], a[164:167], v[76:79], v[148:151]\n"
"s_mov_b32 s75, 82944\n"
"buffer_load_dwordx4 v122, s[52:55], s75 offen offset:0 lds\n"
"ds_read_b64_tr_b16 a[128:129], v112 offset:0\n"
"v_mfma_f32_16x16x32_bf16 v[152:155], a[164:167], v[84:87], v[152:155]\n"
"v_exp_f32 v9, v9\n"
"v_mfma_f32_16x16x32_bf16 v[156:159], a[164:167], v[92:95], v[156:159]\n"
"ds_read_b64_tr_b16 a[130:131], v112 offset:256\n"
"v_cvt_pk_bf16_f32 v80, v8, v9\n"
"v_mfma_f32_16x16x32_bf16 v[32:35], a[104:107], a[8:11], v[32:35]\n"
"v_exp_f32 v10, v10\n"
"v_mfma_f32_16x16x32_bf16 v[36:39], a[104:107], a[24:27], v[36:39]\n"
"s_mov_b32 s75, 82048\n"
"buffer_load_dwordx4 v122, s[52:55], s75 offen offset:1024 lds\n"
"ds_read_b64_tr_b16 a[132:133], v113 offset:0\n"
"v_mfma_f32_16x16x32_bf16 v[40:43], a[104:107], a[40:43], v[40:43]\n"
"v_exp_f32 v11, v11\n"
"v_mfma_f32_16x16x32_bf16 v[44:47], a[104:107], a[56:59], v[44:47]\n"
"ds_read_b64_tr_b16 a[134:135], v113 offset:256\n"
"v_cvt_pk_bf16_f32 v81, v10, v11\n"
"v_mfma_f32_16x16x32_bf16 v[160:163], a[168:171], v[68:71], v[160:163]\n"
"s_add_u32 m0, s72, 90112\n"
"v_exp_f32 v12, v12\n"
"v_mfma_f32_16x16x32_bf16 v[164:167], a[168:171], v[76:79], v[164:167]\n"
"s_mov_b32 s75, 91136\n"
"buffer_load_dwordx4 v122, s[52:55], s75 offen offset:0 lds\n"
"ds_read_b64_tr_b16 a[136:137], v112 offset:512\n"
"v_mfma_f32_16x16x32_bf16 v[168:171], a[168:171], v[84:87], v[168:171]\n"
"v_exp_f32 v13, v13\n"
"v_mfma_f32_16x16x32_bf16 v[172:175], a[168:171], v[92:95], v[172:175]\n"
"ds_read_b64_tr_b16 a[138:139], v112 offset:768\n"
"v_cvt_pk_bf16_f32 v88, v12, v13\n"
"v_mfma_f32_16x16x32_bf16 v[32:35], a[108:111], a[12:15], v[32:35]\n"
"v_exp_f32 v14, v14\n"
"v_mfma_f32_16x16x32_bf16 v[36:39], a[108:111], a[28:31], v[36:39]\n"
"s_mov_b32 s75, 90240\n"
"buffer_load_dwordx4 v122, s[52:55], s75 offen offset:1024 lds\n"
"ds_read_b64_tr_b16 a[140:141], v113 offset:512\n"
"v_mfma_f32_16x16x32_bf16 v[40:43], a[108:111], a[44:47], v[40:43]\n"
"v_exp_f32 v15, v15\n"
"v_mfma_f32_16x16x32_bf16 v[44:47], a[108:111], a[60:63], v[44:47]\n"
"ds_read_b64_tr_b16 a[142:143], v113 offset:768\n"
"v_cvt_pk_bf16_f32 v89, v14, v15\n"
"v_mfma_f32_16x16x32_bf16 v[176:179], a[172:175], v[68:71], v[176:179]\n"
"v_exp_f32 v16, v16\n"
"v_mfma_f32_16x16x32_bf16 v[180:183], a[172:175], v[76:79], v[180:183]\n"
"ds_read_b64_tr_b16 a[144:145], v112 offset:1024\n"
"v_lshlrev_b32 v125, 1, v124\n"
"v_mfma_f32_16x16x32_bf16 v[184:187], a[172:175], v[84:87], v[184:187]\n"
"s_lshr_b32 s73, s93, 3\n"
"v_exp_f32 v17, v17\n"
"v_mfma_f32_16x16x32_bf16 v[188:191], a[172:175], v[92:95], v[188:191]\n"
"s_lshl_b32 s73, s73, 2\n"
"ds_read_b64_tr_b16 a[146:147], v112 offset:1280\n"
"v_cvt_pk_bf16_f32 v66, v16, v17\n"
"v_mfma_f32_16x16x32_bf16 v[48:51], a[112:115], a[0:3], v[96:99]\n"
"s_add_u32 s73, s73, s68\n"
"v_exp_f32 v18, v18\n"
"v_mfma_f32_16x16x32_bf16 v[52:55], a[112:115], a[16:19], v[100:103]\n"
"s_lshl_b32 s73, s73, 14\n"
"s_add_u32 s90, s73, 5120\n"
"ds_read_b64_tr_b16 a[148:149], v113 offset:1024\n"
"v_mfma_f32_16x16x32_bf16 v[56:59], a[112:115], a[32:35], v[104:107]\n"
"v_exp_f32 v19, v19\n"
"v_mfma_f32_16x16x32_bf16 v[60:63], a[112:115], a[48:51], v[108:111]\n"
"ds_read_b64_tr_b16 a[150:151], v113 offset:1280\n"
"v_cvt_pk_bf16_f32 v67, v18, v19\n"
"v_mfma_f32_16x16x32_bf16 v[192:195], a[176:179], v[68:71], v[192:195]\n"
"v_exp_f32 v20, v20\n"
"v_mfma_f32_16x16x32_bf16 v[196:199], a[176:179], v[76:79], v[196:199]\n"
"ds_read_b64_tr_b16 a[152:153], v112 offset:1536\n"
"buffer_load_dword a252, v125, s[36:39], s90 offen offset:0\n"
"v_mfma_f32_16x16x32_bf16 v[200:203], a[176:179], v[84:87], v[200:203]\n"
"v_exp_f32 v21, v21\n"
"v_mfma_f32_16x16x32_bf16 v[204:207], a[176:179], v[92:95], v[204:207]\n"
"ds_read_b64_tr_b16 a[154:155], v112 offset:1792\n"
"v_cvt_pk_bf16_f32 v74, v20, v21\n"
"v_mfma_f32_16x16x32_bf16 v[48:51], a[116:119], a[4:7], v[48:51]\n"
"v_exp_f32 v22, v22\n"
"v_mfma_f32_16x16x32_bf16 v[52:55], a[116:119], a[20:23], v[52:55]\n"
"ds_read_b64_tr_b16 a[156:157], v113 offset:1536\n"
"v_mfma_f32_16x16x32_bf16 v[56:59], a[116:119], a[36:39], v[56:59]\n"
"v_exp_f32 v23, v23\n"
"v_mfma_f32_16x16x32_bf16 v[60:63], a[116:119], a[52:55], v[60:63]\n"
"ds_read_b64_tr_b16 a[158:159], v113 offset:1792\n"
"v_cvt_pk_bf16_f32 v75, v22, v23\n"
"v_mfma_f32_16x16x32_bf16 v[208:211], a[180:183], v[68:71], v[208:211]\n"
"v_exp_f32 v24, v24\n"
"v_mfma_f32_16x16x32_bf16 v[212:215], a[180:183], v[76:79], v[212:215]\n"
"v_mfma_f32_16x16x32_bf16 v[216:219], a[180:183], v[84:87], v[216:219]\n"
"v_exp_f32 v25, v25\n"
"v_mfma_f32_16x16x32_bf16 v[220:223], a[180:183], v[92:95], v[220:223]\n"
"ds_read_b64_tr_b16 a[228:229], v120 offset:0\n"
"v_cvt_pk_bf16_f32 v82, v24, v25\n"
"v_mfma_f32_16x16x32_bf16 v[48:51], a[120:123], a[8:11], v[48:51]\n"
"v_exp_f32 v26, v26\n"
"v_mfma_f32_16x16x32_bf16 v[52:55], a[120:123], a[24:27], v[52:55]\n"
"ds_read_b64_tr_b16 a[230:231], v120 offset:256\n"
"ds_read_b64_tr_b16 a[232:233], v120 offset:8192\n"
"v_mfma_f32_16x16x32_bf16 v[56:59], a[120:123], a[40:43], v[56:59]\n"
"v_exp_f32 v27, v27\n"
"v_mfma_f32_16x16x32_bf16 v[60:63], a[120:123], a[56:59], v[60:63]\n"
"ds_read_b64_tr_b16 a[234:235], v120 offset:8448\n"
"v_cvt_pk_bf16_f32 v83, v26, v27\n"
"v_mfma_f32_16x16x32_bf16 v[224:227], a[184:187], v[68:71], v[224:227]\n"
"v_exp_f32 v28, v28\n"
"v_mfma_f32_16x16x32_bf16 v[228:231], a[184:187], v[76:79], v[228:231]\n"
"ds_read_b64_tr_b16 a[236:237], v120 offset:16384\n"
"ds_read_b64_tr_b16 a[238:239], v120 offset:16640\n"
"v_mfma_f32_16x16x32_bf16 v[232:235], a[184:187], v[84:87], v[232:235]\n"
"v_exp_f32 v29, v29\n"
"v_mfma_f32_16x16x32_bf16 v[236:239], a[184:187], v[92:95], v[236:239]\n"
"ds_read_b64_tr_b16 a[240:241], v120 offset:24576\n"
"v_cvt_pk_bf16_f32 v90, v28, v29\n"
"v_mfma_f32_16x16x32_bf16 v[48:51], a[124:127], a[12:15], v[48:51]\n"
"v_exp_f32 v30, v30\n"
"v_mfma_f32_16x16x32_bf16 v[52:55], a[124:127], a[28:31], v[52:55]\n"
"ds_read_b64_tr_b16 a[242:243], v120 offset:24832\n"
"v_mfma_f32_16x16x32_bf16 v[56:59], a[124:127], a[44:47], v[56:59]\n"
"v_exp_f32 v31, v31\n"
"v_mfma_f32_16x16x32_bf16 v[60:63], a[124:127], a[60:63], v[60:63]\n"
"v_cvt_pk_bf16_f32 v91, v30, v31\n"
"v_mfma_f32_16x16x32_bf16 v[240:243], a[188:191], v[68:71], v[240:243]\n"
"v_mfma_f32_16x16x32_bf16 v[244:247], a[188:191], v[76:79], v[244:247]\n"
"v_mfma_f32_16x16x32_bf16 v[248:251], a[188:191], v[84:87], v[248:251]\n"
"v_mfma_f32_16x16x32_bf16 v[252:255], a[188:191], v[92:95], v[252:255]\n"
"v_mfma_f32_16x16x32_bf16 a[196:199], a[192:195], v[68:71], a[196:199]\n"
"v_mfma_f32_16x16x32_bf16 a[200:203], a[192:195], v[76:79], a[200:203]\n"
"v_mfma_f32_16x16x32_bf16 a[204:207], a[192:195], v[84:87], a[204:207]\n"
"v_mfma_f32_16x16x32_bf16 a[208:211], a[192:195], v[92:95], a[208:211]\n"
"s_waitcnt lgkmcnt(0)\n"
"v_mfma_f32_16x16x32_bf16 a[244:247], a[228:231], a[212:215], 0\n"
"s_and_b32 s73, s93, 7\n"
"v_exp_f32 v32, v32\n"
"v_mfma_f32_16x16x32_bf16 a[244:247], a[232:235], a[216:219], a[244:247]\n"
"s_mul_i32 s73, s73, 57344\n"
"s_lshr_b32 s74, s93, 3\n"
"ds_read_b128 a[96:99], v114 offset:24576\n"
"v_mfma_f32_16x16x32_bf16 a[244:247], a[236:239], a[220:223], a[244:247]\n"
"s_lshl_b32 s74, s74, 2\n"
"v_exp_f32 v33, v33\n"
"v_mfma_f32_16x16x32_bf16 a[244:247], a[240:243], a[224:227], a[244:247]\n"
"s_add_u32 s74, s74, s68\n"
"ds_read_b128 a[100:103], v115 offset:24576\n"
"v_cvt_pk_bf16_f32 v68, v32, v33\n"
"v_mfma_f32_16x16x32_bf16 v[0:3], a[64:67], a[0:3], v[96:99]\n"
"s_lshl_b32 s74, s74, 7\n"
"v_exp_f32 v34, v34\n"
"v_mfma_f32_16x16x32_bf16 v[4:7], a[64:67], a[16:19], v[100:103]\n"
"s_add_u32 s90, s73, s74\n"
"s_add_u32 s90, s90, 163840\n"
"ds_read_b128 a[104:107], v116 offset:24576\n"
"v_mfma_f32_16x16x32_bf16 v[8:11], a[64:67], a[32:35], v[104:107]\n"
"v_exp_f32 v35, v35\n"
"v_mfma_f32_16x16x32_bf16 v[12:15], a[64:67], a[48:51], v[108:111]\n"
"ds_read_b128 a[108:111], v117 offset:24576\n"
"v_cvt_pk_bf16_f32 v69, v34, v35\n"
"v_mfma_f32_16x16x32_bf16 v[128:131], a[128:131], v[64:67], v[128:131]\n"
"v_exp_f32 v36, v36\n"
"v_mfma_f32_16x16x32_bf16 v[132:135], a[128:131], v[72:75], v[132:135]\n"
"ds_read_b128 a[112:115], v114 offset:28672\n"
"buffer_store_dwordx4 a[248:251], v119, s[60:63], s90 offen offset:64 sc1\n"
"v_mfma_f32_16x16x32_bf16 v[136:139], a[128:131], v[80:83], v[136:139]\n"
"v_exp_f32 v37, v37\n"
"v_mfma_f32_16x16x32_bf16 v[140:143], a[128:131], v[88:91], v[140:143]\n"
"ds_read_b128 a[116:119], v115 offset:28672\n"
"v_cvt_pk_bf16_f32 v76, v36, v37\n"
"v_mfma_f32_16x16x32_bf16 v[0:3], a[68:71], a[4:7], v[0:3]\n"
"v_exp_f32 v38, v38\n"
"v_mfma_f32_16x16x32_bf16 v[4:7], a[68:71], a[20:23], v[4:7]\n"
"ds_read_b128 a[120:123], v116 offset:28672\n"
"ds_read_b64_tr_b16 a[228:229], v121 offset:0\n"
"v_mfma_f32_16x16x32_bf16 v[8:11], a[68:71], a[36:39], v[8:11]\n"
"v_exp_f32 v39, v39\n"
"v_mfma_f32_16x16x32_bf16 v[12:15], a[68:71], a[52:55], v[12:15]\n"
"ds_read_b128 a[124:127], v117 offset:28672\n"
"v_cvt_pk_bf16_f32 v77, v38, v39\n"
"v_mfma_f32_16x16x32_bf16 v[144:147], a[132:135], v[64:67], v[144:147]\n"
"v_exp_f32 v40, v40\n"
"v_mfma_f32_16x16x32_bf16 v[148:151], a[132:135], v[72:75], v[148:151]\n"
"ds_read_b64_tr_b16 a[160:161], v112 offset:8192\n"
"ds_read_b64_tr_b16 a[230:231], v121 offset:256\n"
"v_mfma_f32_16x16x32_bf16 v[152:155], a[132:135], v[80:83], v[152:155]\n"
"v_exp_f32 v41, v41\n"
"v_mfma_f32_16x16x32_bf16 v[156:159], a[132:135], v[88:91], v[156:159]\n"
"ds_read_b64_tr_b16 a[162:163], v112 offset:8448\n"
"v_cvt_pk_bf16_f32 v84, v40, v41\n"
"v_mfma_f32_16x16x32_bf16 v[0:3], a[72:75], a[8:11], v[0:3]\n"
"v_exp_f32 v42, v42\n"
"v_mfma_f32_16x16x32_bf16 v[4:7], a[72:75], a[24:27], v[4:7]\n"
"ds_read_b64_tr_b16 a[164:165], v113 offset:8192\n"
"ds_read_b64_tr_b16 a[232:233], v121 offset:8192\n"
"v_mfma_f32_16x16x32_bf16 v[8:11], a[72:75], a[40:43], v[8:11]\n"
"v_exp_f32 v43, v43\n"
"v_mfma_f32_16x16x32_bf16 v[12:15], a[72:75], a[56:59], v[12:15]\n"
"ds_read_b64_tr_b16 a[166:167], v113 offset:8448\n"
"v_cvt_pk_bf16_f32 v85, v42, v43\n"
"v_mfma_f32_16x16x32_bf16 v[160:163], a[136:139], v[64:67], v[160:163]\n"
"v_exp_f32 v44, v44\n"
"v_mfma_f32_16x16x32_bf16 v[164:167], a[136:139], v[72:75], v[164:167]\n"
"ds_read_b64_tr_b16 a[168:169], v112 offset:8704\n"
"ds_read_b64_tr_b16 a[234:235], v121 offset:8448\n"
"v_mfma_f32_16x16x32_bf16 v[168:171], a[136:139], v[80:83], v[168:171]\n"
"v_exp_f32 v45, v45\n"
"v_mfma_f32_16x16x32_bf16 v[172:175], a[136:139], v[88:91], v[172:175]\n"
"ds_read_b64_tr_b16 a[170:171], v112 offset:8960\n"
"v_cvt_pk_bf16_f32 v92, v44, v45\n"
"v_mfma_f32_16x16x32_bf16 v[0:3], a[76:79], a[12:15], v[0:3]\n"
"v_exp_f32 v46, v46\n"
"v_mfma_f32_16x16x32_bf16 v[4:7], a[76:79], a[28:31], v[4:7]\n"
"ds_read_b64_tr_b16 a[172:173], v113 offset:8704\n"
"ds_read_b64_tr_b16 a[236:237], v121 offset:16384\n"
"v_mfma_f32_16x16x32_bf16 v[8:11], a[76:79], a[44:47], v[8:11]\n"
"v_exp_f32 v47, v47\n"
"v_mfma_f32_16x16x32_bf16 v[12:15], a[76:79], a[60:63], v[12:15]\n"
"ds_read_b64_tr_b16 a[174:175], v113 offset:8960\n"
"v_cvt_pk_bf16_f32 v93, v46, v47\n"
"v_mfma_f32_16x16x32_bf16 v[176:179], a[140:143], v[64:67], v[176:179]\n"
"v_exp_f32 v48, v48\n"
"v_mfma_f32_16x16x32_bf16 v[180:183], a[140:143], v[72:75], v[180:183]\n"
"ds_read_b64_tr_b16 a[176:177], v112 offset:9216\n"
"ds_read_b64_tr_b16 a[238:239], v121 offset:16640\n"
"v_mfma_f32_16x16x32_bf16 v[184:187], a[140:143], v[80:83], v[184:187]\n"
"v_exp_f32 v49, v49\n"
"v_mfma_f32_16x16x32_bf16 v[188:191], a[140:143], v[88:91], v[188:191]\n"
"ds_read_b64_tr_b16 a[178:179], v112 offset:9472\n"
"v_cvt_pk_bf16_f32 v70, v48, v49\n"
"v_mfma_f32_16x16x32_bf16 v[16:19], a[80:83], a[0:3], v[96:99]\n"
"v_exp_f32 v50, v50\n"
"v_mfma_f32_16x16x32_bf16 v[20:23], a[80:83], a[16:19], v[100:103]\n"
"ds_read_b64_tr_b16 a[180:181], v113 offset:9216\n"
"ds_read_b64_tr_b16 a[240:241], v121 offset:24576\n"
"v_mfma_f32_16x16x32_bf16 v[24:27], a[80:83], a[32:35], v[104:107]\n"
"v_exp_f32 v51, v51\n"
"v_mfma_f32_16x16x32_bf16 v[28:31], a[80:83], a[48:51], v[108:111]\n"
"ds_read_b64_tr_b16 a[182:183], v113 offset:9472\n"
"v_cvt_pk_bf16_f32 v71, v50, v51\n"
"v_mfma_f32_16x16x32_bf16 v[192:195], a[144:147], v[64:67], v[192:195]\n"
"v_exp_f32 v52, v52\n"
"v_mfma_f32_16x16x32_bf16 v[196:199], a[144:147], v[72:75], v[196:199]\n"
"ds_read_b64_tr_b16 a[184:185], v112 offset:9728\n"
"ds_read_b64_tr_b16 a[242:243], v121 offset:24832\n"
"v_mfma_f32_16x16x32_bf16 v[200:203], a[144:147], v[80:83], v[200:203]\n"
"s_and_b32 s73, s93, 7\n"
"v_exp_f32 v53, v53\n"
"v_mfma_f32_16x16x32_bf16 v[204:207], a[144:147], v[88:91], v[204:207]\n"
"s_mul_i32 s73, s73, 57344\n"
"ds_read_b64_tr_b16 a[186:187], v112 offset:9984\n"
"v_cvt_pk_bf16_f32 v78, v52, v53\n"
"v_mfma_f32_16x16x32_bf16 v[16:19], a[84:87], a[4:7], v[16:19]\n"
"s_lshr_b32 s74, s93, 3\n"
"v_exp_f32 v54, v54\n"
"v_mfma_f32_16x16x32_bf16 v[20:23], a[84:87], a[20:23], v[20:23]\n"
"s_lshl_b32 s74, s74, 2\n"
"s_add_u32 s74, s74, s68\n"
"ds_read_b64_tr_b16 a[188:189], v113 offset:9728\n"
"v_mfma_f32_16x16x32_bf16 v[24:27], a[84:87], a[36:39], v[24:27]\n"
"s_lshl_b32 s74, s74, 7\n"
"v_exp_f32 v55, v55\n"
"v_mfma_f32_16x16x32_bf16 v[28:31], a[84:87], a[52:55], v[28:31]\n"
"s_add_u32 s90, s73, s74\n"
"ds_read_b64_tr_b16 a[190:191], v113 offset:9984\n"
"v_cvt_pk_bf16_f32 v79, v54, v55\n"
"v_mfma_f32_16x16x32_bf16 v[208:211], a[148:151], v[64:67], v[208:211]\n"
"s_add_u32 s90, s90, 192512\n"
"v_exp_f32 v56, v56\n"
"v_mfma_f32_16x16x32_bf16 v[212:215], a[148:151], v[72:75], v[212:215]\n"
"buffer_store_dwordx4 a[244:247], v119, s[60:63], s90 offen offset:0 sc1\n"
"v_mfma_f32_16x16x32_bf16 v[216:219], a[148:151], v[80:83], v[216:219]\n"
"v_exp_f32 v57, v57\n"
"v_mfma_f32_16x16x32_bf16 v[220:223], a[148:151], v[88:91], v[220:223]\n"
"v_cvt_pk_bf16_f32 v86, v56, v57\n"
"v_lshlrev_b32 v125, 1, v124\n"
"v_mfma_f32_16x16x32_bf16 v[16:19], a[88:91], a[8:11], v[16:19]\n"
"s_lshr_b32 s73, s93, 3\n"
"v_exp_f32 v58, v58\n"
"v_mfma_f32_16x16x32_bf16 v[20:23], a[88:91], a[24:27], v[20:23]\n"
"s_lshl_b32 s73, s73, 2\n"
"s_add_u32 s73, s73, s68\n"
"s_lshl_b32 s73, s73, 14\n"
"v_mfma_f32_16x16x32_bf16 v[24:27], a[88:91], a[40:43], v[24:27]\n"
"s_add_u32 s90, s73, 7168\n"
"v_exp_f32 v59, v59\n"
"v_mfma_f32_16x16x32_bf16 v[28:31], a[88:91], a[56:59], v[28:31]\n"
"buffer_load_dword a252, v125, s[36:39], s90 offen offset:0\n"
"v_cvt_pk_bf16_f32 v87, v58, v59\n"
"v_mfma_f32_16x16x32_bf16 v[224:227], a[152:155], v[64:67], v[224:227]\n"
"v_exp_f32 v60, v60\n"
"v_mfma_f32_16x16x32_bf16 v[228:231], a[152:155], v[72:75], v[228:231]\n"
"v_mfma_f32_16x16x32_bf16 v[232:235], a[152:155], v[80:83], v[232:235]\n"
"v_exp_f32 v61, v61\n"
"v_mfma_f32_16x16x32_bf16 v[236:239], a[152:155], v[88:91], v[236:239]\n"
"v_cvt_pk_bf16_f32 v94, v60, v61\n"
"v_mfma_f32_16x16x32_bf16 v[16:19], a[92:95], a[12:15], v[16:19]\n"
"v_exp_f32 v62, v62\n"
"v_mfma_f32_16x16x32_bf16 v[20:23], a[92:95], a[28:31], v[20:23]\n"
"v_mfma_f32_16x16x32_bf16 v[24:27], a[92:95], a[44:47], v[24:27]\n"
"v_exp_f32 v63, v63\n"
"v_mfma_f32_16x16x32_bf16 v[28:31], a[92:95], a[60:63], v[28:31]\n"
"v_cvt_pk_bf16_f32 v95, v62, v63\n"
"v_mfma_f32_16x16x32_bf16 v[240:243], a[156:159], v[64:67], v[240:243]\n"
"v_mfma_f32_16x16x32_bf16 v[244:247], a[156:159], v[72:75], v[244:247]\n"
"v_mfma_f32_16x16x32_bf16 v[248:251], a[156:159], v[80:83], v[248:251]\n"
"v_mfma_f32_16x16x32_bf16 v[252:255], a[156:159], v[88:91], v[252:255]\n"
"v_mfma_f32_16x16x32_bf16 a[196:199], a[192:195], v[64:67], a[196:199]\n"
"v_mfma_f32_16x16x32_bf16 a[200:203], a[192:195], v[72:75], a[200:203]\n"
"v_mfma_f32_16x16x32_bf16 a[204:207], a[192:195], v[80:83], a[204:207]\n"
"v_mfma_f32_16x16x32_bf16 a[208:211], a[192:195], v[88:91], a[208:211]\n"
"s_waitcnt vmcnt(12) lgkmcnt(0)\n"
"s_barrier\n"
"v_mfma_f32_16x16x32_bf16 a[248:251], a[228:231], a[212:215], 0\n"
"s_add_u32 m0, s71, 16384\n"
"v_exp_f32 v0, v0\n"
"v_mfma_f32_16x16x32_bf16 a[248:251], a[232:235], a[216:219], a[248:251]\n"
"s_mov_b32 s75, 114688\n"
"buffer_load_dwordx4 v118, s[48:51], s75 offen offset:0 lds\n"
"ds_read_b128 a[64:67], v114 offset:32768\n"
"v_mfma_f32_16x16x32_bf16 a[248:251], a[236:239], a[220:223], a[248:251]\n"
"v_exp_f32 v1, v1\n"
"v_mfma_f32_16x16x32_bf16 a[248:251], a[240:243], a[224:227], a[248:251]\n"
"ds_read_b128 a[68:71], v115 offset:32768\n"
"v_cvt_pk_bf16_f32 v64, v0, v1\n"
"v_mfma_f32_16x16x32_bf16 v[32:35], a[96:99], a[0:3], v[96:99]\n"
"s_add_u32 m0, s71, 20480\n"
"v_exp_f32 v2, v2\n"
"v_mfma_f32_16x16x32_bf16 v[36:39], a[96:99], a[16:19], v[100:103]\n"
"s_mov_b32 s75, 118784\n"
"buffer_load_dwordx4 v118, s[48:51], s75 offen offset:0 lds\n"
"ds_read_b128 a[72:75], v116 offset:32768\n"
"v_mfma_f32_16x16x32_bf16 v[40:43], a[96:99], a[32:35], v[104:107]\n"
"v_exp_f32 v3, v3\n"
"v_mfma_f32_16x16x32_bf16 v[44:47], a[96:99], a[48:51], v[108:111]\n"
"ds_read_b128 a[76:79], v117 offset:32768\n"
"v_cvt_pk_bf16_f32 v65, v2, v3\n"
"v_mfma_f32_16x16x32_bf16 v[128:131], a[160:163], v[68:71], v[128:131]\n"
"s_add_u32 m0, s71, 24576\n"
"v_exp_f32 v4, v4\n"
"v_mfma_f32_16x16x32_bf16 v[132:135], a[160:163], v[76:79], v[132:135]\n"
"s_mov_b32 s75, 122880\n"
"buffer_load_dwordx4 v118, s[48:51], s75 offen offset:0 lds\n"
"ds_read_b128 a[80:83], v114 offset:36864\n"
"v_mfma_f32_16x16x32_bf16 v[136:139], a[160:163], v[84:87], v[136:139]\n"
"v_exp_f32 v5, v5\n"
"v_mfma_f32_16x16x32_bf16 v[140:143], a[160:163], v[92:95], v[140:143]\n"
"ds_read_b128 a[84:87], v115 offset:36864\n"
"v_cvt_pk_bf16_f32 v72, v4, v5\n"
"v_mfma_f32_16x16x32_bf16 v[32:35], a[100:103], a[4:7], v[32:35]\n"
"s_add_u32 m0, s71, 28672\n"
"v_exp_f32 v6, v6\n"
"v_mfma_f32_16x16x32_bf16 v[36:39], a[100:103], a[20:23], v[36:39]\n"
"s_mov_b32 s75, 126976\n"
"buffer_load_dwordx4 v118, s[48:51], s75 offen offset:0 lds\n"
"ds_read_b128 a[88:91], v116 offset:36864\n"
"v_mfma_f32_16x16x32_bf16 v[40:43], a[100:103], a[36:39], v[40:43]\n"
"v_exp_f32 v7, v7\n"
"v_mfma_f32_16x16x32_bf16 v[44:47], a[100:103], a[52:55], v[44:47]\n"
"ds_read_b128 a[92:95], v117 offset:36864\n"
"v_cvt_pk_bf16_f32 v73, v6, v7\n"
"v_mfma_f32_16x16x32_bf16 v[144:147], a[164:167], v[68:71], v[144:147]\n"
"s_add_u32 m0, s72, 49152\n"
"v_exp_f32 v8, v8\n"
"v_mfma_f32_16x16x32_bf16 v[148:151], a[164:167], v[76:79], v[148:151]\n"
"s_mov_b32 s75, 99328\n"
"buffer_load_dwordx4 v122, s[52:55], s75 offen offset:0 lds\n"
"ds_read_b64_tr_b16 a[128:129], v112 offset:16384\n"
"v_mfma_f32_16x16x32_bf16 v[152:155], a[164:167], v[84:87], v[152:155]\n"
"v_exp_f32 v9, v9\n"
"v_mfma_f32_16x16x32_bf16 v[156:159], a[164:167], v[92:95], v[156:159]\n"
"ds_read_b64_tr_b16 a[130:131], v112 offset:16640\n"
"v_cvt_pk_bf16_f32 v80, v8, v9\n"
"v_mfma_f32_16x16x32_bf16 v[32:35], a[104:107], a[8:11], v[32:35]\n"
"v_exp_f32 v10, v10\n"
"v_mfma_f32_16x16x32_bf16 v[36:39], a[104:107], a[24:27], v[36:39]\n"
"s_mov_b32 s75, 98432\n"
"buffer_load_dwordx4 v122, s[52:55], s75 offen offset:1024 lds\n"
"ds_read_b64_tr_b16 a[132:133], v113 offset:16384\n"
"v_mfma_f32_16x16x32_bf16 v[40:43], a[104:107], a[40:43], v[40:43]\n"
"v_exp_f32 v11, v11\n"
"v_mfma_f32_16x16x32_bf16 v[44:47], a[104:107], a[56:59], v[44:47]\n"
"ds_read_b64_tr_b16 a[134:135], v113 offset:16640\n"
"v_cvt_pk_bf16_f32 v81, v10, v11\n"
"v_mfma_f32_16x16x32_bf16 v[160:163], a[168:171], v[68:71], v[160:163]\n"
"s_add_u32 m0, s72, 57344\n"
"v_exp_f32 v12, v12\n"
"v_mfma_f32_16x16x32_bf16 v[164:167], a[168:171], v[76:79], v[164:167]\n"
"s_mov_b32 s75, 107520\n"
"buffer_load_dwordx4 v122, s[52:55], s75 offen offset:0 lds\n"
"ds_read_b64_tr_b16 a[136:137], v112 offset:16896\n"
"v_mfma_f32_16x16x32_bf16 v[168:171], a[168:171], v[84:87], v[168:171]\n"
"v_exp_f32 v13, v13\n"
"v_mfma_f32_16x16x32_bf16 v[172:175], a[168:171], v[92:95], v[172:175]\n"
"ds_read_b64_tr_b16 a[138:139], v112 offset:17152\n"
"v_cvt_pk_bf16_f32 v88, v12, v13\n"
"v_mfma_f32_16x16x32_bf16 v[32:35], a[108:111], a[12:15], v[32:35]\n"
"v_exp_f32 v14, v14\n"
"v_mfma_f32_16x16x32_bf16 v[36:39], a[108:111], a[28:31], v[36:39]\n"
"s_mov_b32 s75, 106624\n"
"buffer_load_dwordx4 v122, s[52:55], s75 offen offset:1024 lds\n"
"ds_read_b64_tr_b16 a[140:141], v113 offset:16896\n"
"v_mfma_f32_16x16x32_bf16 v[40:43], a[108:111], a[44:47], v[40:43]\n"
"v_exp_f32 v15, v15\n"
"v_mfma_f32_16x16x32_bf16 v[44:47], a[108:111], a[60:63], v[44:47]\n"
"ds_read_b64_tr_b16 a[142:143], v113 offset:17152\n"
"v_cvt_pk_bf16_f32 v89, v14, v15\n"
"v_mfma_f32_16x16x32_bf16 v[176:179], a[172:175], v[68:71], v[176:179]\n"
"s_and_b32 s73, s93, 7\n"
"v_exp_f32 v16, v16\n"
"v_mfma_f32_16x16x32_bf16 v[180:183], a[172:175], v[76:79], v[180:183]\n"
"s_mul_i32 s73, s73, 57344\n"
"s_lshr_b32 s74, s93, 3\n"
"ds_read_b64_tr_b16 a[144:145], v112 offset:17408\n"
"v_mfma_f32_16x16x32_bf16 v[184:187], a[172:175], v[84:87], v[184:187]\n"
"s_lshl_b32 s74, s74, 2\n"
"v_exp_f32 v17, v17\n"
"v_mfma_f32_16x16x32_bf16 v[188:191], a[172:175], v[92:95], v[188:191]\n"
"s_add_u32 s74, s74, s68\n"
"ds_read_b64_tr_b16 a[146:147], v112 offset:17664\n"
"v_cvt_pk_bf16_f32 v66, v16, v17\n"
"v_mfma_f32_16x16x32_bf16 v[48:51], a[112:115], a[0:3], v[96:99]\n"
"s_lshl_b32 s74, s74, 7\n"
"v_exp_f32 v18, v18\n"
"v_mfma_f32_16x16x32_bf16 v[52:55], a[112:115], a[16:19], v[100:103]\n"
"s_add_u32 s90, s73, s74\n"
"s_add_u32 s90, s90, 192512\n"
"ds_read_b64_tr_b16 a[148:149], v113 offset:17408\n"
"v_mfma_f32_16x16x32_bf16 v[56:59], a[112:115], a[32:35], v[104:107]\n"
"v_exp_f32 v19, v19\n"
"v_mfma_f32_16x16x32_bf16 v[60:63], a[112:115], a[48:51], v[108:111]\n"
"ds_read_b64_tr_b16 a[150:151], v113 offset:17664\n"
"v_cvt_pk_bf16_f32 v67, v18, v19\n"
"v_mfma_f32_16x16x32_bf16 v[192:195], a[176:179], v[68:71], v[192:195]\n"
"v_exp_f32 v20, v20\n"
"v_mfma_f32_16x16x32_bf16 v[196:199], a[176:179], v[76:79], v[196:199]\n"
"ds_read_b64_tr_b16 a[152:153], v112 offset:17920\n"
"buffer_store_dwordx4 a[248:251], v119, s[60:63], s90 offen offset:64 sc1\n"
"v_mfma_f32_16x16x32_bf16 v[200:203], a[176:179], v[84:87], v[200:203]\n"
"v_exp_f32 v21, v21\n"
"v_mfma_f32_16x16x32_bf16 v[204:207], a[176:179], v[92:95], v[204:207]\n"
"ds_read_b64_tr_b16 a[154:155], v112 offset:18176\n"
"v_cvt_pk_bf16_f32 v74, v20, v21\n"
"v_mfma_f32_16x16x32_bf16 v[48:51], a[116:119], a[4:7], v[48:51]\n"
"v_exp_f32 v22, v22\n"
"v_mfma_f32_16x16x32_bf16 v[52:55], a[116:119], a[20:23], v[52:55]\n"
"ds_read_b64_tr_b16 a[156:157], v113 offset:17920\n"
"v_lshlrev_b32 v125, 1, v124\n"
"v_mfma_f32_16x16x32_bf16 v[56:59], a[116:119], a[36:39], v[56:59]\n"
"s_lshr_b32 s73, s93, 3\n"
"v_exp_f32 v23, v23\n"
"v_mfma_f32_16x16x32_bf16 v[60:63], a[116:119], a[52:55], v[60:63]\n"
"s_lshl_b32 s73, s73, 2\n"
"ds_read_b64_tr_b16 a[158:159], v113 offset:18176\n"
"v_cvt_pk_bf16_f32 v75, v22, v23\n"
"v_mfma_f32_16x16x32_bf16 v[208:211], a[180:183], v[68:71], v[208:211]\n"
"s_add_u32 s73, s73, s68\n"
"v_exp_f32 v24, v24\n"
"v_mfma_f32_16x16x32_bf16 v[212:215], a[180:183], v[76:79], v[212:215]\n"
"s_lshl_b32 s73, s73, 14\n"
"s_add_u32 s90, s73, 9216\n"
"buffer_load_dword a252, v125, s[36:39], s90 offen offset:0\n"
"v_mfma_f32_16x16x32_bf16 v[216:219], a[180:183], v[84:87], v[216:219]\n"
"v_exp_f32 v25, v25\n"
"v_mfma_f32_16x16x32_bf16 v[220:223], a[180:183], v[92:95], v[220:223]\n"
"v_cvt_pk_bf16_f32 v82, v24, v25\n"
"v_mfma_f32_16x16x32_bf16 v[48:51], a[120:123], a[8:11], v[48:51]\n"
"v_exp_f32 v26, v26\n"
"v_mfma_f32_16x16x32_bf16 v[52:55], a[120:123], a[24:27], v[52:55]\n"
"v_mfma_f32_16x16x32_bf16 v[56:59], a[120:123], a[40:43], v[56:59]\n"
"v_exp_f32 v27, v27\n"
"v_mfma_f32_16x16x32_bf16 v[60:63], a[120:123], a[56:59], v[60:63]\n"
"v_cvt_pk_bf16_f32 v83, v26, v27\n"
"v_mfma_f32_16x16x32_bf16 v[224:227], a[184:187], v[68:71], v[224:227]\n"
"v_exp_f32 v28, v28\n"
"v_mfma_f32_16x16x32_bf16 v[228:231], a[184:187], v[76:79], v[228:231]\n"
"v_mfma_f32_16x16x32_bf16 v[232:235], a[184:187], v[84:87], v[232:235]\n"
"v_exp_f32 v29, v29\n"
"v_mfma_f32_16x16x32_bf16 v[236:239], a[184:187], v[92:95], v[236:239]\n"
"v_cvt_pk_bf16_f32 v90, v28, v29\n"
"v_mfma_f32_16x16x32_bf16 v[48:51], a[124:127], a[12:15], v[48:51]\n"
"v_exp_f32 v30, v30\n"
"v_mfma_f32_16x16x32_bf16 v[52:55], a[124:127], a[28:31], v[52:55]\n"
"v_mfma_f32_16x16x32_bf16 v[56:59], a[124:127], a[44:47], v[56:59]\n"
"v_exp_f32 v31, v31\n"
"v_mfma_f32_16x16x32_bf16 v[60:63], a[124:127], a[60:63], v[60:63]\n"
"v_cvt_pk_bf16_f32 v91, v30, v31\n"
"v_mfma_f32_16x16x32_bf16 v[240:243], a[188:191], v[68:71], v[240:243]\n"
"v_mfma_f32_16x16x32_bf16 v[244:247], a[188:191], v[76:79], v[244:247]\n"
"v_mfma_f32_16x16x32_bf16 v[248:251], a[188:191], v[84:87], v[248:251]\n"
"v_mfma_f32_16x16x32_bf16 v[252:255], a[188:191], v[92:95], v[252:255]\n"
"v_mfma_f32_16x16x32_bf16 a[196:199], a[192:195], v[68:71], a[196:199]\n"
"v_mfma_f32_16x16x32_bf16 a[200:203], a[192:195], v[76:79], a[200:203]\n"
"v_mfma_f32_16x16x32_bf16 a[204:207], a[192:195], v[84:87], a[204:207]\n"
"v_mfma_f32_16x16x32_bf16 a[208:211], a[192:195], v[92:95], a[208:211]\n"
"s_waitcnt lgkmcnt(0)\n"
"v_mfma_f32_16x16x32_bf16 v[0:3], a[64:67], a[0:3], v[96:99]\n"
"v_exp_f32 v32, v32\n"
"v_mfma_f32_16x16x32_bf16 v[4:7], a[64:67], a[16:19], v[100:103]\n"
"ds_read_b128 a[96:99], v114 offset:40960\n"
"v_lshlrev_b32 v125, 1, v124\n"
"v_mfma_f32_16x16x32_bf16 v[8:11], a[64:67], a[32:35], v[104:107]\n"
"s_lshr_b32 s73, s93, 3\n"
"v_exp_f32 v33, v33\n"
"v_mfma_f32_16x16x32_bf16 v[12:15], a[64:67], a[48:51], v[108:111]\n"
"s_lshl_b32 s73, s73, 2\n"
"ds_read_b128 a[100:103], v115 offset:40960\n"
"v_cvt_pk_bf16_f32 v68, v32, v33\n"
"v_mfma_f32_16x16x32_bf16 v[128:131], a[128:131], v[64:67], v[128:131]\n"
"s_add_u32 s73, s73, s68\n"
"v_exp_f32 v34, v34\n"
"v_mfma_f32_16x16x32_bf16 v[132:135], a[128:131], v[72:75], v[132:135]\n"
"s_lshl_b32 s73, s73, 14\n"
"s_add_u32 s90, s73, 11264\n"
"ds_read_b128 a[104:107], v116 offset:40960\n"
"v_mfma_f32_16x16x32_bf16 v[136:139], a[128:131], v[80:83], v[136:139]\n"
"v_exp_f32 v35, v35\n"
"v_mfma_f32_16x16x32_bf16 v[140:143], a[128:131], v[88:91], v[140:143]\n"
"ds_read_b128 a[108:111], v117 offset:40960\n"
"v_cvt_pk_bf16_f32 v69, v34, v35\n"
"v_mfma_f32_16x16x32_bf16 v[0:3], a[68:71], a[4:7], v[0:3]\n"
"v_exp_f32 v36, v36\n"
"v_mfma_f32_16x16x32_bf16 v[4:7], a[68:71], a[20:23], v[4:7]\n"
"ds_read_b128 a[112:115], v114 offset:45056\n"
"buffer_load_dword a252, v125, s[36:39], s90 offen offset:0\n"
"v_mfma_f32_16x16x32_bf16 v[8:11], a[68:71], a[36:39], v[8:11]\n"
"v_exp_f32 v37, v37\n"
"v_mfma_f32_16x16x32_bf16 v[12:15], a[68:71], a[52:55], v[12:15]\n"
"ds_read_b128 a[116:119], v115 offset:45056\n"
"v_cvt_pk_bf16_f32 v76, v36, v37\n"
"v_mfma_f32_16x16x32_bf16 v[144:147], a[132:135], v[64:67], v[144:147]\n"
"v_exp_f32 v38, v38\n"
"v_mfma_f32_16x16x32_bf16 v[148:151], a[132:135], v[72:75], v[148:151]\n"
"ds_read_b128 a[120:123], v116 offset:45056\n"
"v_mfma_f32_16x16x32_bf16 v[152:155], a[132:135], v[80:83], v[152:155]\n"
"v_exp_f32 v39, v39\n"
"v_mfma_f32_16x16x32_bf16 v[156:159], a[132:135], v[88:91], v[156:159]\n"
"ds_read_b128 a[124:127], v117 offset:45056\n"
"v_cvt_pk_bf16_f32 v77, v38, v39\n"
"v_mfma_f32_16x16x32_bf16 v[0:3], a[72:75], a[8:11], v[0:3]\n"
"v_exp_f32 v40, v40\n"
"v_mfma_f32_16x16x32_bf16 v[4:7], a[72:75], a[24:27], v[4:7]\n"
"s_add_u32 s90, s73, 13312\n"
"ds_read_b64_tr_b16 a[160:161], v112 offset:24576\n"
"buffer_load_dword a252, v125, s[36:39], s90 offen offset:0\n"
"v_mfma_f32_16x16x32_bf16 v[8:11], a[72:75], a[40:43], v[8:11]\n"
"v_exp_f32 v41, v41\n"
"v_mfma_f32_16x16x32_bf16 v[12:15], a[72:75], a[56:59], v[12:15]\n"
"ds_read_b64_tr_b16 a[162:163], v112 offset:24832\n"
"v_cvt_pk_bf16_f32 v84, v40, v41\n"
"v_mfma_f32_16x16x32_bf16 v[160:163], a[136:139], v[64:67], v[160:163]\n"
"v_exp_f32 v42, v42\n"
"v_mfma_f32_16x16x32_bf16 v[164:167], a[136:139], v[72:75], v[164:167]\n"
"ds_read_b64_tr_b16 a[164:165], v113 offset:24576\n"
"v_mfma_f32_16x16x32_bf16 v[168:171], a[136:139], v[80:83], v[168:171]\n"
"v_exp_f32 v43, v43\n"
"v_mfma_f32_16x16x32_bf16 v[172:175], a[136:139], v[88:91], v[172:175]\n"
"ds_read_b64_tr_b16 a[166:167], v113 offset:24832\n"
"v_cvt_pk_bf16_f32 v85, v42, v43\n"
"v_mfma_f32_16x16x32_bf16 v[0:3], a[76:79], a[12:15], v[0:3]\n"
"v_exp_f32 v44, v44\n"
"v_mfma_f32_16x16x32_bf16 v[4:7], a[76:79], a[28:31], v[4:7]\n"
"ds_read_b64_tr_b16 a[168:169], v112 offset:25088\n"
"v_mfma_f32_16x16x32_bf16 v[8:11], a[76:79], a[44:47], v[8:11]\n"
"s_add_u32 m0, s69, 155648\n"
"v_exp_f32 v45, v45\n"
"v_mfma_f32_16x16x32_bf16 v[12:15], a[76:79], a[60:63], v[12:15]\n"
"s_mov_b32 s75, 197632\n"
"ds_read_b64_tr_b16 a[170:171], v112 offset:25344\n"
"v_cvt_pk_bf16_f32 v92, v44, v45\n"
"v_mfma_f32_16x16x32_bf16 v[176:179], a[140:143], v[64:67], v[176:179]\n"
"v_exp_f32 v46, v46\n"
"v_mfma_f32_16x16x32_bf16 v[180:183], a[140:143], v[72:75], v[180:183]\n"
"ds_read_b64_tr_b16 a[172:173], v113 offset:25088\n"
"buffer_load_dwordx4 v126, s[20:23], s75 offen offset:0 nt lds\n"
"v_mfma_f32_16x16x32_bf16 v[184:187], a[140:143], v[80:83], v[184:187]\n"
"s_mov_b32 s75, 196736\n"
"v_exp_f32 v47, v47\n"
"v_mfma_f32_16x16x32_bf16 v[188:191], a[140:143], v[88:91], v[188:191]\n"
"ds_read_b64_tr_b16 a[174:175], v113 offset:25344\n"
"v_cvt_pk_bf16_f32 v93, v46, v47\n"
"v_mfma_f32_16x16x32_bf16 v[16:19], a[80:83], a[0:3], v[96:99]\n"
"v_exp_f32 v48, v48\n"
"v_mfma_f32_16x16x32_bf16 v[20:23], a[80:83], a[16:19], v[100:103]\n"
"ds_read_b64_tr_b16 a[176:177], v112 offset:25600\n"
"buffer_load_dwordx4 v126, s[20:23], s75 offen offset:1024 nt lds\n"
"v_mfma_f32_16x16x32_bf16 v[24:27], a[80:83], a[32:35], v[104:107]\n"
"v_exp_f32 v49, v49\n"
"v_mfma_f32_16x16x32_bf16 v[28:31], a[80:83], a[48:51], v[108:111]\n"
"ds_read_b64_tr_b16 a[178:179], v112 offset:25856\n"
"v_cvt_pk_bf16_f32 v70, v48, v49\n"
"v_mfma_f32_16x16x32_bf16 v[192:195], a[144:147], v[64:67], v[192:195]\n"
"v_exp_f32 v50, v50\n"
"v_mfma_f32_16x16x32_bf16 v[196:199], a[144:147], v[72:75], v[196:199]\n"
"ds_read_b64_tr_b16 a[180:181], v113 offset:25600\n"
"v_mfma_f32_16x16x32_bf16 v[200:203], a[144:147], v[80:83], v[200:203]\n"
"v_exp_f32 v51, v51\n"
"v_mfma_f32_16x16x32_bf16 v[204:207], a[144:147], v[88:91], v[204:207]\n"
"ds_read_b64_tr_b16 a[182:183], v113 offset:25856\n"
"v_cvt_pk_bf16_f32 v71, v50, v51\n"
"v_mfma_f32_16x16x32_bf16 v[16:19], a[84:87], a[4:7], v[16:19]\n"
"v_exp_f32 v52, v52\n"
"v_mfma_f32_16x16x32_bf16 v[20:23], a[84:87], a[20:23], v[20:23]\n"
"ds_read_b64_tr_b16 a[184:185], v112 offset:26112\n"
"v_mfma_f32_16x16x32_bf16 v[24:27], a[84:87], a[36:39], v[24:27]\n"
"v_exp_f32 v53, v53\n"
"v_mfma_f32_16x16x32_bf16 v[28:31], a[84:87], a[52:55], v[28:31]\n"
"ds_read_b64_tr_b16 a[186:187], v112 offset:26368\n"
"v_cvt_pk_bf16_f32 v78, v52, v53\n"
"v_mfma_f32_16x16x32_bf16 v[208:211], a[148:151], v[64:67], v[208:211]\n"
"v_exp_f32 v54, v54\n"
"v_mfma_f32_16x16x32_bf16 v[212:215], a[148:151], v[72:75], v[212:215]\n"
"ds_read_b64_tr_b16 a[188:189], v113 offset:26112\n"
"v_mfma_f32_16x16x32_bf16 v[216:219], a[148:151], v[80:83], v[216:219]\n"
"v_exp_f32 v55, v55\n"
"v_mfma_f32_16x16x32_bf16 v[220:223], a[148:151], v[88:91], v[220:223]\n"
"ds_read_b64_tr_b16 a[190:191], v113 offset:26368\n"
"v_cvt_pk_bf16_f32 v79, v54, v55\n"
"v_mfma_f32_16x16x32_bf16 v[16:19], a[88:91], a[8:11], v[16:19]\n"
"v_exp_f32 v56, v56\n"
"v_mfma_f32_16x16x32_bf16 v[20:23], a[88:91], a[24:27], v[20:23]\n"
"v_mfma_f32_16x16x32_bf16 v[24:27], a[88:91], a[40:43], v[24:27]\n"
"v_exp_f32 v57, v57\n"
"v_mfma_f32_16x16x32_bf16 v[28:31], a[88:91], a[56:59], v[28:31]\n"
"v_cvt_pk_bf16_f32 v86, v56, v57\n"
"v_mfma_f32_16x16x32_bf16 v[224:227], a[152:155], v[64:67], v[224:227]\n"
"v_exp_f32 v58, v58\n"
"v_mfma_f32_16x16x32_bf16 v[228:231], a[152:155], v[72:75], v[228:231]\n"
"v_mfma_f32_16x16x32_bf16 v[232:235], a[152:155], v[80:83], v[232:235]\n"
"v_exp_f32 v59, v59\n"
"v_mfma_f32_16x16x32_bf16 v[236:239], a[152:155], v[88:91], v[236:239]\n"
"v_cvt_pk_bf16_f32 v87, v58, v59\n"
"v_mfma_f32_16x16x32_bf16 v[16:19], a[92:95], a[12:15], v[16:19]\n"
"v_exp_f32 v60, v60\n"
"v_mfma_f32_16x16x32_bf16 v[20:23], a[92:95], a[28:31], v[20:23]\n"
"v_mfma_f32_16x16x32_bf16 v[24:27], a[92:95], a[44:47], v[24:27]\n"
"v_exp_f32 v61, v61\n"
"v_mfma_f32_16x16x32_bf16 v[28:31], a[92:95], a[60:63], v[28:31]\n"
"v_cvt_pk_bf16_f32 v94, v60, v61\n"
"v_mfma_f32_16x16x32_bf16 v[240:243], a[156:159], v[64:67], v[240:243]\n"
"v_exp_f32 v62, v62\n"
"v_mfma_f32_16x16x32_bf16 v[244:247], a[156:159], v[72:75], v[244:247]\n"
"v_mfma_f32_16x16x32_bf16 v[248:251], a[156:159], v[80:83], v[248:251]\n"
"v_exp_f32 v63, v63\n"
"v_mfma_f32_16x16x32_bf16 v[252:255], a[156:159], v[88:91], v[252:255]\n"
"v_cvt_pk_bf16_f32 v95, v62, v63\n"
"v_mfma_f32_16x16x32_bf16 a[196:199], a[192:195], v[64:67], a[196:199]\n"
"v_mfma_f32_16x16x32_bf16 a[200:203], a[192:195], v[72:75], a[200:203]\n"
"v_mfma_f32_16x16x32_bf16 a[204:207], a[192:195], v[80:83], a[204:207]\n"
"v_mfma_f32_16x16x32_bf16 a[208:211], a[192:195], v[88:91], a[208:211]\n"
"s_waitcnt vmcnt(14) lgkmcnt(0)\n"
"s_barrier\n"
"v_mfma_f32_16x16x32_bf16 v[32:35], a[96:99], a[0:3], v[96:99]\n"
"s_add_u32 m0, s72, 65536\n"
"v_exp_f32 v0, v0\n"
"v_mfma_f32_16x16x32_bf16 v[36:39], a[96:99], a[16:19], v[100:103]\n"
"s_mov_b32 s75, 115712\n"
"buffer_load_dwordx4 v122, s[52:55], s75 offen offset:0 lds\n"
"ds_read_b128 a[64:67], v114 offset:0\n"
"v_mfma_f32_16x16x32_bf16 v[40:43], a[96:99], a[32:35], v[104:107]\n"
"v_exp_f32 v1, v1\n"
"v_mfma_f32_16x16x32_bf16 v[44:47], a[96:99], a[48:51], v[108:111]\n"
"ds_read_b128 a[68:71], v115 offset:0\n"
"v_cvt_pk_bf16_f32 v64, v0, v1\n"
"v_mfma_f32_16x16x32_bf16 v[128:131], a[160:163], v[68:71], v[128:131]\n"
"v_exp_f32 v2, v2\n"
"v_mfma_f32_16x16x32_bf16 v[132:135], a[160:163], v[76:79], v[132:135]\n"
"s_mov_b32 s75, 114816\n"
"buffer_load_dwordx4 v122, s[52:55], s75 offen offset:1024 lds\n"
"ds_read_b128 a[72:75], v116 offset:0\n"
"v_mfma_f32_16x16x32_bf16 v[136:139], a[160:163], v[84:87], v[136:139]\n"
"v_exp_f32 v3, v3\n"
"v_mfma_f32_16x16x32_bf16 v[140:143], a[160:163], v[92:95], v[140:143]\n"
"ds_read_b128 a[76:79], v117 offset:0\n"
"v_cvt_pk_bf16_f32 v65, v2, v3\n"
"v_mfma_f32_16x16x32_bf16 v[32:35], a[100:103], a[4:7], v[32:35]\n"
"s_add_u32 m0, s72, 73728\n"
"v_exp_f32 v4, v4\n"
"v_mfma_f32_16x16x32_bf16 v[36:39], a[100:103], a[20:23], v[36:39]\n"
"s_mov_b32 s75, 123904\n"
"buffer_load_dwordx4 v122, s[52:55], s75 offen offset:0 lds\n"
"ds_read_b128 a[80:83], v114 offset:4096\n"
"v_mfma_f32_16x16x32_bf16 v[40:43], a[100:103], a[36:39], v[40:43]\n"
"v_exp_f32 v5, v5\n"
"v_mfma_f32_16x16x32_bf16 v[44:47], a[100:103], a[52:55], v[44:47]\n"
"ds_read_b128 a[84:87], v115 offset:4096\n"
"v_cvt_pk_bf16_f32 v72, v4, v5\n"
"v_mfma_f32_16x16x32_bf16 v[144:147], a[164:167], v[68:71], v[144:147]\n"
"v_exp_f32 v6, v6\n"
"v_mfma_f32_16x16x32_bf16 v[148:151], a[164:167], v[76:79], v[148:151]\n"
"s_mov_b32 s75, 123008\n"
"buffer_load_dwordx4 v122, s[52:55], s75 offen offset:1024 lds\n"
"ds_read_b128 a[88:91], v116 offset:4096\n"
"v_mfma_f32_16x16x32_bf16 v[152:155], a[164:167], v[84:87], v[152:155]\n"
"v_exp_f32 v7, v7\n"
"v_mfma_f32_16x16x32_bf16 v[156:159], a[164:167], v[92:95], v[156:159]\n"
"ds_read_b128 a[92:95], v117 offset:4096\n"
"v_cvt_pk_bf16_f32 v73, v6, v7\n"
"v_mfma_f32_16x16x32_bf16 v[32:35], a[104:107], a[8:11], v[32:35]\n"
"v_exp_f32 v8, v8\n"
"v_mfma_f32_16x16x32_bf16 v[36:39], a[104:107], a[24:27], v[36:39]\n"
"ds_read_b64_tr_b16 a[128:129], v112 offset:32768\n"
"v_lshlrev_b32 v125, 1, v124\n"
"v_mfma_f32_16x16x32_bf16 v[40:43], a[104:107], a[40:43], v[40:43]\n"
"s_lshr_b32 s73, s93, 3\n"
"v_exp_f32 v9, v9\n"
"v_mfma_f32_16x16x32_bf16 v[44:47], a[104:107], a[56:59], v[44:47]\n"
"s_lshl_b32 s73, s73, 2\n"
"ds_read_b64_tr_b16 a[130:131], v112 offset:33024\n"
"v_cvt_pk_bf16_f32 v80, v8, v9\n"
"v_mfma_f32_16x16x32_bf16 v[160:163], a[168:171], v[68:71], v[160:163]\n"
"s_add_u32 s73, s73, s68\n"
"v_exp_f32 v10, v10\n"
"v_mfma_f32_16x16x32_bf16 v[164:167], a[168:171], v[76:79], v[164:167]\n"
"s_lshl_b32 s73, s73, 14\n"
"s_add_u32 s90, s73, 15360\n"
"ds_read_b64_tr_b16 a[132:133], v113 offset:32768\n"
"v_mfma_f32_16x16x32_bf16 v[168:171], a[168:171], v[84:87], v[168:171]\n"
"v_exp_f32 v11, v11\n"
"v_mfma_f32_16x16x32_bf16 v[172:175], a[168:171], v[92:95], v[172:175]\n"
"ds_read_b64_tr_b16 a[134:135], v113 offset:33024\n"
"v_cvt_pk_bf16_f32 v81, v10, v11\n"
"v_mfma_f32_16x16x32_bf16 v[32:35], a[108:111], a[12:15], v[32:35]\n"
"v_exp_f32 v12, v12\n"
"v_mfma_f32_16x16x32_bf16 v[36:39], a[108:111], a[28:31], v[36:39]\n"
"ds_read_b64_tr_b16 a[136:137], v112 offset:33280\n"
"buffer_load_dword a252, v125, s[36:39], s90 offen offset:0\n"
"v_mfma_f32_16x16x32_bf16 v[40:43], a[108:111], a[44:47], v[40:43]\n"
"v_exp_f32 v13, v13\n"
"v_mfma_f32_16x16x32_bf16 v[44:47], a[108:111], a[60:63], v[44:47]\n"
"ds_read_b64_tr_b16 a[138:139], v112 offset:33536\n"
"v_cvt_pk_bf16_f32 v88, v12, v13\n"
"v_mfma_f32_16x16x32_bf16 v[176:179], a[172:175], v[68:71], v[176:179]\n"
"v_exp_f32 v14, v14\n"
"v_mfma_f32_16x16x32_bf16 v[180:183], a[172:175], v[76:79], v[180:183]\n"
"ds_read_b64_tr_b16 a[140:141], v113 offset:33280\n"
"v_mfma_f32_16x16x32_bf16 v[184:187], a[172:175], v[84:87], v[184:187]\n"
"v_exp_f32 v15, v15\n"
"v_mfma_f32_16x16x32_bf16 v[188:191], a[172:175], v[92:95], v[188:191]\n"
"ds_read_b64_tr_b16 a[142:143], v113 offset:33536\n"
"v_cvt_pk_bf16_f32 v89, v14, v15\n"
"v_mfma_f32_16x16x32_bf16 v[48:51], a[112:115], a[0:3], v[96:99]\n"
"v_exp_f32 v16, v16\n"
"v_mfma_f32_16x16x32_bf16 v[52:55], a[112:115], a[16:19], v[100:103]\n"
"ds_read_b64_tr_b16 a[144:145], v112 offset:33792\n"
"v_mfma_f32_16x16x32_bf16 v[56:59], a[112:115], a[32:35], v[104:107]\n"
"s_lshr_b32 s73, s93, 3\n"
"v_exp_f32 v17, v17\n"
"v_mfma_f32_16x16x32_bf16 v[60:63], a[112:115], a[48:51], v[108:111]\n"
"s_mul_i32 s73, s73, 512\n"
"ds_read_b64_tr_b16 a[146:147], v112 offset:34048\n"
"v_cvt_pk_bf16_f32 v66, v16, v17\n"
"v_mfma_f32_16x16x32_bf16 v[192:195], a[176:179], v[68:71], v[192:195]\n"
"s_add_u32 s73, s73, 5120\n"
"v_exp_f32 v18, v18\n"
"v_mfma_f32_16x16x32_bf16 v[196:199], a[176:179], v[76:79], v[196:199]\n"
"ds_read_b64_tr_b16 a[148:149], v113 offset:33792\n"
"v_lshrrev_b32 v125, 4, v124\n"
"v_mfma_f32_16x16x32_bf16 v[200:203], a[176:179], v[84:87], v[200:203]\n"
"v_exp_f32 v19, v19\n"
"v_mfma_f32_16x16x32_bf16 v[204:207], a[176:179], v[92:95], v[204:207]\n"
"ds_read_b64_tr_b16 a[150:151], v113 offset:34048\n"
"v_cvt_pk_bf16_f32 v67, v18, v19\n"
"v_mfma_f32_16x16x32_bf16 v[48:51], a[116:119], a[4:7], v[48:51]\n"
"v_exp_f32 v20, v20\n"
"v_mfma_f32_16x16x32_bf16 v[52:55], a[116:119], a[20:23], v[52:55]\n"
"ds_read_b64_tr_b16 a[152:153], v112 offset:34304\n"
"v_min_u32 v125, 3, v125\n"
"v_mfma_f32_16x16x32_bf16 v[56:59], a[116:119], a[36:39], v[56:59]\n"
"v_exp_f32 v21, v21\n"
"v_mfma_f32_16x16x32_bf16 v[60:63], a[116:119], a[52:55], v[60:63]\n"
"ds_read_b64_tr_b16 a[154:155], v112 offset:34560\n"
"v_cvt_pk_bf16_f32 v74, v20, v21\n"
"v_mfma_f32_16x16x32_bf16 v[208:211], a[180:183], v[68:71], v[208:211]\n"
"v_exp_f32 v22, v22\n"
"v_mfma_f32_16x16x32_bf16 v[212:215], a[180:183], v[76:79], v[212:215]\n"
"ds_read_b64_tr_b16 a[156:157], v113 offset:34304\n"
"v_lshlrev_b32 v125, 7, v125\n"
"v_mfma_f32_16x16x32_bf16 v[216:219], a[180:183], v[84:87], v[216:219]\n"
"v_exp_f32 v23, v23\n"
"v_mfma_f32_16x16x32_bf16 v[220:223], a[180:183], v[92:95], v[220:223]\n"
"ds_read_b64_tr_b16 a[158:159], v113 offset:34560\n"
"v_cvt_pk_bf16_f32 v75, v22, v23\n"
"v_mfma_f32_16x16x32_bf16 v[48:51], a[120:123], a[8:11], v[48:51]\n"
"v_exp_f32 v24, v24\n"
"v_mfma_f32_16x16x32_bf16 v[52:55], a[120:123], a[24:27], v[52:55]\n"
"v_add_u32 v125, s73, v125\n"
"v_mfma_f32_16x16x32_bf16 v[56:59], a[120:123], a[40:43], v[56:59]\n"
"s_getpc_b64 s[88:89]\n"
"v_exp_f32 v25, v25\n"
"v_mfma_f32_16x16x32_bf16 v[60:63], a[120:123], a[56:59], v[60:63]\n"
"global_load_dword a252, v125, s[88:89]\n"
"v_cvt_pk_bf16_f32 v82, v24, v25\n"
"v_mfma_f32_16x16x32_bf16 v[224:227], a[184:187], v[68:71], v[224:227]\n"
"v_exp_f32 v26, v26\n"
"v_mfma_f32_16x16x32_bf16 v[228:231], a[184:187], v[76:79], v[228:231]\n"
"v_mfma_f32_16x16x32_bf16 v[232:235], a[184:187], v[84:87], v[232:235]\n"
"v_exp_f32 v27, v27\n"
"v_mfma_f32_16x16x32_bf16 v[236:239], a[184:187], v[92:95], v[236:239]\n"
"v_cvt_pk_bf16_f32 v83, v26, v27\n"
"v_mfma_f32_16x16x32_bf16 v[48:51], a[124:127], a[12:15], v[48:51]\n"
"v_exp_f32 v28, v28\n"
"v_mfma_f32_16x16x32_bf16 v[52:55], a[124:127], a[28:31], v[52:55]\n"
"v_mfma_f32_16x16x32_bf16 v[56:59], a[124:127], a[44:47], v[56:59]\n"
"v_exp_f32 v29, v29\n"
"v_mfma_f32_16x16x32_bf16 v[60:63], a[124:127], a[60:63], v[60:63]\n"
"v_cvt_pk_bf16_f32 v90, v28, v29\n"
"v_mfma_f32_16x16x32_bf16 v[240:243], a[188:191], v[68:71], v[240:243]\n"
"v_exp_f32 v30, v30\n"
"v_mfma_f32_16x16x32_bf16 v[244:247], a[188:191], v[76:79], v[244:247]\n"
"v_mfma_f32_16x16x32_bf16 v[248:251], a[188:191], v[84:87], v[248:251]\n"
"v_exp_f32 v31, v31\n"
"v_mfma_f32_16x16x32_bf16 v[252:255], a[188:191], v[92:95], v[252:255]\n"
"v_cvt_pk_bf16_f32 v91, v30, v31\n"
"v_mfma_f32_16x16x32_bf16 a[196:199], a[192:195], v[68:71], a[196:199]\n"
"v_mfma_f32_16x16x32_bf16 a[200:203], a[192:195], v[76:79], a[200:203]\n"
"v_mfma_f32_16x16x32_bf16 a[204:207], a[192:195], v[84:87], a[204:207]\n"
"v_mfma_f32_16x16x32_bf16 a[208:211], a[192:195], v[92:95], a[208:211]\n"
"s_waitcnt lgkmcnt(0)\n"
"v_mfma_f32_16x16x32_bf16 v[0:3], a[64:67], a[0:3], v[96:99]\n"
"s_add_u32 m0, s69, 131072\n"
"v_exp_f32 v32, v32\n"
"v_mfma_f32_16x16x32_bf16 v[4:7], a[64:67], a[16:19], v[100:103]\n"
"ds_read_b128 a[96:99], v114 offset:8192\n"
"v_mfma_f32_16x16x32_bf16 v[8:11], a[64:67], a[32:35], v[104:107]\n"
"v_exp_f32 v33, v33\n"
"v_mfma_f32_16x16x32_bf16 v[12:15], a[64:67], a[48:51], v[108:111]\n"
"ds_read_b128 a[100:103], v115 offset:8192\n"
"v_cvt_pk_bf16_f32 v68, v32, v33\n"
"v_mfma_f32_16x16x32_bf16 v[128:131], a[128:131], v[64:67], v[128:131]\n"
"s_mov_b32 s75, 1024\n"
"v_exp_f32 v34, v34\n"
"v_mfma_f32_16x16x32_bf16 v[132:135], a[128:131], v[72:75], v[132:135]\n"
"ds_read_b128 a[104:107], v116 offset:8192\n"
"buffer_load_dwordx4 v126, s[20:23], s75 offen offset:0 nt lds\n"
"v_mfma_f32_16x16x32_bf16 v[136:139], a[128:131], v[80:83], v[136:139]\n"
"v_exp_f32 v35, v35\n"
"v_mfma_f32_16x16x32_bf16 v[140:143], a[128:131], v[88:91], v[140:143]\n"
"ds_read_b128 a[108:111], v117 offset:8192\n"
"v_cvt_pk_bf16_f32 v69, v34, v35\n"
"v_mfma_f32_16x16x32_bf16 v[0:3], a[68:71], a[4:7], v[0:3]\n"
"v_exp_f32 v36, v36\n"
"v_mfma_f32_16x16x32_bf16 v[4:7], a[68:71], a[20:23], v[4:7]\n"
"s_mov_b32 s75, 128\n"
"ds_read_b128 a[112:115], v114 offset:12288\n"
"buffer_load_dwordx4 v126, s[20:23], s75 offen offset:1024 nt lds\n"
"v_mfma_f32_16x16x32_bf16 v[8:11], a[68:71], a[36:39], v[8:11]\n"
"v_exp_f32 v37, v37\n"
"v_mfma_f32_16x16x32_bf16 v[12:15], a[68:71], a[52:55], v[12:15]\n"
"ds_read_b128 a[116:119], v115 offset:12288\n"
"v_cvt_pk_bf16_f32 v76, v36, v37\n"
"v_mfma_f32_16x16x32_bf16 v[144:147], a[132:135], v[64:67], v[144:147]\n"
"v_exp_f32 v38, v38\n"
"v_mfma_f32_16x16x32_bf16 v[148:151], a[132:135], v[72:75], v[148:151]\n"
"s_add_u32 m0, s69, 139264\n"
"ds_read_b128 a[120:123], v116 offset:12288\n"
"v_mfma_f32_16x16x32_bf16 v[152:155], a[132:135], v[80:83], v[152:155]\n"
"v_exp_f32 v39, v39\n"
"v_mfma_f32_16x16x32_bf16 v[156:159], a[132:135], v[88:91], v[156:159]\n"
"ds_read_b128 a[124:127], v117 offset:12288\n"
"v_cvt_pk_bf16_f32 v77, v38, v39\n"
"v_mfma_f32_16x16x32_bf16 v[0:3], a[72:75], a[8:11], v[0:3]\n"
"v_exp_f32 v40, v40\n"
"v_mfma_f32_16x16x32_bf16 v[4:7], a[72:75], a[24:27], v[4:7]\n"
"s_mov_b32 s75, 66560\n"
"ds_read_b64_tr_b16 a[160:161], v112 offset:40960\n"
"buffer_load_dwordx4 v126, s[20:23], s75 offen offset:0 nt lds\n"
"v_mfma_f32_16x16x32_bf16 v[8:11], a[72:75], a[40:43], v[8:11]\n"
"v_exp_f32 v41, v41\n"
"v_mfma_f32_16x16x32_bf16 v[12:15], a[72:75], a[56:59], v[12:15]\n"
"ds_read_b64_tr_b16 a[162:163], v112 offset:41216\n"
"v_cvt_pk_bf16_f32 v84, v40, v41\n"
"v_mfma_f32_16x16x32_bf16 v[160:163], a[136:139], v[64:67], v[160:163]\n"
"v_exp_f32 v42, v42\n"
"v_mfma_f32_16x16x32_bf16 v[164:167], a[136:139], v[72:75], v[164:167]\n"
"s_mov_b32 s75, 65664\n"
"ds_read_b64_tr_b16 a[164:165], v113 offset:40960\n"
"buffer_load_dwordx4 v126, s[20:23], s75 offen offset:1024 nt lds\n"
"v_mfma_f32_16x16x32_bf16 v[168:171], a[136:139], v[80:83], v[168:171]\n"
"v_exp_f32 v43, v43\n"
"v_mfma_f32_16x16x32_bf16 v[172:175], a[136:139], v[88:91], v[172:175]\n"
"ds_read_b64_tr_b16 a[166:167], v113 offset:41216\n"
"v_cvt_pk_bf16_f32 v85, v42, v43\n"
"v_mfma_f32_16x16x32_bf16 v[0:3], a[76:79], a[12:15], v[0:3]\n"
"v_exp_f32 v44, v44\n"
"v_mfma_f32_16x16x32_bf16 v[4:7], a[76:79], a[28:31], v[4:7]\n"
"ds_read_b64_tr_b16 a[168:169], v112 offset:41472\n"
"v_mfma_f32_16x16x32_bf16 v[8:11], a[76:79], a[44:47], v[8:11]\n"
"s_mov_b32 s77, 127\n"
"v_exp_f32 v45, v45\n"
"v_mfma_f32_16x16x32_bf16 v[12:15], a[76:79], a[60:63], v[12:15]\n"
"ds_read_b64_tr_b16 a[170:171], v112 offset:41728\n"
"v_cvt_pk_bf16_f32 v92, v44, v45\n"
"v_mfma_f32_16x16x32_bf16 v[176:179], a[140:143], v[64:67], v[176:179]\n"
"v_exp_f32 v46, v46\n"
"v_mfma_f32_16x16x32_bf16 v[180:183], a[140:143], v[72:75], v[180:183]\n"
"ds_read_b64_tr_b16 a[172:173], v113 offset:41472\n"
"v_subrev_u32 v121, 1024, v122\n"
"v_mfma_f32_16x16x32_bf16 v[184:187], a[140:143], v[80:83], v[184:187]\n"
"v_exp_f32 v47, v47\n"
"v_mfma_f32_16x16x32_bf16 v[188:191], a[140:143], v[88:91], v[188:191]\n"
"ds_read_b64_tr_b16 a[174:175], v113 offset:41728\n"
"v_cvt_pk_bf16_f32 v93, v46, v47\n"
"v_mfma_f32_16x16x32_bf16 v[16:19], a[80:83], a[0:3], v[96:99]\n"
"v_exp_f32 v48, v48\n"
"v_mfma_f32_16x16x32_bf16 v[20:23], a[80:83], a[16:19], v[100:103]\n"
"ds_read_b64_tr_b16 a[176:177], v112 offset:41984\n"
"v_lshrrev_b32 v121, 8, v121\n"
"v_mfma_f32_16x16x32_bf16 v[24:27], a[80:83], a[32:35], v[104:107]\n"
"v_exp_f32 v49, v49\n"
"v_mfma_f32_16x16x32_bf16 v[28:31], a[80:83], a[48:51], v[108:111]\n"
"ds_read_b64_tr_b16 a[178:179], v112 offset:42240\n"
"v_cvt_pk_bf16_f32 v70, v48, v49\n"
"v_mfma_f32_16x16x32_bf16 v[192:195], a[144:147], v[64:67], v[192:195]\n"
"v_exp_f32 v50, v50\n"
"v_mfma_f32_16x16x32_bf16 v[196:199], a[144:147], v[72:75], v[196:199]\n"
"ds_read_b64_tr_b16 a[180:181], v113 offset:41984\n"
"v_lshlrev_b32 v121, 11, v121\n"
"v_mfma_f32_16x16x32_bf16 v[200:203], a[144:147], v[80:83], v[200:203]\n"
"v_exp_f32 v51, v51\n"
"v_mfma_f32_16x16x32_bf16 v[204:207], a[144:147], v[88:91], v[204:207]\n"
"ds_read_b64_tr_b16 a[182:183], v113 offset:42240\n"
"v_cvt_pk_bf16_f32 v71, v50, v51\n"
"v_mfma_f32_16x16x32_bf16 v[16:19], a[84:87], a[4:7], v[16:19]\n"
"v_exp_f32 v52, v52\n"
"v_mfma_f32_16x16x32_bf16 v[20:23], a[84:87], a[20:23], v[20:23]\n"
"ds_read_b64_tr_b16 a[184:185], v112 offset:42496\n"
"v_and_or_b32 v121, v122, s77, v121\n"
"v_mfma_f32_16x16x32_bf16 v[24:27], a[84:87], a[36:39], v[24:27]\n"
"s_and_b32 s74, s93, 3\n"
"v_exp_f32 v53, v53\n"
"v_mfma_f32_16x16x32_bf16 v[28:31], a[84:87], a[52:55], v[28:31]\n"
"s_lshl_b32 s74, s74, 6\n"
"ds_read_b64_tr_b16 a[186:187], v112 offset:42752\n"
"v_cvt_pk_bf16_f32 v78, v52, v53\n"
"v_mfma_f32_16x16x32_bf16 v[208:211], a[148:151], v[64:67], v[208:211]\n"
"v_exp_f32 v54, v54\n"
"v_mfma_f32_16x16x32_bf16 v[212:215], a[148:151], v[72:75], v[212:215]\n"
"ds_read_b64_tr_b16 a[188:189], v113 offset:42496\n"
"v_xor_b32 v121, s74, v121\n"
"v_mfma_f32_16x16x32_bf16 v[216:219], a[148:151], v[80:83], v[216:219]\n"
"s_and_b32 s84, s91, 7\n"
"v_exp_f32 v55, v55\n"
"v_mfma_f32_16x16x32_bf16 v[220:223], a[148:151], v[88:91], v[220:223]\n"
"s_lshl_b32 s73, s84, 8\n"
"ds_read_b64_tr_b16 a[190:191], v113 offset:42752\n"
"v_cvt_pk_bf16_f32 v79, v54, v55\n"
"v_mfma_f32_16x16x32_bf16 v[16:19], a[88:91], a[8:11], v[16:19]\n"
"v_exp_f32 v56, v56\n"
"v_mfma_f32_16x16x32_bf16 v[20:23], a[88:91], a[24:27], v[20:23]\n"
"v_add_u32 v121, s73, v121\n"
"v_xor_b32 v118, 128, v121\n"
"v_mfma_f32_16x16x32_bf16 v[24:27], a[88:91], a[40:43], v[24:27]\n"
"v_exp_f32 v57, v57\n"
"v_mfma_f32_16x16x32_bf16 v[28:31], a[88:91], a[56:59], v[28:31]\n"
"v_cvt_pk_bf16_f32 v86, v56, v57\n"
"v_add_u32 v121, 1024, v121\n"
"v_mfma_f32_16x16x32_bf16 v[224:227], a[152:155], v[64:67], v[224:227]\n"
"s_lshr_b32 s73, s93, 3\n"
"v_exp_f32 v58, v58\n"
"v_mfma_f32_16x16x32_bf16 v[228:231], a[152:155], v[72:75], v[228:231]\n"
"s_mul_i32 s73, s73, 256\n"
"v_lshrrev_b32 v125, 4, v124\n"
"v_min_u32 v125, 1, v125\n"
"v_mfma_f32_16x16x32_bf16 v[232:235], a[152:155], v[80:83], v[232:235]\n"
"v_exp_f32 v59, v59\n"
"v_mfma_f32_16x16x32_bf16 v[236:239], a[152:155], v[88:91], v[236:239]\n"
"v_cvt_pk_bf16_f32 v87, v58, v59\n"
"v_lshlrev_b32 v125, 7, v125\n"
"v_mfma_f32_16x16x32_bf16 v[16:19], a[92:95], a[12:15], v[16:19]\n"
"v_exp_f32 v60, v60\n"
"v_mfma_f32_16x16x32_bf16 v[20:23], a[92:95], a[28:31], v[20:23]\n"
"v_add_u32 v125, s73, v125\n"
"v_mfma_f32_16x16x32_bf16 v[24:27], a[92:95], a[44:47], v[24:27]\n"
"s_getpc_b64 s[88:89]\nFK_H2:\ns_add_u32 s88, s88, FK_TAIL-FK_H2\ns_addc_u32 s89, s89, 0\n"
"v_exp_f32 v61, v61\n"
"v_mfma_f32_16x16x32_bf16 v[28:31], a[92:95], a[60:63], v[28:31]\n"
"global_load_dword a252, v125, s[88:89]\n"
"v_cvt_pk_bf16_f32 v94, v60, v61\n"
"v_mfma_f32_16x16x32_bf16 v[240:243], a[156:159], v[64:67], v[240:243]\n"
"v_exp_f32 v62, v62\n"
"v_mfma_f32_16x16x32_bf16 v[244:247], a[156:159], v[72:75], v[244:247]\n"
"v_mfma_f32_16x16x32_bf16 v[248:251], a[156:159], v[80:83], v[248:251]\n"
"v_exp_f32 v63, v63\n"
"v_mfma_f32_16x16x32_bf16 v[252:255], a[156:159], v[88:91], v[252:255]\n"
"v_cvt_pk_bf16_f32 v95, v62, v63\n"
"v_mfma_f32_16x16x32_bf16 a[196:199], a[192:195], v[64:67], a[196:199]\n"
"v_mfma_f32_16x16x32_bf16 a[200:203], a[192:195], v[72:75], a[200:203]\n"
"v_mfma_f32_16x16x32_bf16 a[204:207], a[192:195], v[80:83], a[204:207]\n"
"v_mfma_f32_16x16x32_bf16 a[208:211], a[192:195], v[88:91], a[208:211]\n"
"s_waitcnt vmcnt(11) lgkmcnt(0)\n"
"s_barrier\n"
"v_mfma_f32_16x16x32_bf16 v[32:35], a[96:99], a[0:3], v[96:99]\n"
"v_exp_f32 v0, v0\n"
"v_mfma_f32_16x16x32_bf16 v[36:39], a[96:99], a[16:19], v[100:103]\n"
"ds_read_b128 a[64:67], v114 offset:16384\n"
"v_lshlrev_b32 v119, 20, v124\n"
"v_mfma_f32_16x16x32_bf16 v[40:43], a[96:99], a[32:35], v[104:107]\n"
"v_exp_f32 v1, v1\n"
"v_mfma_f32_16x16x32_bf16 v[44:47], a[96:99], a[48:51], v[108:111]\n"
"ds_read_b128 a[68:71], v115 offset:16384\n"
"v_cvt_pk_bf16_f32 v64, v0, v1\n"
"v_mfma_f32_16x16x32_bf16 v[128:131], a[160:163], v[68:71], v[128:131]\n"
"v_exp_f32 v2, v2\n"
"v_mfma_f32_16x16x32_bf16 v[132:135], a[160:163], v[76:79], v[132:135]\n"
"ds_read_b128 a[72:75], v116 offset:16384\n"
"v_mov_b32 v120, s76\n"
"v_mfma_f32_16x16x32_bf16 v[136:139], a[160:163], v[84:87], v[136:139]\n"
"s_lshl_b32 s90, s93, 6\n"
"v_exp_f32 v3, v3\n"
"v_mfma_f32_16x16x32_bf16 v[140:143], a[160:163], v[92:95], v[140:143]\n"
"s_add_u32 s90, s90, 147456\n"
"ds_read_b128 a[76:79], v117 offset:16384\n"
"v_cvt_pk_bf16_f32 v65, v2, v3\n"
"v_mfma_f32_16x16x32_bf16 v[32:35], a[100:103], a[4:7], v[32:35]\n"
"v_exp_f32 v4, v4\n"
"v_mfma_f32_16x16x32_bf16 v[36:39], a[100:103], a[20:23], v[36:39]\n"
"ds_read_b128 a[80:83], v114 offset:20480\n"
"buffer_store_dword v120, v119, s[64:67], s90 offen offset:0 sc1\n"
"v_mfma_f32_16x16x32_bf16 v[40:43], a[100:103], a[36:39], v[40:43]\n"
"s_add_u32 m0, s69, 147456\n"
"v_exp_f32 v5, v5\n"
"v_mfma_f32_16x16x32_bf16 v[44:47], a[100:103], a[52:55], v[44:47]\n"
"s_mov_b32 s75, 132096\n"
"ds_read_b128 a[84:87], v115 offset:20480\n"
"v_cvt_pk_bf16_f32 v72, v4, v5\n"
"v_mfma_f32_16x16x32_bf16 v[144:147], a[164:167], v[68:71], v[144:147]\n"
"v_exp_f32 v6, v6\n"
"v_mfma_f32_16x16x32_bf16 v[148:151], a[164:167], v[76:79], v[148:151]\n"
"ds_read_b128 a[88:91], v116 offset:20480\n"
"buffer_load_dwordx4 v126, s[20:23], s75 offen offset:0 nt lds\n"
"v_mfma_f32_16x16x32_bf16 v[152:155], a[164:167], v[84:87], v[152:155]\n"
"s_mov_b32 s75, 131200\n"
"v_exp_f32 v7, v7\n"
"v_mfma_f32_16x16x32_bf16 v[156:159], a[164:167], v[92:95], v[156:159]\n"
"ds_read_b128 a[92:95], v117 offset:20480\n"
"v_cvt_pk_bf16_f32 v73, v6, v7\n"
"v_mfma_f32_16x16x32_bf16 v[32:35], a[104:107], a[8:11], v[32:35]\n"
"v_exp_f32 v8, v8\n"
"v_mfma_f32_16x16x32_bf16 v[36:39], a[104:107], a[24:27], v[36:39]\n"
"ds_read_b64_tr_b16 a[128:129], v112 offset:0\n"
"buffer_load_dwordx4 v126, s[20:23], s75 offen offset:1024 nt lds\n"
"v_mfma_f32_16x16x32_bf16 v[40:43], a[104:107], a[40:43], v[40:43]\n"
"v_exp_f32 v9, v9\n"
"v_mfma_f32_16x16x32_bf16 v[44:47], a[104:107], a[56:59], v[44:47]\n"
"s_lshl_b32 s73, s92, 2\n"
"ds_read_b64_tr_b16 a[130:131], v112 offset:256\n"
"v_cvt_pk_bf16_f32 v80, v8, v9\n"
"v_mfma_f32_16x16x32_bf16 v[160:163], a[168:171], v[68:71], v[160:163]\n"
"s_add_u32 s73, s73, s68\n"
"v_exp_f32 v10, v10\n"
"v_mfma_f32_16x16x32_bf16 v[164:167], a[168:171], v[76:79], v[164:167]\n"
"s_lshl_b32 s87, s73, 17\n"
"s_lshl_b32 s74, s84, 8\n"
"ds_read_b64_tr_b16 a[132:133], v113 offset:0\n"
"v_mfma_f32_16x16x32_bf16 v[168:171], a[168:171], v[84:87], v[168:171]\n"
"s_add_u32 s87, s87, s74\n"
"v_exp_f32 v11, v11\n"
"v_mfma_f32_16x16x32_bf16 v[172:175], a[168:171], v[92:95], v[172:175]\n"
"s_and_b32 s74, s93, 3\n"
"ds_read_b64_tr_b16 a[134:135], v113 offset:256\n"
"v_cvt_pk_bf16_f32 v81, v10, v11\n"
"v_mfma_f32_16x16x32_bf16 v[32:35], a[108:111], a[12:15], v[32:35]\n"
"s_lshl_b32 s74, s74, 6\n"
"v_exp_f32 v12, v12\n"
"v_mfma_f32_16x16x32_bf16 v[36:39], a[108:111], a[28:31], v[36:39]\n"
"s_add_u32 s87, s87, s74\n"
"s_lshl_b32 s73, s73, 8\n"
"ds_read_b64_tr_b16 a[136:137], v112 offset:512\n"
"v_mfma_f32_16x16x32_bf16 v[40:43], a[108:111], a[44:47], v[40:43]\n"
"s_add_u32 s73, s73, 4096\n"
"v_exp_f32 v13, v13\n"
"v_mfma_f32_16x16x32_bf16 v[44:47], a[108:111], a[60:63], v[44:47]\n"
"s_lshl_b32 s74, s68, 12\n"
"ds_read_b64_tr_b16 a[138:139], v112 offset:768\n"
"v_cvt_pk_bf16_f32 v88, v12, v13\n"
"v_mfma_f32_16x16x32_bf16 v[176:179], a[172:175], v[68:71], v[176:179]\n"
"s_add_u32 s74, s74, 81920\n"
"v_exp_f32 v14, v14\n"
"v_mfma_f32_16x16x32_bf16 v[180:183], a[172:175], v[76:79], v[180:183]\n"
"s_mov_b32 m0, s74\n"
"ds_read_b64_tr_b16 a[140:141], v113 offset:512\n"
"v_lshrrev_b32 v120, 6, v124\n"
"v_mfma_f32_16x16x32_bf16 v[184:187], a[172:175], v[84:87], v[184:187]\n"
"v_exp_f32 v15, v15\n"
"v_mfma_f32_16x16x32_bf16 v[188:191], a[172:175], v[92:95], v[188:191]\n"
"ds_read_b64_tr_b16 a[142:143], v113 offset:768\n"
"v_cvt_pk_bf16_f32 v89, v14, v15\n"
"v_mfma_f32_16x16x32_bf16 v[48:51], a[112:115], a[0:3], v[96:99]\n"
"v_exp_f32 v16, v16\n"
"v_mfma_f32_16x16x32_bf16 v[52:55], a[112:115], a[16:19], v[100:103]\n"
"ds_read_b64_tr_b16 a[144:145], v112 offset:1024\n"
"v_lshlrev_b32 v120, 11, v120\n"
"v_mfma_f32_16x16x32_bf16 v[56:59], a[112:115], a[32:35], v[104:107]\n"
"v_exp_f32 v17, v17\n"
"v_mfma_f32_16x16x32_bf16 v[60:63], a[112:115], a[48:51], v[108:111]\n"
"ds_read_b64_tr_b16 a[146:147], v112 offset:1280\n"
"v_cvt_pk_bf16_f32 v66, v16, v17\n"
"v_mfma_f32_16x16x32_bf16 v[192:195], a[176:179], v[68:71], v[192:195]\n"
"v_exp_f32 v18, v18\n"
"v_mfma_f32_16x16x32_bf16 v[196:199], a[176:179], v[76:79], v[196:199]\n"
"ds_read_b64_tr_b16 a[148:149], v113 offset:1024\n"
"v_and_b32 v119, 48, v124\n"
"v_mfma_f32_16x16x32_bf16 v[200:203], a[176:179], v[84:87], v[200:203]\n"
"v_exp_f32 v19, v19\n"
"v_mfma_f32_16x16x32_bf16 v[204:207], a[176:179], v[92:95], v[204:207]\n"
"ds_read_b64_tr_b16 a[150:151], v113 offset:1280\n"
"v_cvt_pk_bf16_f32 v67, v18, v19\n"
"v_mfma_f32_16x16x32_bf16 v[48:51], a[116:119], a[4:7], v[48:51]\n"
"v_exp_f32 v20, v20\n"
"v_mfma_f32_16x16x32_bf16 v[52:55], a[116:119], a[20:23], v[52:55]\n"
"ds_read_b64_tr_b16 a[152:153], v112 offset:1536\n"
"v_add_u32 v120, v120, v119\n"
"v_mfma_f32_16x16x32_bf16 v[56:59], a[116:119], a[36:39], v[56:59]\n"
"s_add_u32 s90, s87, 0\n"
"v_exp_f32 v21, v21\n"
"v_mfma_f32_16x16x32_bf16 v[60:63], a[116:119], a[52:55], v[60:63]\n"
"ds_read_b64_tr_b16 a[154:155], v112 offset:1792\n"
"v_cvt_pk_bf16_f32 v74, v20, v21\n"
"v_mfma_f32_16x16x32_bf16 v[208:211], a[180:183], v[68:71], v[208:211]\n"
"v_exp_f32 v22, v22\n"
"v_mfma_f32_16x16x32_bf16 v[212:215], a[180:183], v[76:79], v[212:215]\n"
"ds_read_b64_tr_b16 a[156:157], v113 offset:1536\n"
"buffer_load_dwordx4 v120, s[96:99], s90 offen offset:0 lds\n"
"v_mfma_f32_16x16x32_bf16 v[216:219], a[180:183], v[84:87], v[216:219]\n"
"v_exp_f32 v23, v23\n"
"v_mfma_f32_16x16x32_bf16 v[220:223], a[180:183], v[92:95], v[220:223]\n"
"s_add_u32 s90, s87, 31744\n"
"ds_read_b64_tr_b16 a[158:159], v113 offset:1792\n"
"v_cvt_pk_bf16_f32 v75, v22, v23\n"
"v_mfma_f32_16x16x32_bf16 v[48:51], a[120:123], a[8:11], v[48:51]\n"
"v_exp_f32 v24, v24\n"
"v_mfma_f32_16x16x32_bf16 v[52:55], a[120:123], a[24:27], v[52:55]\n"
"buffer_load_dwordx4 v120, s[96:99], s90 offen offset:1024 lds\n"
"v_mfma_f32_16x16x32_bf16 v[56:59], a[120:123], a[40:43], v[56:59]\n"
"v_exp_f32 v25, v25\n"
"v_mfma_f32_16x16x32_bf16 v[60:63], a[120:123], a[56:59], v[60:63]\n"
"s_add_u32 s90, s87, 63488\n"
"buffer_load_dwordx4 v120, s[96:99], s90 offen offset:2048 lds\n"
"v_cvt_pk_bf16_f32 v82, v24, v25\n"
"v_mfma_f32_16x16x32_bf16 v[224:227], a[184:187], v[68:71], v[224:227]\n"
"v_exp_f32 v26, v26\n"
"v_mfma_f32_16x16x32_bf16 v[228:231], a[184:187], v[76:79], v[228:231]\n"
"s_add_u32 s90, s87, 95232\n"
"buffer_load_dwordx4 v120, s[96:99], s90 offen offset:3072 lds\n"
"v_mfma_f32_16x16x32_bf16 v[232:235], a[184:187], v[84:87], v[232:235]\n"
"v_exp_f32 v27, v27\n"
"v_mfma_f32_16x16x32_bf16 v[236:239], a[184:187], v[92:95], v[236:239]\n"
"v_cvt_pk_bf16_f32 v83, v26, v27\n"
"v_and_b32 v119, 240, v124\n"
"v_mfma_f32_16x16x32_bf16 v[48:51], a[124:127], a[12:15], v[48:51]\n"
"v_exp_f32 v28, v28\n"
"v_mfma_f32_16x16x32_bf16 v[52:55], a[124:127], a[28:31], v[52:55]\n"
"v_lshlrev_b32 v120, 2, v119\n"
"v_lshrrev_b32 v119, 2, v119\n"
"v_mfma_f32_16x16x32_bf16 v[56:59], a[124:127], a[44:47], v[56:59]\n"
"v_exp_f32 v29, v29\n"
"v_mfma_f32_16x16x32_bf16 v[60:63], a[124:127], a[60:63], v[60:63]\n"
"v_cvt_pk_bf16_f32 v90, v28, v29\n"
"v_lshrrev_b32 v126, 8, v124\n"
"v_mfma_f32_16x16x32_bf16 v[240:243], a[188:191], v[68:71], v[240:243]\n"
"v_exp_f32 v30, v30\n"
"v_mfma_f32_16x16x32_bf16 v[244:247], a[188:191], v[76:79], v[244:247]\n"
"v_lshl_add_u32 v120, v126, 3, v120\n"
"v_add_u32 v120, s74, v120\n"
"v_mfma_f32_16x16x32_bf16 v[248:251], a[188:191], v[84:87], v[248:251]\n"
"v_exp_f32 v31, v31\n"
"v_mfma_f32_16x16x32_bf16 v[252:255], a[188:191], v[92:95], v[252:255]\n"
"buffer_load_dword a228, v119, s[60:63], s73 offen offset:0 sc1\n"
"v_cvt_pk_bf16_f32 v91, v30, v31\n"
"buffer_load_dword a229, v119, s[60:63], s73 offen offset:64 sc1\n"
"buffer_load_dword a230, v119, s[60:63], s73 offen offset:128 sc1\n"
"buffer_load_dword a231, v119, s[60:63], s73 offen offset:192 sc1\n"
"v_mfma_f32_16x16x32_bf16 a[196:199], a[192:195], v[68:71], a[196:199]\n"
"v_mfma_f32_16x16x32_bf16 a[200:203], a[192:195], v[76:79], a[200:203]\n"
"v_mfma_f32_16x16x32_bf16 a[204:207], a[192:195], v[84:87], a[204:207]\n"
"v_mfma_f32_16x16x32_bf16 a[208:211], a[192:195], v[92:95], a[208:211]\n"
"s_waitcnt lgkmcnt(0)\n"
"v_mfma_f32_16x16x32_bf16 v[0:3], a[64:67], a[0:3], v[96:99]\n"
"s_lshl_b32 s86, s84, 18\n"
"v_exp_f32 v32, v32\n"
"v_mfma_f32_16x16x32_bf16 v[4:7], a[64:67], a[16:19], v[100:103]\n"
"s_add_u32 m0, s69, 98304\n"
"ds_read_b128 a[96:99], v114 offset:24576\n"
"v_mfma_f32_16x16x32_bf16 v[8:11], a[64:67], a[32:35], v[104:107]\n"
"s_add_u32 s75, s86, 0\n"
"v_exp_f32 v33, v33\n"
"v_mfma_f32_16x16x32_bf16 v[12:15], a[64:67], a[48:51], v[108:111]\n"
"ds_read_b128 a[100:103], v115 offset:24576\n"
"v_cvt_pk_bf16_f32 v68, v32, v33\n"
"v_mfma_f32_16x16x32_bf16 v[128:131], a[128:131], v[64:67], v[128:131]\n"
"v_exp_f32 v34, v34\n"
"v_mfma_f32_16x16x32_bf16 v[132:135], a[128:131], v[72:75], v[132:135]\n"
"ds_read_b128 a[104:107], v116 offset:24576\n"
"buffer_load_dwordx4 v121, s[36:39], s75 offen offset:0 lds\n"
"v_mfma_f32_16x16x32_bf16 v[136:139], a[128:131], v[80:83], v[136:139]\n"
"v_exp_f32 v35, v35\n"
"v_mfma_f32_16x16x32_bf16 v[140:143], a[128:131], v[88:91], v[140:143]\n"
"ds_read_b128 a[108:111], v117 offset:24576\n"
"v_cvt_pk_bf16_f32 v69, v34, v35\n"
"v_mfma_f32_16x16x32_bf16 v[0:3], a[68:71], a[4:7], v[0:3]\n"
"v_exp_f32 v36, v36\n"
"v_mfma_f32_16x16x32_bf16 v[4:7], a[68:71], a[20:23], v[4:7]\n"
"ds_read_b128 a[112:115], v114 offset:28672\n"
"buffer_load_dwordx4 v118, s[36:39], s75 offen offset:1024 lds\n"
"v_mfma_f32_16x16x32_bf16 v[8:11], a[68:71], a[36:39], v[8:11]\n"
"s_add_u32 m0, s69, 106496\n"
"v_exp_f32 v37, v37\n"
"v_mfma_f32_16x16x32_bf16 v[12:15], a[68:71], a[52:55], v[12:15]\n"
"s_add_u32 s75, s86, 65536\n"
"ds_read_b128 a[116:119], v115 offset:28672\n"
"v_cvt_pk_bf16_f32 v76, v36, v37\n"
"v_mfma_f32_16x16x32_bf16 v[144:147], a[132:135], v[64:67], v[144:147]\n"
"v_exp_f32 v38, v38\n"
"v_mfma_f32_16x16x32_bf16 v[148:151], a[132:135], v[72:75], v[148:151]\n"
"ds_read_b128 a[120:123], v116 offset:28672\n"
"buffer_load_dwordx4 v121, s[36:39], s75 offen offset:0 lds\n"
"v_mfma_f32_16x16x32_bf16 v[152:155], a[132:135], v[80:83], v[152:155]\n"
"v_exp_f32 v39, v39\n"
"v_mfma_f32_16x16x32_bf16 v[156:159], a[132:135], v[88:91], v[156:159]\n"
"ds_read_b128 a[124:127], v117 offset:28672\n"
"v_cvt_pk_bf16_f32 v77, v38, v39\n"
"v_mfma_f32_16x16x32_bf16 v[0:3], a[72:75], a[8:11], v[0:3]\n"
"v_exp_f32 v40, v40\n"
"v_mfma_f32_16x16x32_bf16 v[4:7], a[72:75], a[24:27], v[4:7]\n"
"ds_read_b64_tr_b16 a[160:161], v112 offset:8192\n"
"buffer_load_dwordx4 v118, s[36:39], s75 offen offset:1024 lds\n"
"v_mfma_f32_16x16x32_bf16 v[8:11], a[72:75], a[40:43], v[8:11]\n"
"s_add_u32 m0, s69, 114688\n"
"v_exp_f32 v41, v41\n"
"v_mfma_f32_16x16x32_bf16 v[12:15], a[72:75], a[56:59], v[12:15]\n"
"s_add_u32 s75, s86, 131072\n"
"ds_read_b64_tr_b16 a[162:163], v112 offset:8448\n"
"v_cvt_pk_bf16_f32 v84, v40, v41\n"
"v_mfma_f32_16x16x32_bf16 v[160:163], a[136:139], v[64:67], v[160:163]\n"
"v_exp_f32 v42, v42\n"
"v_mfma_f32_16x16x32_bf16 v[164:167], a[136:139], v[72:75], v[164:167]\n"
"ds_read_b64_tr_b16 a[164:165], v113 offset:8192\n"
"buffer_load_dwordx4 v121, s[36:39], s75 offen offset:0 lds\n"
"v_mfma_f32_16x16x32_bf16 v[168:171], a[136:139], v[80:83], v[168:171]\n"
"v_exp_f32 v43, v43\n"
"v_mfma_f32_16x16x32_bf16 v[172:175], a[136:139], v[88:91], v[172:175]\n"
"ds_read_b64_tr_b16 a[166:167], v113 offset:8448\n"
"v_cvt_pk_bf16_f32 v85, v42, v43\n"
"v_mfma_f32_16x16x32_bf16 v[0:3], a[76:79], a[12:15], v[0:3]\n"
"v_exp_f32 v44, v44\n"
"v_mfma_f32_16x16x32_bf16 v[4:7], a[76:79], a[28:31], v[4:7]\n"
"ds_read_b64_tr_b16 a[168:169], v112 offset:8704\n"
"buffer_load_dwordx4 v118, s[36:39], s75 offen offset:1024 lds\n"
"v_mfma_f32_16x16x32_bf16 v[8:11], a[76:79], a[44:47], v[8:11]\n"
"s_add_u32 m0, s69, 122880\n"
"v_exp_f32 v45, v45\n"
"v_mfma_f32_16x16x32_bf16 v[12:15], a[76:79], a[60:63], v[12:15]\n"
"s_add_u32 s75, s86, 196608\n"
"ds_read_b64_tr_b16 a[170:171], v112 offset:8960\n"
"v_cvt_pk_bf16_f32 v92, v44, v45\n"
"v_mfma_f32_16x16x32_bf16 v[176:179], a[140:143], v[64:67], v[176:179]\n"
"v_exp_f32 v46, v46\n"
"v_mfma_f32_16x16x32_bf16 v[180:183], a[140:143], v[72:75], v[180:183]\n"
"ds_read_b64_tr_b16 a[172:173], v113 offset:8704\n"
"buffer_load_dwordx4 v121, s[36:39], s75 offen offset:0 lds\n"
"v_mfma_f32_16x16x32_bf16 v[184:187], a[140:143], v[80:83], v[184:187]\n"
"v_exp_f32 v47, v47\n"
"v_mfma_f32_16x16x32_bf16 v[188:191], a[140:143], v[88:91], v[188:191]\n"
"ds_read_b64_tr_b16 a[174:175], v113 offset:8960\n"
"v_cvt_pk_bf16_f32 v93, v46, v47\n"
"v_mfma_f32_16x16x32_bf16 v[16:19], a[80:83], a[0:3], v[96:99]\n"
"v_exp_f32 v48, v48\n"
"v_mfma_f32_16x16x32_bf16 v[20:23], a[80:83], a[16:19], v[100:103]\n"
"ds_read_b64_tr_b16 a[176:177], v112 offset:9216\n"
"buffer_load_dwordx4 v118, s[36:39], s75 offen offset:1024 lds\n"
"v_mfma_f32_16x16x32_bf16 v[24:27], a[80:83], a[32:35], v[104:107]\n"
"v_exp_f32 v49, v49\n"
"v_mfma_f32_16x16x32_bf16 v[28:31], a[80:83], a[48:51], v[108:111]\n"
"ds_read_b64_tr_b16 a[178:179], v112 offset:9472\n"
"v_cvt_pk_bf16_f32 v70, v48, v49\n"
"v_mfma_f32_16x16x32_bf16 v[192:195], a[144:147], v[64:67], v[192:195]\n"
"v_exp_f32 v50, v50\n"
"v_mfma_f32_16x16x32_bf16 v[196:199], a[144:147], v[72:75], v[196:199]\n"
"ds_read_b64_tr_b16 a[180:181], v113 offset:9216\n"
"v_mfma_f32_16x16x32_bf16 v[200:203], a[144:147], v[80:83], v[200:203]\n"
"v_exp_f32 v51, v51\n"
"v_mfma_f32_16x16x32_bf16 v[204:207], a[144:147], v[88:91], v[204:207]\n"
"ds_read_b64_tr_b16 a[182:183], v113 offset:9472\n"
"v_cvt_pk_bf16_f32 v71, v50, v51\n"
"v_mfma_f32_16x16x32_bf16 v[16:19], a[84:87], a[4:7], v[16:19]\n"
"v_exp_f32 v52, v52\n"
"v_mfma_f32_16x16x32_bf16 v[20:23], a[84:87], a[20:23], v[20:23]\n"
"ds_read_b64_tr_b16 a[184:185], v112 offset:9728\n"
"v_mfma_f32_16x16x32_bf16 v[24:27], a[84:87], a[36:39], v[24:27]\n"
"v_exp_f32 v53, v53\n"
"v_mfma_f32_16x16x32_bf16 v[28:31], a[84:87], a[52:55], v[28:31]\n"
"ds_read_b64_tr_b16 a[186:187], v112 offset:9984\n"
"v_cvt_pk_bf16_f32 v78, v52, v53\n"
"v_mfma_f32_16x16x32_bf16 v[208:211], a[148:151], v[64:67], v[208:211]\n"
"v_exp_f32 v54, v54\n"
"v_mfma_f32_16x16x32_bf16 v[212:215], a[148:151], v[72:75], v[212:215]\n"
"ds_read_b64_tr_b16 a[188:189], v113 offset:9728\n"
"v_mfma_f32_16x16x32_bf16 v[216:219], a[148:151], v[80:83], v[216:219]\n"
"v_exp_f32 v55, v55\n"
"v_mfma_f32_16x16x32_bf16 v[220:223], a[148:151], v[88:91], v[220:223]\n"
"ds_read_b64_tr_b16 a[190:191], v113 offset:9984\n"
"v_cvt_pk_bf16_f32 v79, v54, v55\n"
"v_mfma_f32_16x16x32_bf16 v[16:19], a[88:91], a[8:11], v[16:19]\n"
"v_exp_f32 v56, v56\n"
"v_mfma_f32_16x16x32_bf16 v[20:23], a[88:91], a[24:27], v[20:23]\n"
"v_mfma_f32_16x16x32_bf16 v[24:27], a[88:91], a[40:43], v[24:27]\n"
"v_exp_f32 v57, v57\n"
"v_mfma_f32_16x16x32_bf16 v[28:31], a[88:91], a[56:59], v[28:31]\n"
"v_cvt_pk_bf16_f32 v86, v56, v57\n"
"v_mfma_f32_16x16x32_bf16 v[224:227], a[152:155], v[64:67], v[224:227]\n"
"v_exp_f32 v58, v58\n"
"v_mfma_f32_16x16x32_bf16 v[228:231], a[152:155], v[72:75], v[228:231]\n"
"v_mfma_f32_16x16x32_bf16 v[232:235], a[152:155], v[80:83], v[232:235]\n"
"v_exp_f32 v59, v59\n"
"v_mfma_f32_16x16x32_bf16 v[236:239], a[152:155], v[88:91], v[236:239]\n"
"v_cvt_pk_bf16_f32 v87, v58, v59\n"
"v_mfma_f32_16x16x32_bf16 v[16:19], a[92:95], a[12:15], v[16:19]\n"
"v_exp_f32 v60, v60\n"
"v_mfma_f32_16x16x32_bf16 v[20:23], a[92:95], a[28:31], v[20:23]\n"
"v_mfma_f32_16x16x32_bf16 v[24:27], a[92:95], a[44:47], v[24:27]\n"
"v_exp_f32 v61, v61\n"
"v_mfma_f32_16x16x32_bf16 v[28:31], a[92:95], a[60:63], v[28:31]\n"
"v_cvt_pk_bf16_f32 v94, v60, v61\n"
"v_mfma_f32_16x16x32_bf16 v[240:243], a[156:159], v[64:67], v[240:243]\n"
"v_exp_f32 v62, v62\n"
"v_mfma_f32_16x16x32_bf16 v[244:247], a[156:159], v[72:75], v[244:247]\n"
"v_mfma_f32_16x16x32_bf16 v[248:251], a[156:159], v[80:83], v[248:251]\n"
"v_exp_f32 v63, v63\n"
"v_mfma_f32_16x16x32_bf16 v[252:255], a[156:159], v[88:91], v[252:255]\n"
"v_cvt_pk_bf16_f32 v95, v62, v63\n"
"v_mfma_f32_16x16x32_bf16 a[196:199], a[192:195], v[64:67], a[196:199]\n"
"v_mfma_f32_16x16x32_bf16 a[200:203], a[192:195], v[72:75], a[200:203]\n"
"v_mfma_f32_16x16x32_bf16 a[204:207], a[192:195], v[80:83], a[204:207]\n"
"v_mfma_f32_16x16x32_bf16 a[208:211], a[192:195], v[88:91], a[208:211]\n"
"s_waitcnt vmcnt(8) lgkmcnt(0)\n"
"s_barrier\n"
"v_mfma_f32_16x16x32_bf16 v[32:35], a[96:99], a[0:3], v[96:99]\n"
"v_exp_f32 v0, v0\n"
"v_mfma_f32_16x16x32_bf16 v[36:39], a[96:99], a[16:19], v[100:103]\n"
"ds_read_b64_tr_b16 a[128:129], v112 offset:16384\n"
"ds_read_b64 a[212:213], v120 offset:0\n"
"v_mfma_f32_16x16x32_bf16 v[40:43], a[96:99], a[32:35], v[104:107]\n"
"v_exp_f32 v1, v1\n"
"v_mfma_f32_16x16x32_bf16 v[44:47], a[96:99], a[48:51], v[108:111]\n"
"ds_read_b64_tr_b16 a[130:131], v112 offset:16640\n"
"v_cvt_pk_bf16_f32 v64, v0, v1\n"
"v_mfma_f32_16x16x32_bf16 v[128:131], a[160:163], v[68:71], v[128:131]\n"
"v_exp_f32 v2, v2\n"
"v_mfma_f32_16x16x32_bf16 v[132:135], a[160:163], v[76:79], v[132:135]\n"
"ds_read_b64_tr_b16 a[132:133], v113 offset:16384\n"
"ds_read_b64 a[214:215], v120 offset:1024\n"
"v_mfma_f32_16x16x32_bf16 v[136:139], a[160:163], v[84:87], v[136:139]\n"
"v_exp_f32 v3, v3\n"
"v_mfma_f32_16x16x32_bf16 v[140:143], a[160:163], v[92:95], v[140:143]\n"
"ds_read_b64_tr_b16 a[134:135], v113 offset:16640\n"
"v_cvt_pk_bf16_f32 v65, v2, v3\n"
"v_mfma_f32_16x16x32_bf16 v[32:35], a[100:103], a[4:7], v[32:35]\n"
"v_exp_f32 v4, v4\n"
"v_mfma_f32_16x16x32_bf16 v[36:39], a[100:103], a[20:23], v[36:39]\n"
"ds_read_b64_tr_b16 a[136:137], v112 offset:16896\n"
"ds_read_b64 a[216:217], v120 offset:2048\n"
"v_mfma_f32_16x16x32_bf16 v[40:43], a[100:103], a[36:39], v[40:43]\n"
"v_exp_f32 v5, v5\n"
"v_mfma_f32_16x16x32_bf16 v[44:47], a[100:103], a[52:55], v[44:47]\n"
"ds_read_b64_tr_b16 a[138:139], v112 offset:17152\n"
"v_cvt_pk_bf16_f32 v72, v4, v5\n"
"v_mfma_f32_16x16x32_bf16 v[144:147], a[164:167], v[68:71], v[144:147]\n"
"v_exp_f32 v6, v6\n"
"v_mfma_f32_16x16x32_bf16 v[148:151], a[164:167], v[76:79], v[148:151]\n"
"ds_read_b64_tr_b16 a[140:141], v113 offset:16896\n"
"ds_read_b64 a[218:219], v120 offset:3072\n"
"v_mfma_f32_16x16x32_bf16 v[152:155], a[164:167], v[84:87], v[152:155]\n"
"v_exp_f32 v7, v7\n"
"v_mfma_f32_16x16x32_bf16 v[156:159], a[164:167], v[92:95], v[156:159]\n"
"ds_read_b64_tr_b16 a[142:143], v113 offset:17152\n"
"v_cvt_pk_bf16_f32 v73, v6, v7\n"
"v_mfma_f32_16x16x32_bf16 v[32:35], a[104:107], a[8:11], v[32:35]\n"
"v_exp_f32 v8, v8\n"
"v_mfma_f32_16x16x32_bf16 v[36:39], a[104:107], a[24:27], v[36:39]\n"
"ds_read_b64_tr_b16 a[144:145], v112 offset:17408\n"
"ds_read_b64 a[220:221], v120 offset:32\n"
"v_mfma_f32_16x16x32_bf16 v[40:43], a[104:107], a[40:43], v[40:43]\n"
"v_exp_f32 v9, v9\n"
"v_mfma_f32_16x16x32_bf16 v[44:47], a[104:107], a[56:59], v[44:47]\n"
"ds_read_b64_tr_b16 a[146:147], v112 offset:17664\n"
"v_cvt_pk_bf16_f32 v80, v8, v9\n"
"v_mfma_f32_16x16x32_bf16 v[160:163], a[168:171], v[68:71], v[160:163]\n"
"v_exp_f32 v10, v10\n"
"v_mfma_f32_16x16x32_bf16 v[164:167], a[168:171], v[76:79], v[164:167]\n"
"ds_read_b64_tr_b16 a[148:149], v113 offset:17408\n"
"ds_read_b64 a[222:223], v120 offset:1056\n"
"v_mfma_f32_16x16x32_bf16 v[168:171], a[168:171], v[84:87], v[168:171]\n"
"v_exp_f32 v11, v11\n"
"v_mfma_f32_16x16x32_bf16 v[172:175], a[168:171], v[92:95], v[172:175]\n"
"ds_read_b64_tr_b16 a[150:151], v113 offset:17664\n"
"v_cvt_pk_bf16_f32 v81, v10, v11\n"
"v_mfma_f32_16x16x32_bf16 v[32:35], a[108:111], a[12:15], v[32:35]\n"
"v_exp_f32 v12, v12\n"
"v_mfma_f32_16x16x32_bf16 v[36:39], a[108:111], a[28:31], v[36:39]\n"
"ds_read_b64_tr_b16 a[152:153], v112 offset:17920\n"
"ds_read_b64 a[224:225], v120 offset:2080\n"
"v_mfma_f32_16x16x32_bf16 v[40:43], a[108:111], a[44:47], v[40:43]\n"
"v_exp_f32 v13, v13\n"
"v_mfma_f32_16x16x32_bf16 v[44:47], a[108:111], a[60:63], v[44:47]\n"
"ds_read_b64_tr_b16 a[154:155], v112 offset:18176\n"
"v_cvt_pk_bf16_f32 v88, v12, v13\n"
"v_mfma_f32_16x16x32_bf16 v[176:179], a[172:175], v[68:71], v[176:179]\n"
"v_exp_f32 v14, v14\n"
"v_mfma_f32_16x16x32_bf16 v[180:183], a[172:175], v[76:79], v[180:183]\n"
"ds_read_b64_tr_b16 a[156:157], v113 offset:17920\n"
"ds_read_b64 a[226:227], v120 offset:3104\n"
"v_mfma_f32_16x16x32_bf16 v[184:187], a[172:175], v[84:87], v[184:187]\n"
"s_cmp_le_u32 s84, 0\ns_cselect_b32 s86, 1, 0\ns_lshl_b32 s86, s86, 18\n"
"v_exp_f32 v15, v15\n"
"v_mfma_f32_16x16x32_bf16 v[188:191], a[172:175], v[92:95], v[188:191]\n"
"s_add_u32 m0, s69, 0\n"
"ds_read_b64_tr_b16 a[158:159], v113 offset:18176\n"
"v_cvt_pk_bf16_f32 v89, v14, v15\n"
"v_mfma_f32_16x16x32_bf16 v[48:51], a[112:115], a[0:3], v[96:99]\n"
"s_add_u32 s75, s86, 0\n"
"v_exp_f32 v16, v16\n"
"v_mfma_f32_16x16x32_bf16 v[52:55], a[112:115], a[16:19], v[100:103]\n"
"buffer_load_dwordx4 v121, s[36:39], s75 offen offset:0 lds\n"
"v_mfma_f32_16x16x32_bf16 v[56:59], a[112:115], a[32:35], v[104:107]\n"
"v_exp_f32 v17, v17\n"
"v_mfma_f32_16x16x32_bf16 v[60:63], a[112:115], a[48:51], v[108:111]\n"
"buffer_load_dwordx4 v118, s[36:39], s75 offen offset:1024 lds\n"
"v_cvt_pk_bf16_f32 v66, v16, v17\n"
"v_mfma_f32_16x16x32_bf16 v[192:195], a[176:179], v[68:71], v[192:195]\n"
"s_add_u32 m0, s69, 8192\n"
"v_exp_f32 v18, v18\n"
"v_mfma_f32_16x16x32_bf16 v[196:199], a[176:179], v[76:79], v[196:199]\n"
"s_add_u32 s75, s86, 65536\n"
"buffer_load_dwordx4 v121, s[36:39], s75 offen offset:0 lds\n"
"v_mfma_f32_16x16x32_bf16 v[200:203], a[176:179], v[84:87], v[200:203]\n"
"v_exp_f32 v19, v19\n"
"v_mfma_f32_16x16x32_bf16 v[204:207], a[176:179], v[92:95], v[204:207]\n"
"buffer_load_dwordx4 v118, s[36:39], s75 offen offset:1024 lds\n"
"v_cvt_pk_bf16_f32 v67, v18, v19\n"
"v_mfma_f32_16x16x32_bf16 v[48:51], a[116:119], a[4:7], v[48:51]\n"
"s_add_u32 m0, s69, 16384\n"
"v_exp_f32 v20, v20\n"
"v_mfma_f32_16x16x32_bf16 v[52:55], a[116:119], a[20:23], v[52:55]\n"
"s_add_u32 s75, s86, 131072\n"
"buffer_load_dwordx4 v121, s[36:39], s75 offen offset:0 lds\n"
"v_mfma_f32_16x16x32_bf16 v[56:59], a[116:119], a[36:39], v[56:59]\n"
"v_exp_f32 v21, v21\n"
"v_mfma_f32_16x16x32_bf16 v[60:63], a[116:119], a[52:55], v[60:63]\n"
"buffer_load_dwordx4 v118, s[36:39], s75 offen offset:1024 lds\n"
"v_cvt_pk_bf16_f32 v74, v20, v21\n"
"v_mfma_f32_16x16x32_bf16 v[208:211], a[180:183], v[68:71], v[208:211]\n"
"s_add_u32 m0, s69, 24576\n"
"v_exp_f32 v22, v22\n"
"v_mfma_f32_16x16x32_bf16 v[212:215], a[180:183], v[76:79], v[212:215]\n"
"s_add_u32 s75, s86, 196608\n"
"buffer_load_dwordx4 v121, s[36:39], s75 offen offset:0 lds\n"
"v_mfma_f32_16x16x32_bf16 v[216:219], a[180:183], v[84:87], v[216:219]\n"
"v_exp_f32 v23, v23\n"
"v_mfma_f32_16x16x32_bf16 v[220:223], a[180:183], v[92:95], v[220:223]\n"
"buffer_load_dwordx4 v118, s[36:39], s75 offen offset:1024 lds\n"
"v_cvt_pk_bf16_f32 v75, v22, v23\n"
"v_mfma_f32_16x16x32_bf16 v[48:51], a[120:123], a[8:11], v[48:51]\n"
"v_exp_f32 v24, v24\n"
"v_mfma_f32_16x16x32_bf16 v[52:55], a[120:123], a[24:27], v[52:55]\n"
"s_cmp_le_u32 s84, 1\ns_cselect_b32 s86, 2, 1\ns_lshl_b32 s86, s86, 18\n"
"s_add_u32 m0, s69, 32768\n"
"v_mfma_f32_16x16x32_bf16 v[56:59], a[120:123], a[40:43], v[56:59]\n"
"s_add_u32 s75, s86, 0\n"
"v_exp_f32 v25, v25\n"
"v_mfma_f32_16x16x32_bf16 v[60:63], a[120:123], a[56:59], v[60:63]\n"
"buffer_load_dwordx4 v121, s[36:39], s75 offen offset:0 lds\n"
"v_cvt_pk_bf16_f32 v82, v24, v25\n"
"v_mfma_f32_16x16x32_bf16 v[224:227], a[184:187], v[68:71], v[224:227]\n"
"v_exp_f32 v26, v26\n"
"v_mfma_f32_16x16x32_bf16 v[228:231], a[184:187], v[76:79], v[228:231]\n"
"buffer_load_dwordx4 v118, s[36:39], s75 offen offset:1024 lds\n"
"v_mfma_f32_16x16x32_bf16 v[232:235], a[184:187], v[84:87], v[232:235]\n"
"s_add_u32 m0, s69, 40960\n"
"v_exp_f32 v27, v27\n"
"v_mfma_f32_16x16x32_bf16 v[236:239], a[184:187], v[92:95], v[236:239]\n"
"s_add_u32 s75, s86, 65536\n"
"buffer_load_dwordx4 v121, s[36:39], s75 offen offset:0 lds\n"
"v_cvt_pk_bf16_f32 v83, v26, v27\n"
"v_mfma_f32_16x16x32_bf16 v[48:51], a[124:127], a[12:15], v[48:51]\n"
"v_exp_f32 v28, v28\n"
"v_mfma_f32_16x16x32_bf16 v[52:55], a[124:127], a[28:31], v[52:55]\n"
"buffer_load_dwordx4 v118, s[36:39], s75 offen offset:1024 lds\n"
"v_mfma_f32_16x16x32_bf16 v[56:59], a[124:127], a[44:47], v[56:59]\n"
"s_add_u32 m0, s69, 49152\n"
"v_exp_f32 v29, v29\n"
"v_mfma_f32_16x16x32_bf16 v[60:63], a[124:127], a[60:63], v[60:63]\n"
"s_add_u32 s75, s86, 131072\n"
"buffer_load_dwordx4 v121, s[36:39], s75 offen offset:0 lds\n"
"v_cvt_pk_bf16_f32 v90, v28, v29\n"
"v_mfma_f32_16x16x32_bf16 v[240:243], a[188:191], v[68:71], v[240:243]\n"
"v_exp_f32 v30, v30\n"
"v_mfma_f32_16x16x32_bf16 v[244:247], a[188:191], v[76:79], v[244:247]\n"
"buffer_load_dwordx4 v118, s[36:39], s75 offen offset:1024 lds\n"
"v_mfma_f32_16x16x32_bf16 v[248:251], a[188:191], v[84:87], v[248:251]\n"
"s_add_u32 m0, s69, 57344\n"
"v_exp_f32 v31, v31\n"
"v_mfma_f32_16x16x32_bf16 v[252:255], a[188:191], v[92:95], v[252:255]\n"
"s_add_u32 s75, s86, 196608\n"
"buffer_load_dwordx4 v121, s[36:39], s75 offen offset:0 lds\n"
"v_cvt_pk_bf16_f32 v91, v30, v31\n"
"buffer_load_dwordx4 v118, s[36:39], s75 offen offset:1024 lds\n"
"v_mfma_f32_16x16x32_bf16 a[196:199], a[192:195], v[68:71], a[196:199]\n"
"v_mfma_f32_16x16x32_bf16 a[200:203], a[192:195], v[76:79], a[200:203]\n"
"v_mfma_f32_16x16x32_bf16 a[204:207], a[192:195], v[84:87], a[204:207]\n"
"v_mfma_f32_16x16x32_bf16 a[208:211], a[192:195], v[92:95], a[208:211]\n"
"s_waitcnt lgkmcnt(0)\n"
"v_mfma_f32_16x16x32_bf16 v[128:131], a[128:131], v[64:67], v[128:131]\n"
"v_and_b32 v125, 112, v124\n"
"v_lshlrev_b32 v125, 2, v125\n"
"v_mfma_f32_16x16x32_bf16 v[132:135], a[128:131], v[72:75], v[132:135]\n"
"s_and_b32 s90, s93, 0xf8\n"
"s_lshl_b32 s90, s90, 6\n"
"s_add_u32 s90, s90, 147456\n"
"v_mfma_f32_16x16x32_bf16 v[136:139], a[128:131], v[80:83], v[136:139]\n"
"ds_read_b64_tr_b16 a[160:161], v112 offset:24576\n"
"ds_read_b64_tr_b16 a[162:163], v112 offset:24832\n"
"v_mfma_f32_16x16x32_bf16 v[140:143], a[128:131], v[88:91], v[140:143]\n"
"buffer_load_dword v126, v125, s[60:63], s90 offen offset:0 sc1\n"
"v_mfma_f32_16x16x32_bf16 v[144:147], a[132:135], v[64:67], v[144:147]\n"
"v_exp_f32 v32, v32\n"
"v_mfma_f32_16x16x32_bf16 v[148:151], a[132:135], v[72:75], v[148:151]\n"
"ds_read_b64_tr_b16 a[164:165], v113 offset:24576\n"
"ds_read_b64_tr_b16 a[166:167], v113 offset:24832\n"
"v_mfma_f32_16x16x32_bf16 v[152:155], a[132:135], v[80:83], v[152:155]\n"
"v_exp_f32 v33, v33\n"
"v_mfma_f32_16x16x32_bf16 v[156:159], a[132:135], v[88:91], v[156:159]\n"
"v_exp_f32 v34, v34\n"
"v_mfma_f32_16x16x32_bf16 v[160:163], a[136:139], v[64:67], v[160:163]\n"
"ds_read_b64_tr_b16 a[168:169], v112 offset:25088\n"
"ds_read_b64_tr_b16 a[170:171], v112 offset:25344\n"
"v_mfma_f32_16x16x32_bf16 v[164:167], a[136:139], v[72:75], v[164:167]\n"
"v_exp_f32 v35, v35\n"
"v_mfma_f32_16x16x32_bf16 v[168:171], a[136:139], v[80:83], v[168:171]\n"
"v_cvt_pk_bf16_f32 v68, v32, v33\n"
"v_cvt_pk_bf16_f32 v69, v34, v35\n"
"v_mfma_f32_16x16x32_bf16 v[172:175], a[136:139], v[88:91], v[172:175]\n"
"ds_read_b64_tr_b16 a[172:173], v113 offset:25088\n"
"ds_read_b64_tr_b16 a[174:175], v113 offset:25344\n"
"v_mfma_f32_16x16x32_bf16 v[176:179], a[140:143], v[64:67], v[176:179]\n"
"v_exp_f32 v48, v48\n"
"v_mfma_f32_16x16x32_bf16 v[180:183], a[140:143], v[72:75], v[180:183]\n"
"v_exp_f32 v49, v49\n"
"v_mfma_f32_16x16x32_bf16 v[184:187], a[140:143], v[80:83], v[184:187]\n"
"ds_read_b64_tr_b16 a[176:177], v112 offset:25600\n"
"ds_read_b64_tr_b16 a[178:179], v112 offset:25856\n"
"v_mfma_f32_16x16x32_bf16 v[188:191], a[140:143], v[88:91], v[188:191]\n"
"v_exp_f32 v50, v50\n"
"v_mfma_f32_16x16x32_bf16 v[192:195], a[144:147], v[64:67], v[192:195]\n"
"v_exp_f32 v51, v51\n"
"v_mfma_f32_16x16x32_bf16 v[196:199], a[144:147], v[72:75], v[196:199]\n"
"ds_read_b64_tr_b16 a[180:181], v113 offset:25600\n"
"ds_read_b64_tr_b16 a[182:183], v113 offset:25856\n"
"v_mfma_f32_16x16x32_bf16 v[200:203], a[144:147], v[80:83], v[200:203]\n"
"v_cvt_pk_bf16_f32 v70, v48, v49\n"
"v_cvt_pk_bf16_f32 v71, v50, v51\n"
"v_mfma_f32_16x16x32_bf16 v[204:207], a[144:147], v[88:91], v[204:207]\n"
"v_exp_f32 v36, v36\n"
"v_mfma_f32_16x16x32_bf16 v[208:211], a[148:151], v[64:67], v[208:211]\n"
"ds_read_b64_tr_b16 a[184:185], v112 offset:26112\n"
"ds_read_b64_tr_b16 a[186:187], v112 offset:26368\n"
"v_mfma_f32_16x16x32_bf16 v[212:215], a[148:151], v[72:75], v[212:215]\n"
"v_exp_f32 v37, v37\n"
"v_mfma_f32_16x16x32_bf16 v[216:219], a[148:151], v[80:83], v[216:219]\n"
"v_exp_f32 v38, v38\n"
"v_mfma_f32_16x16x32_bf16 v[220:223], a[148:151], v[88:91], v[220:223]\n"
"ds_read_b64_tr_b16 a[188:189], v113 offset:26112\n"
"ds_read_b64_tr_b16 a[190:191], v113 offset:26368\n"
"v_mfma_f32_16x16x32_bf16 v[224:227], a[152:155], v[64:67], v[224:227]\n"
"v_exp_f32 v39, v39\n"
"v_mfma_f32_16x16x32_bf16 v[228:231], a[152:155], v[72:75], v[228:231]\n"
"v_cvt_pk_bf16_f32 v76, v36, v37\n"
"v_cvt_pk_bf16_f32 v77, v38, v39\n"
"v_mfma_f32_16x16x32_bf16 v[232:235], a[152:155], v[80:83], v[232:235]\n"
"v_exp_f32 v52, v52\n"
"v_mfma_f32_16x16x32_bf16 v[236:239], a[152:155], v[88:91], v[236:239]\n"
"v_exp_f32 v53, v53\n"
"v_mfma_f32_16x16x32_bf16 v[240:243], a[156:159], v[64:67], v[240:243]\n"
"v_exp_f32 v54, v54\n"
"v_mfma_f32_16x16x32_bf16 v[244:247], a[156:159], v[72:75], v[244:247]\n"
"v_exp_f32 v55, v55\n"
"v_mfma_f32_16x16x32_bf16 v[248:251], a[156:159], v[80:83], v[248:251]\n"
"v_cvt_pk_bf16_f32 v78, v52, v53\n"
"v_cvt_pk_bf16_f32 v79, v54, v55\n"
"v_mfma_f32_16x16x32_bf16 v[252:255], a[156:159], v[88:91], v[252:255]\n"
"v_exp_f32 v40, v40\n"
"v_mfma_f32_16x16x32_bf16 a[196:199], a[192:195], v[64:67], a[196:199]\n"
"v_exp_f32 v41, v41\n"
"v_mfma_f32_16x16x32_bf16 a[200:203], a[192:195], v[72:75], a[200:203]\n"
"v_exp_f32 v42, v42\n"
"v_mfma_f32_16x16x32_bf16 a[204:207], a[192:195], v[80:83], a[204:207]\n"
"v_exp_f32 v43, v43\n"
"v_mfma_f32_16x16x32_bf16 a[208:211], a[192:195], v[88:91], a[208:211]\n"
"v_cvt_pk_bf16_f32 v84, v40, v41\n"
"v_cvt_pk_bf16_f32 v85, v42, v43\n"
"v_exp_f32 v56, v56\n"
"v_exp_f32 v57, v57\n"
"v_exp_f32 v58, v58\n"
"v_exp_f32 v59, v59\n"
"v_cvt_pk_bf16_f32 v86, v56, v57\n"
"v_cvt_pk_bf16_f32 v87, v58, v59\n"
"s_waitcnt vmcnt(0) lgkmcnt(0)\n"
"s_barrier\n"
"v_cmp_ne_u32 vcc, s76, v126\n"
"s_nop 4\n"
"s_cbranch_vccnz LSPIN2_%=\n"
"LBACK3_%=:\n"
"v_mfma_f32_16x16x32_bf16 a[196:199], a[192:195], v[68:71], a[196:199]\n"
"v_exp_f32 v44, v44\n"
"v_mfma_f32_16x16x32_bf16 v[128:131], a[160:163], v[68:71], v[128:131]\n"
"v_exp_f32 v45, v45\n"
"v_mfma_f32_16x16x32_bf16 v[144:147], a[164:167], v[68:71], v[144:147]\n"
"v_exp_f32 v46, v46\n"
"v_mfma_f32_16x16x32_bf16 v[160:163], a[168:171], v[68:71], v[160:163]\n"
"v_exp_f32 v47, v47\n"
"v_mfma_f32_16x16x32_bf16 v[176:179], a[172:175], v[68:71], v[176:179]\n"
"v_cvt_pk_bf16_f32 v92, v44, v45\n"
"v_cvt_pk_bf16_f32 v93, v46, v47\n"
"v_mfma_f32_16x16x32_bf16 v[192:195], a[176:179], v[68:71], v[192:195]\n"
"v_exp_f32 v60, v60\n"
"v_mfma_f32_16x16x32_bf16 v[208:211], a[180:183], v[68:71], v[208:211]\n"
"v_exp_f32 v61, v61\n"
"v_mfma_f32_16x16x32_bf16 v[224:227], a[184:187], v[68:71], v[224:227]\n"
"v_exp_f32 v62, v62\n"
"v_mfma_f32_16x16x32_bf16 v[240:243], a[188:191], v[68:71], v[240:243]\n"
"v_exp_f32 v63, v63\n"
"v_mfma_f32_16x16x32_bf16 a[200:203], a[192:195], v[76:79], a[200:203]\n"
"v_cvt_pk_bf16_f32 v94, v60, v61\n"
"v_cvt_pk_bf16_f32 v95, v62, v63\n"
"v_mfma_f32_16x16x32_bf16 v[132:135], a[160:163], v[76:79], v[132:135]\n"
"v_lshrrev_b32 v125, 7, v124\n"
"v_mul_u32_u24 v125, 57344, v125\n"
"v_mfma_f32_16x16x32_bf16 v[148:151], a[164:167], v[76:79], v[148:151]\n"
"v_and_b32 v126, 112, v124\n"
"v_add_u32 v125, v125, v126\n"
"v_mfma_f32_16x16x32_bf16 v[164:167], a[168:171], v[76:79], v[164:167]\n"
"s_lshr_b32 s73, s93, 3\n"
"s_lshl_b32 s73, s73, 2\n"
"s_add_u32 s73, s73, s68\n"
"v_mfma_f32_16x16x32_bf16 v[180:183], a[172:175], v[76:79], v[180:183]\n"
"s_lshl_b32 s73, s73, 7\n"
"s_lshl_b32 s74, s68, 12\n"
"s_add_u32 m0, s74, 81920\n"
"v_mfma_f32_16x16x32_bf16 v[196:199], a[176:179], v[76:79], v[196:199]\n"
"s_add_u32 s75, s73, 163840\n"
"buffer_load_dwordx4 v125, s[60:63], s75 offen offset:0 sc1 lds\n"
"v_mfma_f32_16x16x32_bf16 v[212:215], a[180:183], v[76:79], v[212:215]\n"
"s_add_u32 s75, s73, 177152\n"
"buffer_load_dwordx4 v125, s[60:63], s75 offen offset:1024 sc1 lds\n"
"v_mfma_f32_16x16x32_bf16 v[228:231], a[184:187], v[76:79], v[228:231]\n"
"s_add_u32 s75, s73, 190464\n"
"buffer_load_dwordx4 v125, s[60:63], s75 offen offset:2048 sc1 lds\n"
"v_mfma_f32_16x16x32_bf16 v[244:247], a[188:191], v[76:79], v[244:247]\n"
"s_add_u32 s75, s73, 203776\n"
"buffer_load_dwordx4 v125, s[60:63], s75 offen offset:3072 sc1 lds\n"
"v_mfma_f32_16x16x32_bf16 a[204:207], a[192:195], v[84:87], a[204:207]\n"
"v_accvgpr_read_b32 v4, a196\n"
"v_mfma_f32_16x16x32_bf16 v[136:139], a[160:163], v[84:87], v[136:139]\n"
"v_rcp_f32 v0, v4\n"
"v_mfma_f32_16x16x32_bf16 v[152:155], a[164:167], v[84:87], v[152:155]\n"
"v_mul_f32 v128, v128, v0\n"
"v_mul_f32 v129, v129, v0\n"
"v_mfma_f32_16x16x32_bf16 v[168:171], a[168:171], v[84:87], v[168:171]\n"
"v_mul_f32 v130, v130, v0\n"
"v_mul_f32 v131, v131, v0\n"
"v_mfma_f32_16x16x32_bf16 v[184:187], a[172:175], v[84:87], v[184:187]\n"
"v_cvt_pk_bf16_f32 v128, v128, v129\n"
"v_cvt_pk_bf16_f32 v129, v130, v131\n"
"v_mfma_f32_16x16x32_bf16 v[200:203], a[176:179], v[84:87], v[200:203]\n"
"v_mul_f32 v144, v144, v0\n"
"v_mul_f32 v145, v145, v0\n"
"v_mfma_f32_16x16x32_bf16 v[216:219], a[180:183], v[84:87], v[216:219]\n"
"v_mul_f32 v146, v146, v0\n"
"v_mul_f32 v147, v147, v0\n"
"v_mfma_f32_16x16x32_bf16 v[232:235], a[184:187], v[84:87], v[232:235]\n"
"v_cvt_pk_bf16_f32 v130, v144, v145\n"
"v_cvt_pk_bf16_f32 v131, v146, v147\n"
"v_mfma_f32_16x16x32_bf16 v[248:251], a[188:191], v[84:87], v[248:251]\n"
"buffer_store_dwordx4 v[128:131], v124, s[56:59], 0 offen offset:0 sc1\n"
"v_mul_f32 v160, v160, v0\n"
"v_mfma_f32_16x16x32_bf16 a[208:211], a[192:195], v[92:95], a[208:211]\n"
"v_mul_f32 v161, v161, v0\n"
"v_mul_f32 v162, v162, v0\n"
"v_mfma_f32_16x16x32_bf16 v[140:143], a[160:163], v[92:95], v[140:143]\n"
"v_mul_f32 v163, v163, v0\n"
"v_cvt_pk_bf16_f32 v160, v160, v161\n"
"v_mfma_f32_16x16x32_bf16 v[156:159], a[164:167], v[92:95], v[156:159]\n"
"v_cvt_pk_bf16_f32 v161, v162, v163\n"
"v_mul_f32 v176, v176, v0\n"
"v_mfma_f32_16x16x32_bf16 v[172:175], a[168:171], v[92:95], v[172:175]\n"
"v_mul_f32 v177, v177, v0\n"
"v_mul_f32 v178, v178, v0\n"
"v_mfma_f32_16x16x32_bf16 v[188:191], a[172:175], v[92:95], v[188:191]\n"
"v_mul_f32 v179, v179, v0\n"
"v_cvt_pk_bf16_f32 v162, v176, v177\n"
"v_mfma_f32_16x16x32_bf16 v[204:207], a[176:179], v[92:95], v[204:207]\n"
"v_cvt_pk_bf16_f32 v163, v178, v179\n"
"v_mfma_f32_16x16x32_bf16 v[220:223], a[180:183], v[92:95], v[220:223]\n"
"buffer_store_dwordx4 v[160:163], v124, s[56:59], 0 offen offset:1024 sc1\n"
"v_mul_f32 v192, v192, v0\n"
"v_mfma_f32_16x16x32_bf16 v[236:239], a[184:187], v[92:95], v[236:239]\n"
"v_mul_f32 v193, v193, v0\n"
"v_mul_f32 v194, v194, v0\n"
"v_mfma_f32_16x16x32_bf16 v[252:255], a[188:191], v[92:95], v[252:255]\n"
"v_mul_f32 v195, v195, v0\n"
"v_cvt_pk_bf16_f32 v192, v192, v193\n"
"v_cvt_pk_bf16_f32 v193, v194, v195\n"
"v_mul_f32 v208, v208, v0\n"
"v_mul_f32 v209, v209, v0\n"
"v_mul_f32 v210, v210, v0\n"
"v_mul_f32 v211, v211, v0\n"
"v_cvt_pk_bf16_f32 v194, v208, v209\n"
"v_cvt_pk_bf16_f32 v195, v210, v211\n"
"buffer_store_dwordx4 v[192:195], v124, s[56:59], 0 offen offset:2048 sc1\n"
"v_mul_f32 v224, v224, v0\n"
"v_mul_f32 v225, v225, v0\n"
"v_mul_f32 v226, v226, v0\n"
"v_mul_f32 v227, v227, v0\n"
"v_cvt_pk_bf16_f32 v224, v224, v225\n"
"v_cvt_pk_bf16_f32 v225, v226, v227\n"
"v_mul_f32 v240, v240, v0\n"
"v_mul_f32 v241, v241, v0\n"
"v_mul_f32 v242, v242, v0\n"
"v_mul_f32 v243, v243, v0\n"
"v_cvt_pk_bf16_f32 v226, v240, v241\n"
"v_cvt_pk_bf16_f32 v227, v242, v243\n"
"buffer_store_dwordx4 v[224:227], v124, s[56:59], 0 offen offset:3072 sc1\n"
"v_accvgpr_read_b32 v5, a200\n"
"v_rcp_f32 v1, v5\n"
"s_nop 0\n"
"v_mul_f32 v132, v132, v1\n"
"v_mul_f32 v133, v133, v1\n"
"v_mul_f32 v134, v134, v1\n"
"v_mul_f32 v135, v135, v1\n"
"v_cvt_pk_bf16_f32 v132, v132, v133\n"
"v_cvt_pk_bf16_f32 v133, v134, v135\n"
"v_mul_f32 v148, v148, v1\n"
"v_mul_f32 v149, v149, v1\n"
"v_mul_f32 v150, v150, v1\n"
"v_mul_f32 v151, v151, v1\n"
"v_cvt_pk_bf16_f32 v134, v148, v149\n"
"v_cvt_pk_bf16_f32 v135, v150, v151\n"
"buffer_store_dwordx4 v[132:135], v124, s[56:59], s78 offen offset:0 sc1\n"
"v_mul_f32 v164, v164, v1\n"
"v_mul_f32 v165, v165, v1\n"
"v_mul_f32 v166, v166, v1\n"
"v_mul_f32 v167, v167, v1\n"
"v_cvt_pk_bf16_f32 v164, v164, v165\n"
"v_cvt_pk_bf16_f32 v165, v166, v167\n"
"v_mul_f32 v180, v180, v1\n"
"v_mul_f32 v181, v181, v1\n"
"v_mul_f32 v182, v182, v1\n"
"v_mul_f32 v183, v183, v1\n"
"v_cvt_pk_bf16_f32 v166, v180, v181\n"
"v_cvt_pk_bf16_f32 v167, v182, v183\n"
"buffer_store_dwordx4 v[164:167], v124, s[56:59], s78 offen offset:1024 sc1\n"
"v_mul_f32 v196, v196, v1\n"
"v_mul_f32 v197, v197, v1\n"
"v_mul_f32 v198, v198, v1\n"
"v_mul_f32 v199, v199, v1\n"
"v_cvt_pk_bf16_f32 v196, v196, v197\n"
"v_cvt_pk_bf16_f32 v197, v198, v199\n"
"v_mul_f32 v212, v212, v1\n"
"v_mul_f32 v213, v213, v1\n"
"v_mul_f32 v214, v214, v1\n"
"v_mul_f32 v215, v215, v1\n"
"v_cvt_pk_bf16_f32 v198, v212, v213\n"
"v_cvt_pk_bf16_f32 v199, v214, v215\n"
"buffer_store_dwordx4 v[196:199], v124, s[56:59], s78 offen offset:2048 sc1\n"
"v_mul_f32 v228, v228, v1\n"
"v_mul_f32 v229, v229, v1\n"
"v_mul_f32 v230, v230, v1\n"
"v_mul_f32 v231, v231, v1\n"
"v_cvt_pk_bf16_f32 v228, v228, v229\n"
"v_cvt_pk_bf16_f32 v229, v230, v231\n"
"v_mul_f32 v244, v244, v1\n"
"v_mul_f32 v245, v245, v1\n"
"v_mul_f32 v246, v246, v1\n"
"v_mul_f32 v247, v247, v1\n"
"v_cvt_pk_bf16_f32 v230, v244, v245\n"
"v_cvt_pk_bf16_f32 v231, v246, v247\n"
"buffer_store_dwordx4 v[228:231], v124, s[56:59], s78 offen offset:3072 sc1\n"
"v_accvgpr_read_b32 v6, a204\n"
"v_rcp_f32 v2, v6\n"
"s_nop 0\n"
"v_mul_f32 v136, v136, v2\n"
"v_mul_f32 v137, v137, v2\n"
"v_mul_f32 v138, v138, v2\n"
"v_mul_f32 v139, v139, v2\n"
"v_cvt_pk_bf16_f32 v136, v136, v137\n"
"v_cvt_pk_bf16_f32 v137, v138, v139\n"
"v_mul_f32 v152, v152, v2\n"
"v_mul_f32 v153, v153, v2\n"
"v_mul_f32 v154, v154, v2\n"
"v_mul_f32 v155, v155, v2\n"
"v_cvt_pk_bf16_f32 v138, v152, v153\n"
"v_cvt_pk_bf16_f32 v139, v154, v155\n"
"buffer_store_dwordx4 v[136:139], v124, s[56:59], s79 offen offset:0 sc1\n"
"v_mul_f32 v168, v168, v2\n"
"v_mul_f32 v169, v169, v2\n"
"v_mul_f32 v170, v170, v2\n"
"v_mul_f32 v171, v171, v2\n"
"v_cvt_pk_bf16_f32 v168, v168, v169\n"
"v_cvt_pk_bf16_f32 v169, v170, v171\n"
"v_mul_f32 v184, v184, v2\n"
"v_mul_f32 v185, v185, v2\n"
"v_mul_f32 v186, v186, v2\n"
"v_mul_f32 v187, v187, v2\n"
"v_cvt_pk_bf16_f32 v170, v184, v185\n"
"v_cvt_pk_bf16_f32 v171, v186, v187\n"
"buffer_store_dwordx4 v[168:171], v124, s[56:59], s79 offen offset:1024 sc1\n"
"v_mul_f32 v200, v200, v2\n"
"v_mul_f32 v201, v201, v2\n"
"v_mul_f32 v202, v202, v2\n"
"v_mul_f32 v203, v203, v2\n"
"v_cvt_pk_bf16_f32 v200, v200, v201\n"
"v_cvt_pk_bf16_f32 v201, v202, v203\n"
"v_mul_f32 v216, v216, v2\n"
"v_mul_f32 v217, v217, v2\n"
"v_mul_f32 v218, v218, v2\n"
"v_mul_f32 v219, v219, v2\n"
"v_cvt_pk_bf16_f32 v202, v216, v217\n"
"v_cvt_pk_bf16_f32 v203, v218, v219\n"
"buffer_store_dwordx4 v[200:203], v124, s[56:59], s79 offen offset:2048 sc1\n"
"v_mul_f32 v232, v232, v2\n"
"v_mul_f32 v233, v233, v2\n"
"v_mul_f32 v234, v234, v2\n"
"v_mul_f32 v235, v235, v2\n"
"v_cvt_pk_bf16_f32 v232, v232, v233\n"
"v_cvt_pk_bf16_f32 v233, v234, v235\n"
"v_mul_f32 v248, v248, v2\n"
"v_mul_f32 v249, v249, v2\n"
"v_mul_f32 v250, v250, v2\n"
"v_mul_f32 v251, v251, v2\n"
"v_cvt_pk_bf16_f32 v234, v248, v249\n"
"v_cvt_pk_bf16_f32 v235, v250, v251\n"
"buffer_store_dwordx4 v[232:235], v124, s[56:59], s79 offen offset:3072 sc1\n"
"v_accvgpr_read_b32 v7, a208\n"
"v_rcp_f32 v3, v7\n"
"s_nop 0\n"
"v_mul_f32 v140, v140, v3\n"
"v_mul_f32 v141, v141, v3\n"
"v_mul_f32 v142, v142, v3\n"
"v_mul_f32 v143, v143, v3\n"
"v_cvt_pk_bf16_f32 v140, v140, v141\n"
"v_cvt_pk_bf16_f32 v141, v142, v143\n"
"v_mul_f32 v156, v156, v3\n"
"v_mul_f32 v157, v157, v3\n"
"v_mul_f32 v158, v158, v3\n"
"v_mul_f32 v159, v159, v3\n"
"v_cvt_pk_bf16_f32 v142, v156, v157\n"
"v_cvt_pk_bf16_f32 v143, v158, v159\n"
"buffer_store_dwordx4 v[140:143], v124, s[56:59], s80 offen offset:0 sc1\n"
"v_mul_f32 v172, v172, v3\n"
"v_mul_f32 v173, v173, v3\n"
"v_mul_f32 v174, v174, v3\n"
"v_mul_f32 v175, v175, v3\n"
"v_cvt_pk_bf16_f32 v172, v172, v173\n"
"v_cvt_pk_bf16_f32 v173, v174, v175\n"
"v_mul_f32 v188, v188, v3\n"
"v_mul_f32 v189, v189, v3\n"
"v_mul_f32 v190, v190, v3\n"
"v_mul_f32 v191, v191, v3\n"
"v_cvt_pk_bf16_f32 v174, v188, v189\n"
"v_cvt_pk_bf16_f32 v175, v190, v191\n"
"buffer_store_dwordx4 v[172:175], v124, s[56:59], s80 offen offset:1024 sc1\n"
"v_mul_f32 v204, v204, v3\n"
"v_mul_f32 v205, v205, v3\n"
"v_mul_f32 v206, v206, v3\n"
"v_mul_f32 v207, v207, v3\n"
"v_cvt_pk_bf16_f32 v204, v204, v205\n"
"v_cvt_pk_bf16_f32 v205, v206, v207\n"
"v_mul_f32 v220, v220, v3\n"
"v_mul_f32 v221, v221, v3\n"
"v_mul_f32 v222, v222, v3\n"
"v_mul_f32 v223, v223, v3\n"
"v_cvt_pk_bf16_f32 v206, v220, v221\n"
"v_cvt_pk_bf16_f32 v207, v222, v223\n"
"buffer_store_dwordx4 v[204:207], v124, s[56:59], s80 offen offset:2048 sc1\n"
"v_mul_f32 v236, v236, v3\n"
"v_mul_f32 v237, v237, v3\n"
"v_mul_f32 v238, v238, v3\n"
"v_mul_f32 v239, v239, v3\n"
"v_cvt_pk_bf16_f32 v236, v236, v237\n"
"v_cvt_pk_bf16_f32 v237, v238, v239\n"
"v_mul_f32 v252, v252, v3\n"
"v_mul_f32 v253, v253, v3\n"
"v_mul_f32 v254, v254, v3\n"
"v_mul_f32 v255, v255, v3\n"
"v_cvt_pk_bf16_f32 v238, v252, v253\n"
"v_cvt_pk_bf16_f32 v239, v254, v255\n"
"buffer_store_dwordx4 v[236:239], v124, s[56:59], s80 offen offset:3072 sc1\n"
"v_add_f32 v8, v4, v5\n"
"v_add_f32 v9, v6, v7\n"
"v_add_f32 v8, v8, v9\n"
"v_mov_b32 v9, 0x5f800000\n"
"v_cmp_nle_f32 s[82:83], v8, v9\n"
"s_nop 3\n"
"s_mov_b64 %[bad], s[82:83]\n"
"s_mov_b32 %[key], s94\n"
"s_mov_b32 %[tag], s76\n"
"s_mov_b32 %[rsel], s85\n"
"s_cmp_lg_u64 s[82:83], 0\n"
"s_cbranch_scc1 LBAD_%=\n"
"LBACK2_%=:\n"
"s_branch LEND_%=\n"
"LSPIN1_%=:\n"
"s_mov_b32 s77, 0\n"
"LSPIN1L_%=:\n"
"s_sleep 2\n"
"v_lshlrev_b32 v109, 5, v108\n"
"buffer_load_dwordx4 a[176:179], v109, s[60:63], 0 offen offset:0 sc1\n"
"buffer_load_dwordx4 a[180:183], v109, s[60:63], 0 offen offset:16 sc1\n"
"s_waitcnt vmcnt(0)\n"
"v_accvgpr_read_b32 v99, a177\n"
"v_accvgpr_read_b32 v103, a179\n"
"v_accvgpr_read_b32 v107, a181\n"
"v_accvgpr_read_b32 v111, a183\n"
"v_xor_b32 v99, s76, v99\n"
"v_xor_b32 v103, s76, v103\n"
"v_xor_b32 v107, s76, v107\n"
"v_xor_b32 v111, s76, v111\n"
"v_or3_b32 v99, v99, v103, v107\n"
"v_or_b32 v99, v99, v111\n"
"v_accvgpr_read_b32 v110, a176\n"
"v_accvgpr_read_b32 v103, a178\n"
"v_accvgpr_read_b32 v107, a180\n"
"v_accvgpr_read_b32 v111, a182\n"
"v_max3_u32 v110, v110, v103, v107\n"
"v_max_u32 v110, v110, v111\n"
"v_cmp_ne_u32 vcc, 0, v99\n"
"s_add_u32 s77, s77, 1\n"
"s_cmp_gt_u32 s77, 1048576\n"
"s_cbranch_scc1 LBACK1_%=\n"
"s_nop 4\n"
"s_cbranch_vccnz LSPIN1L_%=\n"
"s_branch LBACK1_%=\n"
"LSPIN2_%=:\n"
"s_mov_b32 s77, 0\n"
"LSPIN2L_%=:\n"
"s_sleep 2\n"
"s_and_b32 s90, s93, 0xf8\n"
"s_lshl_b32 s90, s90, 6\n"
"s_add_u32 s90, s90, 147456\n"
"buffer_load_dword v126, v125, s[60:63], s90 offen offset:0 sc1\n"
"s_waitcnt vmcnt(0)\n"
"v_cmp_ne_u32 vcc, s76, v126\n"
"s_add_u32 s77, s77, 1\n"
"s_cmp_gt_u32 s77, 1048576\n"
"s_cbranch_scc1 LBACK3_%=\n"
"s_nop 4\n"
"s_cbranch_vccnz LSPIN2L_%=\n"
"s_branch LBACK3_%=\n"
"LBAD_%=:\n"
"s_waitcnt vmcnt(0)\n"
"s_mov_b32 s58, 524288\n"
"buffer_store_dwordx4 a[0:3], v124, s[56:59], 0 offen offset:0\n"
"buffer_store_dwordx4 a[4:7], v124, s[56:59], 0 offen offset:1024\n"
"buffer_store_dwordx4 a[8:11], v124, s[56:59], 0 offen offset:2048\n"
"buffer_store_dwordx4 a[12:15], v124, s[56:59], 0 offen offset:3072\n"
"buffer_store_dwordx4 a[16:19], v124, s[56:59], s78 offen offset:0\n"
"buffer_store_dwordx4 a[20:23], v124, s[56:59], s78 offen offset:1024\n"
"buffer_store_dwordx4 a[24:27], v124, s[56:59], s78 offen offset:2048\n"
"buffer_store_dwordx4 a[28:31], v124, s[56:59], s78 offen offset:3072\n"
"buffer_store_dwordx4 a[32:35], v124, s[56:59], s79 offen offset:0\n"
"buffer_store_dwordx4 a[36:39], v124, s[56:59], s79 offen offset:1024\n"
"buffer_store_dwordx4 a[40:43], v124, s[56:59], s79 offen offset:2048\n"
"buffer_store_dwordx4 a[44:47], v124, s[56:59], s79 offen offset:3072\n"
"buffer_store_dwordx4 a[48:51], v124, s[56:59], s80 offen offset:0\n"
"buffer_store_dwordx4 a[52:55], v124, s[56:59], s80 offen offset:1024\n"
"buffer_store_dwordx4 a[56:59], v124, s[56:59], s80 offen offset:2048\n"
"buffer_store_dwordx4 a[60:63], v124, s[56:59], s80 offen offset:3072\n"
"s_waitcnt vmcnt(0)\n"
"s_branch LBACK2_%=\n"
"LEND_%=:\n"
    : [bad] "=&s"(bad), [key] "=&s"(key), [tag] "=&s"(tag), [rsel] "=&s"(rsel), [ka] "+s"(ka), [b] "+s"(b), [wid] "+s"(wid),
      "={v[128:131]}"(op[0]), "={v[160:163]}"(op[1]), "={v[192:195]}"(op[2]), "={v[224:227]}"(op[3]), "={v[132:135]}"(op[4]), "={v[164:167]}"(op[5]), "={v[196:199]}"(op[6]), "={v[228:231]}"(op[7]), "={v[136:139]}"(op[8]), "={v[168:171]}"(op[9]), "={v[200:203]}"(op[10]), "={v[232:235]}"(op[11]), "={v[140:143]}"(op[12]), "={v[172:175]}"(op[13]), "={v[204:207]}"(op[14]), "={v[236:239]}"(op[15]), "={a[212:215]}"(op[16]), "={a[216:219]}"(op[17]), "={a[220:223]}"(op[18]), "={a[224:227]}"(op[19]), "={a[228:231]}"(op[20])
    :
    : "v0", "v1", "v2", "v3", "v4", "v5", "v6", "v7", "v8", "v9", "v10", "v11", "v12", "v13", "v14", "v15", "v16", "v17", "v18", "v19", "v20", "v21", "v22", "v23", "v24", "v25", "v26", "v27", "v28", "v29", "v30", "v31", "v32", "v33", "v34", "v35", "v36", "v37", "v38", "v39", "v40", "v41", "v42", "v43", "v44", "v45", "v46", "v47", "v48", "v49", "v50", "v51", "v52", "v53", "v54", "v55", "v56", "v57", "v58", "v59", "v60", "v61", "v62", "v63", "v64", "v65", "v66", "v67", "v68", "v69", "v70", "v71", "v72", "v73", "v74", "v75", "v76", "v77", "v78", "v79", "v80", "v81", "v82", "v83", "v84", "v85", "v86", "v87", "v88", "v89", "v90", "v91", "v92", "v93", "v94", "v95", "v96", "v97", "v98", "v99", "v100", "v101", "v102", "v103", "v104", "v105", "v106", "v107", "v108", "v109", "v110", "v111", "v112", "v113", "v114", "v115", "v116", "v117", "v118", "v119", "v120", "v121", "v122", "v123", "v124", "v125", "v126", "v127", "v144", "v145", "v146", "v147", "v148", "v149", "v150", "v151", "v152", "v153", "v154", "v155", "v156", "v157", "v158", "v159", "v176", "v177", "v178", "v179", "v180", "v181", "v182", "v183", "v184", "v185", "v186", "v187", "v188", "v189", "v190", "v191", "v208", "v209", "v210", "v211", "v212", "v213", "v214", "v215", "v216", "v217", "v218", "v219", "v220", "v221", "v222", "v223", "v240", "v241", "v242", "v243", "v244", "v245", "v246", "v247", "v248", "v249", "v250", "v251", "v252", "v253", "v254", "v255", "a0", "a1", "a2", "a3", "a4", "a5", "a6", "a7", "a8", "a9", "a10", "a11", "a12", "a13", "a14", "a15", "a16", "a17", "a18", "a19", "a20", "a21", "a22", "a23", "a24", "a25", "a26", "a27", "a28", "a29", "a30", "a31", "a32", "a33", "a34", "a35", "a36", "a37", "a38", "a39", "a40", "a41", "a42", "a43", "a44", "a45", "a46", "a47", "a48", "a49", "a50", "a51", "a52", "a53", "a54", "a55", "a56", "a57", "a58", "a59", "a60", "a61", "a62", "a63", "a64", "a65", "a66", "a67", "a68", "a69", "a70", "a71", "a72", "a73", "a74", "a75", "a76", "a77", "a78", "a79", "a80", "a81", "a82", "a83", "a84", "a85", "a86", "a87", "a88", "a89", "a90", "a91", "a92", "a93", "a94", "a95", "a96", "a97", "a98", "a99", "a100", "a101", "a102", "a103", "a104", "a105", "a106", "a107", "a108", "a109", "a110", "a111", "a112", "a113", "a114", "a115", "a116", "a117", "a118", "a119", "a120", "a121", "a122", "a123", "a124", "a125", "a126", "a127", "a128", "a129", "a130", "a131", "a132", "a133", "a134", "a135", "a136", "a137", "a138", "a139", "a140", "a141", "a142", "a143", "a144", "a145", "a146", "a147", "a148", "a149", "a150", "a151", "a152", "a153", "a154", "a155", "a156", "a157", "a158", "a159", "a160", "a161", "a162", "a163", "a164", "a165", "a166", "a167", "a168", "a169", "a170", "a171", "a172", "a173", "a174", "a175", "a176", "a177", "a178", "a179", "a180", "a181", "a182", "a183", "a184", "a185", "a186", "a187", "a188", "a189", "a190", "a191", "a192", "a193", "a194", "a195", "a196", "a197", "a198", "a199", "a200", "a201", "a202", "a203", "a204", "a205", "a206", "a207", "a208", "a209", "a210", "a211", "a232", "a233", "a234", "a235", "a236", "a237", "a238", "a239", "a240", "a241", "a242", "a243", "a244", "a245", "a246", "a247", "a248", "a249", "a250", "a251", "a252", "a253", "a254", "a255", "s20", "s21", "s22", "s23", "s24", "s25", "s26", "s27", "s28", "s29", "s30", "s31", "s36", "s37", "s38", "s39", "s40", "s41", "s42", "s43", "s44", "s45", "s46", "s47", "s48", "s49", "s50", "s51", "s52", "s53", "s54", "s55", "s56", "s57", "s58", "s59", "s60", "s61", "s62", "s63", "s64", "s65", "s66", "s67", "s68", "s69", "s70", "s71", "s72", "s73", "s74", "s75", "s76", "s77", "s78", "s79", "s80", "s81", "s82", "s83", "s84", "s85", "s86", "s87", "s88", "s89", "s90", "s91", "s92", "s93", "s94", "s95", "s96", "s97", "s98", "s99", "vcc", "scc", "memory");
  int cls = ((b >> 3) & 7) < 4 ? 0 : ((b >> 3) & 7);
  if (__builtin_expect(bad != 0, 0)) {
    typedef __attribute__((address_space(4))) const u64 c_u64;
    typedef __attribute__((address_space(1))) char g_char;
    const u16* kc = (const u16*)(g_char*)((c_u64*)ka)[1]; const u16* vc = (const u16*)(g_char*)((c_u64*)ka)[2]; char* ws = (char*)(g_char*)((c_u64*)ka)[10];
    int lane; asm volatile("v_mbcnt_lo_u32_b32 %0, -1, 0\n\tv_mbcnt_hi_u32_b32 %0, -1, %0" : "=v"(lane));
    const int h = (b & 3) * 8 + ((b >> 3) & 7), qt = 2 * (b >> 6) + ((b >> 2) & 1);
    slow_attention(ws + WS_OF + (size_t)(qt * 4 + wid) * 524288 + h * 4096, kc + ((h >> 3) * 2048 + 1536) * 128, vc + ((h >> 3) * 2048 + 1536) * 128, lane);
    cls = 0;
    asm volatile("s_waitcnt vmcnt(0)" ::: "memory");
    const __amdgpu_buffer_rsrc_t ro = mk_rsrc(ws + WS_OF + (size_t)(qt * 4 + wid) * 524288 + h * 4096, 4 * 131072);
#pragma unroll
    for (int k = 0; k < 16; ++k) op[k] = __builtin_amdgcn_raw_buffer_load_b128(ro, lane * 16 + (k & 3) * 1024, (k >> 2) * 131072, 16);
    asm volatile("" : "+v"(op[0]), "+v"(op[1]), "+v"(op[2]), "+v"(op[3]), "+v"(op[4]), "+v"(op[5]), "+v"(op[6]), "+v"(op[7]),
                      "+v"(op[8]), "+v"(op[9]), "+v"(op[10]), "+v"(op[11]), "+v"(op[12]), "+v"(op[13]), "+v"(op[14]), "+v"(op[15]));
  }
  asm volatile(
"s_load_dwordx2 s[20:21], %[ka], 80\n"
"s_load_dwordx2 s[52:53], %[ka], 0\n"
"s_load_dwordx2 s[36:37], %[ka], 32\n"
"s_load_dwordx2 s[56:57], %[ka], 72\n"
"s_mov_b32 s78, %[b]\n"
"s_mov_b32 s79, %[wid]\n"
"s_mov_b32 s77, %[tag]\n"
"v_mbcnt_lo_u32_b32 v158, -1, 0\n"
"v_mbcnt_hi_u32_b32 v158, -1, v158\n"
"v_lshlrev_b32 v152, 4, v158\n"
"v_lshrrev_b32 v244, 4, v158\n"
"v_bfe_u32 v245, v158, 2, 2\n"
"v_lshlrev_b32 v245, 6, v245\n"
"v_lshl_add_u32 v245, v244, 11, v245\n"
"v_and_b32 v246, 3, v158\n"
"v_lshl_add_u32 v245, v246, 3, v245\n"
"v_and_b32 v246, 1, v244\n"
"v_lshl_add_u32 v144, v246, 5, v245\n"
"v_xor_b32 v145, 32, v144\n"
"v_add_u32 v146, 65536, v144\n"
"v_add_u32 v147, 65536, v145\n"
"s_and_b32 s82, s78, 3\n"
"s_bfe_u32 s83, s78, 0x30003\n"
"s_lshr_b32 s84, s78, 6\n"
"s_lshl_b32 s84, s84, 3\n"
"s_and_b32 s72, s78, 4\n"
"s_add_u32 s84, s84, s72\n"
"s_add_u32 s84, s84, s79\n"
"s_waitcnt vmcnt(16) lgkmcnt(0)\n"
"s_barrier\n"
"ds_read_b64_tr_b16 a[0:1], v146 offset:32768\n"
"ds_read_b64_tr_b16 a[2:3], v146 offset:33024\n"
"ds_read_b64_tr_b16 a[4:5], v147 offset:32768\n"
"ds_read_b64_tr_b16 a[6:7], v147 offset:33024\n"
"ds_read_b64_tr_b16 a[8:9], v146 offset:33280\n"
"ds_read_b64_tr_b16 a[10:11], v146 offset:33536\n"
"ds_read_b64_tr_b16 a[12:13], v147 offset:33280\n"
"ds_read_b64_tr_b16 a[14:15], v147 offset:33536\n"
"ds_read_b64_tr_b16 a[16:17], v146 offset:33792\n"
"ds_read_b64_tr_b16 a[18:19], v146 offset:34048\n"
"ds_read_b64_tr_b16 a[20:21], v147 offset:33792\n"
"ds_read_b64_tr_b16 a[22:23], v147 offset:34048\n"
"ds_read_b64_tr_b16 a[24:25], v146 offset:34304\n"
"ds_read_b64_tr_b16 a[26:27], v146 offset:34560\n"
"ds_read_b64_tr_b16 a[28:29], v147 offset:34304\n"
"ds_read_b64_tr_b16 a[30:31], v147 offset:34560\n"
"s_waitcnt lgkmcnt(0)\n"
"v_mfma_f32_16x16x32_bf16 v[0:3], a[0:3], v[128:131], 0\n"
"s_lshl_b32 s71, s84, 19\n"
"s_lshl_b32 s72, s82, 15\n"
"ds_read_b64_tr_b16 a[32:33], v146 offset:40960\n"
"v_mfma_f32_16x16x32_bf16 v[4:7], a[0:3], v[132:135], 0\n"
"s_add_u32 s71, s71, s72\n"
"s_add_u32 s71, s71, 8388608\n"
"ds_read_b64_tr_b16 a[34:35], v146 offset:41216\n"
"v_mfma_f32_16x16x32_bf16 v[8:11], a[0:3], v[136:139], 0\n"
"s_add_u32 s40, s20, s71\ns_addc_u32 s41, s21, 0\n"
"s_mov_b32 s42, 524288\n"
"ds_read_b64_tr_b16 a[36:37], v147 offset:40960\n"
"v_mfma_f32_16x16x32_bf16 v[12:15], a[0:3], v[140:143], 0\n"
"s_mov_b32 s43, 0x00020000\n"
"s_lshl_b32 s72, s83, 12\n"
"ds_read_b64_tr_b16 a[38:39], v147 offset:41216\n"
"v_mfma_f32_16x16x32_bf16 v[28:31], a[4:7], v[140:143], 0\n"
"s_add_u32 s71, s71, s72\n"
"s_add_u32 s44, s20, s71\ns_addc_u32 s45, s21, 0\n"
"ds_read_b64_tr_b16 a[40:41], v146 offset:41472\n"
"v_mfma_f32_16x16x32_bf16 v[24:27], a[4:7], v[136:139], 0\n"
"s_mov_b32 s46, 524288\n"
"s_mov_b32 s47, 0x00020000\n"
"ds_read_b64_tr_b16 a[42:43], v146 offset:41728\n"
"v_mfma_f32_16x16x32_bf16 v[20:23], a[4:7], v[132:135], 0\n"
"s_mov_b32 s97, 131072\n"
"s_mov_b32 s98, 262144\n"
"ds_read_b64_tr_b16 a[44:45], v147 offset:41472\n"
"v_mfma_f32_16x16x32_bf16 v[16:19], a[4:7], v[128:131], 0\n"
"s_mov_b32 s99, 393216\n"
"s_add_u32 s48, s20, 34603008\ns_addc_u32 s49, s21, 0\n"
"ds_read_b64_tr_b16 a[46:47], v147 offset:41728\n"
"v_mfma_f32_16x16x32_bf16 v[32:35], a[8:11], v[128:131], 0\n"
"s_mov_b32 s50, 835584\n"
"s_mov_b32 s51, 0x00020000\n"
"ds_read_b64_tr_b16 a[48:49], v146 offset:41984\n"
"v_mfma_f32_16x16x32_bf16 v[36:39], a[8:11], v[132:135], 0\n"
"s_lshl_b32 s75, s84, 2\n"
"s_add_u32 s75, s75, s82\n"
"ds_read_b64_tr_b16 a[50:51], v146 offset:42240\n"
"v_mfma_f32_16x16x32_bf16 v[40:43], a[8:11], v[136:139], 0\n"
"s_lshl_b32 s75, s75, 9\n"
"s_add_u32 s75, s75, 622592\n"
"ds_read_b64_tr_b16 a[52:53], v147 offset:41984\n"
"v_mfma_f32_16x16x32_bf16 v[44:47], a[8:11], v[140:143], 0\n"
"s_lshl_b32 s80, s83, 6\n"
"s_add_u32 s80, s80, s75\n"
"ds_read_b64_tr_b16 a[54:55], v147 offset:42240\n"
"v_mfma_f32_16x16x32_bf16 v[60:63], a[12:15], v[140:143], 0\n"
"s_lshl_b32 s92, s79, 12\n"
"ds_read_b64_tr_b16 a[56:57], v146 offset:42496\n"
"v_mov_b32 v153, s77\n"
"v_mfma_f32_16x16x32_bf16 v[56:59], a[12:15], v[136:139], 0\n"
"s_add_u32 s92, s92, 81920\n"
"ds_read_b64_tr_b16 a[58:59], v146 offset:42752\n"
"v_lshlrev_b32 v154, 24, v158\n"
"v_mfma_f32_16x16x32_bf16 v[52:55], a[12:15], v[132:135], 0\n"
"ds_read_b64_tr_b16 a[60:61], v147 offset:42496\n"
"v_and_b32 v155, 7, v158\n"
"v_mfma_f32_16x16x32_bf16 v[48:51], a[12:15], v[128:131], 0\n"
"ds_read_b64_tr_b16 a[62:63], v147 offset:42752\n"
"v_lshlrev_b32 v155, 6, v155\n"
"v_mfma_f32_16x16x32_bf16 v[64:67], a[16:19], v[128:131], 0\n"
"s_waitcnt vmcnt(0)\n"
"s_cmp_eq_u32 %[cls], 0\ns_cselect_b32 s91, s80, 1073741824\n"
"buffer_store_dword v153, v154, s[48:51], s91 offen offset:0 sc1\n"
"v_mfma_f32_16x16x32_bf16 v[68:71], a[16:19], v[132:135], 0\n"
"v_add_u32 v244, s92, v152\n"
"v_mfma_f32_16x16x32_bf16 v[72:75], a[16:19], v[136:139], 0\n"
"s_lshl_b32 s71, s82, 21\n"
"ds_read_b128 v[208:211], v244 offset:0\n"
"ds_read_b128 v[212:215], v244 offset:1024\n"
"v_mfma_f32_16x16x32_bf16 v[76:79], a[16:19], v[140:143], 0\n"
"s_add_u32 s36, s36, s71\ns_addc_u32 s37, s37, 0\n"
"ds_read_b128 v[216:219], v244 offset:2048\n"
"ds_read_b128 v[220:223], v244 offset:3072\n"
"v_mfma_f32_16x16x32_bf16 v[92:95], a[20:23], v[140:143], 0\n"
"s_lshl_b32 s92, s79, 9\n"
"s_add_u32 s92, s92, 131072\n"
"v_add_u32 v245, s92, v144\n"
"v_mfma_f32_16x16x32_bf16 v[88:91], a[20:23], v[136:139], 0\n"
"s_sub_u32 s36, s36, 1024\ns_subb_u32 s37, s37, 0\n"
"s_mov_b32 s38, 2098176\n"
"v_add_u32 v246, s92, v145\n"
"v_mfma_f32_16x16x32_bf16 v[84:87], a[20:23], v[132:135], 0\n"
"s_mov_b32 s39, 0x00020000\n"
"ds_read_b64_tr_b16 a[64:65], v245 offset:0\n"
"ds_read_b64_tr_b16 a[66:67], v245 offset:256\n"
"v_mfma_f32_16x16x32_bf16 v[80:83], a[20:23], v[128:131], 0\n"
"ds_read_b64_tr_b16 a[68:69], v245 offset:8192\n"
"ds_read_b64_tr_b16 a[70:71], v245 offset:8448\n"
"v_mfma_f32_16x16x32_bf16 v[96:99], a[24:27], v[128:131], 0\n"
"ds_read_b64_tr_b16 a[72:73], v245 offset:16384\n"
"ds_read_b64_tr_b16 a[74:75], v245 offset:16640\n"
"v_mfma_f32_16x16x32_bf16 v[100:103], a[24:27], v[132:135], 0\n"
"ds_read_b64_tr_b16 a[76:77], v245 offset:24576\n"
"ds_read_b64_tr_b16 a[78:79], v245 offset:24832\n"
"v_mfma_f32_16x16x32_bf16 v[104:107], a[24:27], v[136:139], 0\n"
"ds_read_b64_tr_b16 a[80:81], v246 offset:0\n"
"ds_read_b64_tr_b16 a[82:83], v246 offset:256\n"
"v_mfma_f32_16x16x32_bf16 v[108:111], a[24:27], v[140:143], 0\n"
"ds_read_b64_tr_b16 a[84:85], v246 offset:8192\n"
"ds_read_b64_tr_b16 a[86:87], v246 offset:8448\n"
"v_mfma_f32_16x16x32_bf16 v[124:127], a[28:31], v[140:143], 0\n"
"ds_read_b64_tr_b16 a[88:89], v246 offset:16384\n"
"ds_read_b64_tr_b16 a[90:91], v246 offset:16640\n"
"v_mfma_f32_16x16x32_bf16 v[120:123], a[28:31], v[136:139], 0\n"
"ds_read_b64_tr_b16 a[92:93], v246 offset:24576\n"
"ds_read_b64_tr_b16 a[94:95], v246 offset:24832\n"
"v_mfma_f32_16x16x32_bf16 v[116:119], a[28:31], v[132:135], 0\n"
"s_and_b32 s86, %[rsel], 0xffff0000\n"
"s_lshl_b32 s87, %[rsel], 16\n"
"v_rcp_f32 v247, s87\n"
"s_nop 0\n"
"v_readfirstlane_b32 s87, v247\n"
"v_mfma_f32_16x16x32_bf16 v[112:115], a[28:31], v[128:131], 0\n"
"v_bfe_u32 v250, v158, 2, 2\n"
"v_bfe_u32 v251, v158, 4, 1\n"
"s_waitcnt lgkmcnt(0)\n"
"v_mfma_f32_16x16x32_bf16 v[0:3], a[32:35], v[160:163], v[0:3]\n"
"ds_read_b64_tr_b16 a[0:1], v146 offset:49152\n"
"v_lshl_add_u32 v250, v251, 4, v250\n"
"v_mfma_f32_16x16x32_bf16 v[4:7], a[32:35], v[164:167], v[4:7]\n"
"s_lshl_b32 s71, s79, 2\n"
"ds_read_b64_tr_b16 a[2:3], v146 offset:49408\n"
"v_add_u32 v250, s71, v250\n"
"v_mfma_f32_16x16x32_bf16 v[8:11], a[32:35], v[168:171], v[8:11]\n"
"ds_read_b64_tr_b16 a[4:5], v147 offset:49152\n"
"v_lshlrev_b32 v250, 11, v250\n"
"v_mfma_f32_16x16x32_bf16 v[12:15], a[32:35], v[172:175], v[12:15]\n"
"ds_read_b64_tr_b16 a[6:7], v147 offset:49408\n"
"v_lshrrev_b32 v251, 5, v158\n"
"v_mfma_f32_16x16x32_bf16 v[28:31], a[36:39], v[172:175], v[28:31]\n"
"ds_read_b64_tr_b16 a[8:9], v146 offset:49664\n"
"v_lshl_add_u32 v250, v251, 6, v250\n"
"v_mfma_f32_16x16x32_bf16 v[24:27], a[36:39], v[168:171], v[24:27]\n"
"s_and_b32 s71, s79, 1\n"
"s_lshl_b32 s71, s71, 1\n"
"ds_read_b64_tr_b16 a[10:11], v146 offset:49920\n"
"v_mfma_f32_16x16x32_bf16 v[20:23], a[36:39], v[164:167], v[20:23]\n"
"ds_read_b64_tr_b16 a[12:13], v147 offset:49664\n"
"v_and_b32 v251, 3, v158\n"
"v_mfma_f32_16x16x32_bf16 v[16:19], a[36:39], v[160:163], v[16:19]\n"
"ds_read_b64_tr_b16 a[14:15], v147 offset:49920\n"
"v_xor_b32 v251, s71, v251\n"
"v_mfma_f32_16x16x32_bf16 v[32:35], a[40:43], v[160:163], v[32:35]\n"
"ds_read_b64_tr_b16 a[16:17], v146 offset:50176\n"
"v_lshl_add_u32 v250, v251, 4, v250\n"
"v_mfma_f32_16x16x32_bf16 v[36:39], a[40:43], v[164:167], v[36:39]\n"
"s_lshl_b32 s71, s82, 6\n"
"ds_read_b64_tr_b16 a[18:19], v146 offset:50432\n"
"v_xor_b32 v250, s71, v250\n"
"v_mfma_f32_16x16x32_bf16 v[40:43], a[40:43], v[168:171], v[40:43]\n"
"s_lshl_b32 s71, s83, 8\n"
"ds_read_b64_tr_b16 a[20:21], v147 offset:50176\n"
"v_add_u32 v250, s71, v250\n"
"v_mfma_f32_16x16x32_bf16 v[44:47], a[40:43], v[172:175], v[44:47]\n"
"ds_read_b64_tr_b16 a[22:23], v147 offset:50432\n"
"v_xor_b32 v151, 128, v250\n"
"v_mfma_f32_16x16x32_bf16 v[60:63], a[44:47], v[172:175], v[60:63]\n"
"ds_read_b64_tr_b16 a[24:25], v146 offset:50688\n"
"v_add_u32 v150, 1024, v250\n"
"v_mfma_f32_16x16x32_bf16 v[56:59], a[44:47], v[168:171], v[56:59]\n"
"s_lshl_b32 s68, s79, 11\n"
"ds_read_b64_tr_b16 a[26:27], v146 offset:50944\n"
"v_add_u32 v148, 131072, v144\n"
"v_mfma_f32_16x16x32_bf16 v[52:55], a[44:47], v[164:167], v[52:55]\n"
"ds_read_b64_tr_b16 a[28:29], v147 offset:50688\n"
"v_add_u32 v149, 131072, v145\n"
"v_mfma_f32_16x16x32_bf16 v[48:51], a[44:47], v[160:163], v[48:51]\n"
"ds_read_b64_tr_b16 a[30:31], v147 offset:50944\n"
"v_fma_f32 v208, -v212, s86, v208\n"
"v_mfma_f32_16x16x32_bf16 v[64:67], a[48:51], v[160:163], v[64:67]\n"
"v_fma_f32 v209, -v213, s86, v209\n"
"v_fma_f32 v210, -v214, s86, v210\n"
"v_mfma_f32_16x16x32_bf16 v[68:71], a[48:51], v[164:167], v[68:71]\n"
"v_fma_f32 v211, -v215, s86, v211\n"
"v_fma_f32 v216, -v220, s86, v216\n"
"v_mfma_f32_16x16x32_bf16 v[72:75], a[48:51], v[168:171], v[72:75]\n"
"v_fma_f32 v217, -v221, s86, v217\n"
"v_fma_f32 v218, -v222, s86, v218\n"
"v_mfma_f32_16x16x32_bf16 v[76:79], a[48:51], v[172:175], v[76:79]\n"
"v_fma_f32 v219, -v223, s86, v219\n"
"v_add_f32_dpp v208, v208, v208 row_ror:8 row_mask:0xf bank_mask:0xf\n"
"v_mfma_f32_16x16x32_bf16 v[92:95], a[52:55], v[172:175], v[92:95]\n"
"v_add_f32_dpp v209, v209, v209 row_ror:8 row_mask:0xf bank_mask:0xf\n"
"v_add_f32_dpp v210, v210, v210 row_ror:8 row_mask:0xf bank_mask:0xf\n"
"v_mfma_f32_16x16x32_bf16 v[88:91], a[52:55], v[168:171], v[88:91]\n"
"v_add_f32_dpp v211, v211, v211 row_ror:8 row_mask:0xf bank_mask:0xf\n"
"v_add_f32_dpp v216, v216, v216 row_ror:8 row_mask:0xf bank_mask:0xf\n"
"v_mfma_f32_16x16x32_bf16 v[84:87], a[52:55], v[164:167], v[84:87]\n"
"v_add_f32_dpp v217, v217, v217 row_ror:8 row_mask:0xf bank_mask:0xf\n"
"v_add_f32_dpp v218, v218, v218 row_ror:8 row_mask:0xf bank_mask:0xf\n"
"v_mfma_f32_16x16x32_bf16 v[80:83], a[52:55], v[160:163], v[80:83]\n"
"v_add_f32_dpp v219, v219, v219 row_ror:8 row_mask:0xf bank_mask:0xf\n"
"v_mfma_f32_16x16x32_bf16 v[96:99], a[56:59], v[160:163], v[96:99]\n"
"v_permlane32_swap_b32 v208, v216\n"
"v_mfma_f32_16x16x32_bf16 v[100:103], a[56:59], v[164:167], v[100:103]\n"
"v_permlane32_swap_b32 v209, v217\n"
"v_mfma_f32_16x16x32_bf16 v[104:107], a[56:59], v[168:171], v[104:107]\n"
"v_permlane32_swap_b32 v210, v218\n"
"v_mfma_f32_16x16x32_bf16 v[108:111], a[56:59], v[172:175], v[108:111]\n"
"v_permlane32_swap_b32 v211, v219\n"
"v_mfma_f32_16x16x32_bf16 v[124:127], a[60:63], v[172:175], v[124:127]\n"
"buffer_load_dword v156, v155, s[48:51], s75 offen offset:0 sc1\n"
"v_add_f32 v208, v208, v216\n"
"v_mfma_f32_16x16x32_bf16 v[120:123], a[60:63], v[168:171], v[120:123]\n"
"v_add_f32 v209, v209, v217\n"
"v_add_f32 v210, v210, v218\n"
"v_mfma_f32_16x16x32_bf16 v[116:119], a[60:63], v[164:167], v[116:119]\n"
"v_add_f32 v211, v211, v219\n"
"v_mov_b32 v212, v208\n"
"v_mfma_f32_16x16x32_bf16 v[112:115], a[60:63], v[160:163], v[112:115]\n"
"v_mov_b32 v213, v209\n"
"v_mov_b32 v214, v210\n"
"s_waitcnt lgkmcnt(0)\n"
"v_mfma_f32_16x16x32_bf16 v[0:3], a[0:3], v[192:195], v[0:3]\n"
"ds_read_b64_tr_b16 a[32:33], v146 offset:57344\n"
"v_mov_b32 v215, v211\n"
"v_mfma_f32_16x16x32_bf16 v[4:7], a[0:3], v[196:199], v[4:7]\n"
"ds_read_b64_tr_b16 a[34:35], v146 offset:57600\n"
"v_permlane16_swap_b32 v208, v212\n"
"v_mfma_f32_16x16x32_bf16 v[8:11], a[0:3], v[200:203], v[8:11]\n"
"ds_read_b64_tr_b16 a[36:37], v147 offset:57344\n"
"v_permlane16_swap_b32 v209, v213\n"
"v_mfma_f32_16x16x32_bf16 v[12:15], a[0:3], v[204:207], v[12:15]\n"
"ds_read_b64_tr_b16 a[38:39], v147 offset:57600\n"
"v_permlane16_swap_b32 v210, v214\n"
"v_mfma_f32_16x16x32_bf16 v[28:31], a[4:7], v[204:207], v[28:31]\n"
"ds_read_b64_tr_b16 a[40:41], v146 offset:57856\n"
"v_permlane16_swap_b32 v211, v215\n"
"v_mfma_f32_16x16x32_bf16 v[24:27], a[4:7], v[200:203], v[24:27]\n"
"ds_read_b64_tr_b16 a[42:43], v146 offset:58112\n"
"v_add_f32 v208, v208, v212\n"
"v_mfma_f32_16x16x32_bf16 v[20:23], a[4:7], v[196:199], v[20:23]\n"
"ds_read_b64_tr_b16 a[44:45], v147 offset:57856\n"
"v_add_f32 v209, v209, v213\n"
"v_mfma_f32_16x16x32_bf16 v[16:19], a[4:7], v[192:195], v[16:19]\n"
"ds_read_b64_tr_b16 a[46:47], v147 offset:58112\n"
"v_add_f32 v210, v210, v214\n"
"v_mfma_f32_16x16x32_bf16 v[32:35], a[8:11], v[192:195], v[32:35]\n"
"ds_read_b64_tr_b16 a[48:49], v146 offset:58368\n"
"v_add_f32 v211, v211, v215\n"
"v_mfma_f32_16x16x32_bf16 v[36:39], a[8:11], v[196:199], v[36:39]\n"
"ds_read_b64_tr_b16 a[50:51], v146 offset:58624\n"
"v_mul_f32 v208, s87, v208\n"
"v_mfma_f32_16x16x32_bf16 v[40:43], a[8:11], v[200:203], v[40:43]\n"
"ds_read_b64_tr_b16 a[52:53], v147 offset:58368\n"
"v_mul_f32 v209, s87, v209\n"
"v_mfma_f32_16x16x32_bf16 v[44:47], a[8:11], v[204:207], v[44:47]\n"
"ds_read_b64_tr_b16 a[54:55], v147 offset:58624\n"
"v_mul_f32 v210, s87, v210\n"
"v_mfma_f32_16x16x32_bf16 v[60:63], a[12:15], v[204:207], v[60:63]\n"
"ds_read_b64_tr_b16 a[56:57], v146 offset:58880\n"
"v_mul_f32 v211, s87, v211\n"
"v_mfma_f32_16x16x32_bf16 v[56:59], a[12:15], v[200:203], v[56:59]\n"
"ds_read_b64_tr_b16 a[58:59], v146 offset:59136\n"
"v_cvt_pk_bf16_f32 v208, v208, v209\n"
"v_mfma_f32_16x16x32_bf16 v[52:55], a[12:15], v[196:199], v[52:55]\n"
"ds_read_b64_tr_b16 a[60:61], v147 offset:58880\n"
"v_cvt_pk_bf16_f32 v210, v210, v211\n"
"v_mfma_f32_16x16x32_bf16 v[48:51], a[12:15], v[192:195], v[48:51]\n"
"ds_read_b64_tr_b16 a[62:63], v147 offset:59136\n"
"v_mov_b32 v212, v208\n"
"v_mfma_f32_16x16x32_bf16 v[64:67], a[16:19], v[192:195], v[64:67]\n"
"v_mov_b32 v213, v210\n"
"v_mfma_f32_16x16x32_bf16 v[68:71], a[16:19], v[196:199], v[68:71]\n"
"v_permlane32_swap_b32 v208, v212\n"
"v_mfma_f32_16x16x32_bf16 v[72:75], a[16:19], v[200:203], v[72:75]\n"
"v_permlane32_swap_b32 v210, v213\n"
"v_mfma_f32_16x16x32_bf16 v[76:79], a[16:19], v[204:207], v[76:79]\n"
"v_lshlrev_b32 v244, 16, v208\n"
"v_and_b32 v245, 0xffff0000, v208\n"
"v_mfma_f32_16x16x32_bf16 v[92:95], a[20:23], v[204:207], v[92:95]\n"
"v_lshlrev_b32 v246, 16, v210\n"
"v_and_b32 v247, 0xffff0000, v210\n"
"v_mfma_f32_16x16x32_bf16 v[88:91], a[20:23], v[200:203], v[88:91]\n"
"v_mul_f32 v216, 0xbfb8aa3b, v244\n"
"v_mul_f32 v217, 0xbfb8aa3b, v245\n"
"v_mfma_f32_16x16x32_bf16 v[84:87], a[20:23], v[196:199], v[84:87]\n"
"v_mul_f32 v218, 0xbfb8aa3b, v246\n"
"v_mul_f32 v219, 0xbfb8aa3b, v247\n"
"v_mfma_f32_16x16x32_bf16 v[80:83], a[20:23], v[192:195], v[80:83]\n"
"v_exp_f32 v216, v216\n"
"v_mfma_f32_16x16x32_bf16 v[96:99], a[24:27], v[192:195], v[96:99]\n"
"v_exp_f32 v217, v217\n"
"v_mfma_f32_16x16x32_bf16 v[100:103], a[24:27], v[196:199], v[100:103]\n"
"v_exp_f32 v218, v218\n"
"v_mfma_f32_16x16x32_bf16 v[104:107], a[24:27], v[200:203], v[104:107]\n"
"s_waitcnt vmcnt(0)\n"
"v_cmp_ne_u32 vcc, s77, v156\n"
"s_cmp_le_u32 s83, 0\ns_cselect_b32 s24, 2, 1\ns_mov_b32 s25, 0\n"
"s_nop 0\n"
"s_and_b64 vcc, vcc, s[24:25]\n"
"s_nop 4\n"
"s_cbranch_vccnz LSO1_%=\n"
"LBO1_%=:\n"
"v_mfma_f32_16x16x32_bf16 v[108:111], a[24:27], v[204:207], v[108:111]\n"
"s_cmp_le_u32 s83, 0\ns_cselect_b32 s90, 1, 0\ns_lshl_b32 s90, s90, 12\n"
"s_add_u32 s70, s90, 0\n"
"buffer_load_dwordx4 a[128:131], v152, s[40:43], s70 offen offset:0 sc1\n"
"v_mfma_f32_16x16x32_bf16 v[124:127], a[28:31], v[204:207], v[124:127]\n"
"buffer_load_dword v156, v155, s[48:51], s75 offen offset:0 sc1\n"
"v_exp_f32 v219, v219\n"
"v_mfma_f32_16x16x32_bf16 v[120:123], a[28:31], v[200:203], v[120:123]\n"
"v_lshlrev_b32 v220, 16, v212\n"
"v_and_b32 v221, 0xffff0000, v212\n"
"v_mfma_f32_16x16x32_bf16 v[116:119], a[28:31], v[196:199], v[116:119]\n"
"s_add_u32 s70, s90, 131072\n"
"buffer_load_dwordx4 a[132:135], v152, s[40:43], s70 offen offset:0 sc1\n"
"v_lshlrev_b32 v222, 16, v213\n"
"v_mfma_f32_16x16x32_bf16 v[112:115], a[28:31], v[192:195], v[112:115]\n"
"v_and_b32 v223, 0xffff0000, v213\n"
"v_add_f32 v216, 1.0, v216\n"
"s_waitcnt lgkmcnt(0)\n"
"s_barrier\n"
"v_mfma_f32_16x16x32_bf16 v[0:3], a[32:35], v[224:227], v[0:3]\n"
"ds_read_b64_tr_b16 a[0:1], v144 offset:0\n"
"v_add_f32 v217, 1.0, v217\n"
"v_mfma_f32_16x16x32_bf16 v[4:7], a[32:35], v[228:231], v[4:7]\n"
"s_add_u32 s70, s90, 262144\n"
"ds_read_b64_tr_b16 a[2:3], v144 offset:256\n"
"buffer_load_dwordx4 a[136:139], v152, s[40:43], s70 offen offset:0 sc1\n"
"v_mfma_f32_16x16x32_bf16 v[8:11], a[32:35], v[232:235], v[8:11]\n"
"ds_read_b64_tr_b16 a[4:5], v145 offset:0\n"
"v_add_f32 v218, 1.0, v218\n"
"v_mfma_f32_16x16x32_bf16 v[12:15], a[32:35], v[236:239], v[12:15]\n"
"ds_read_b64_tr_b16 a[6:7], v145 offset:256\n"
"v_add_f32 v219, 1.0, v219\n"
"v_mfma_f32_16x16x32_bf16 v[28:31], a[36:39], v[236:239], v[28:31]\n"
"s_sub_u32 s74, %[cls], 4\ns_cmp_le_u32 s74, 3\ns_cselect_b64 s[22:23], -1, 0\ns_cselect_b32 s95, s80, 1073741824\n"
"ds_read_b64_tr_b16 a[8:9], v144 offset:512\n"
"s_mov_b64 exec, s[22:23]\nbuffer_store_dwordx4 v[128:131], v152, s[44:47], 0 offen offset:0 sc1\ns_mov_b64 exec, -1\n"
"v_mfma_f32_16x16x32_bf16 v[24:27], a[36:39], v[232:235], v[24:27]\n"
"s_add_u32 s70, s90, 393216\n"
"ds_read_b64_tr_b16 a[10:11], v144 offset:768\n"
"buffer_load_dwordx4 a[140:143], v152, s[40:43], s70 offen offset:0 sc1\n"
"v_mfma_f32_16x16x32_bf16 v[20:23], a[36:39], v[228:231], v[20:23]\n"
"ds_read_b64_tr_b16 a[12:13], v145 offset:512\n"
"v_rcp_f32 v216, v216\n"
"v_mfma_f32_16x16x32_bf16 v[16:19], a[36:39], v[224:227], v[16:19]\n"
"ds_read_b64_tr_b16 a[14:15], v145 offset:768\n"
"v_rcp_f32 v217, v217\n"
"v_mfma_f32_16x16x32_bf16 v[32:35], a[40:43], v[224:227], v[32:35]\n"
"s_add_u32 s70, s90, 0\n"
"ds_read_b64_tr_b16 a[16:17], v144 offset:1024\n"
"buffer_load_dwordx4 a[144:147], v152, s[40:43], s70 offen offset:1024 sc1\n"
"v_mfma_f32_16x16x32_bf16 v[36:39], a[40:43], v[228:231], v[36:39]\n"
"ds_read_b64_tr_b16 a[18:19], v144 offset:1280\n"
"v_rcp_f32 v218, v218\n"
"v_mfma_f32_16x16x32_bf16 v[40:43], a[40:43], v[232:235], v[40:43]\n"
"ds_read_b64_tr_b16 a[20:21], v145 offset:1024\n"
"v_rcp_f32 v219, v219\n"
"v_mfma_f32_16x16x32_bf16 v[44:47], a[40:43], v[236:239], v[44:47]\n"
"s_add_u32 s70, s90, 131072\n"
"ds_read_b64_tr_b16 a[22:23], v145 offset:1280\n"
"s_mov_b64 exec, s[22:23]\nbuffer_store_dwordx4 v[160:163], v152, s[44:47], 0 offen offset:1024 sc1\ns_mov_b64 exec, -1\n"
"v_mfma_f32_16x16x32_bf16 v[60:63], a[44:47], v[236:239], v[60:63]\n"
"ds_read_b64_tr_b16 a[24:25], v144 offset:1536\n"
"buffer_load_dwordx4 a[148:151], v152, s[40:43], s70 offen offset:1024 sc1\n"
"v_mfma_f32_16x16x32_bf16 v[56:59], a[44:47], v[232:235], v[56:59]\n"
"ds_read_b64_tr_b16 a[26:27], v144 offset:1792\n"
"v_mul_f32 v244, v244, v216\n"
"v_mfma_f32_16x16x32_bf16 v[52:55], a[44:47], v[228:231], v[52:55]\n"
"ds_read_b64_tr_b16 a[28:29], v145 offset:1536\n"
"v_mul_f32 v245, v245, v217\n"
"v_mfma_f32_16x16x32_bf16 v[48:51], a[44:47], v[224:227], v[48:51]\n"
"s_add_u32 s70, s90, 262144\n"
"ds_read_b64_tr_b16 a[30:31], v145 offset:1792\n"
"buffer_load_dwordx4 a[152:155], v152, s[40:43], s70 offen offset:1024 sc1\n"
"v_mfma_f32_16x16x32_bf16 v[64:67], a[48:51], v[224:227], v[64:67]\n"
"v_mul_f32 v246, v246, v218\n"
"v_mul_f32 v247, v247, v219\n"
"v_mfma_f32_16x16x32_bf16 v[68:71], a[48:51], v[228:231], v[68:71]\n"
"v_cvt_pk_bf16_f32 v244, v244, v245\n"
"v_cvt_pk_bf16_f32 v246, v246, v247\n"
"v_mfma_f32_16x16x32_bf16 v[72:75], a[48:51], v[232:235], v[72:75]\n"
"s_add_u32 s70, s90, 393216\n"
"s_mov_b64 exec, s[22:23]\nbuffer_store_dwordx4 v[192:195], v152, s[44:47], 0 offen offset:2048 sc1\ns_mov_b64 exec, -1\n"
"buffer_load_dwordx4 a[156:159], v152, s[40:43], s70 offen offset:1024 sc1\n"
"v_mfma_f32_16x16x32_bf16 v[76:79], a[48:51], v[236:239], v[76:79]\n"
"v_and_b32 v245, 0xffff0000, v244\n"
"v_lshlrev_b32 v244, 16, v244\n"
"v_mfma_f32_16x16x32_bf16 v[92:95], a[52:55], v[236:239], v[92:95]\n"
"v_and_b32 v247, 0xffff0000, v246\n"
"v_lshlrev_b32 v246, 16, v246\n"
"v_mfma_f32_16x16x32_bf16 v[88:91], a[52:55], v[232:235], v[88:91]\n"
"s_add_u32 s70, s90, 0\n"
"buffer_load_dwordx4 a[160:163], v152, s[40:43], s70 offen offset:2048 sc1\n"
"v_mul_f32 v244, v244, v220\n"
"v_mfma_f32_16x16x32_bf16 v[84:87], a[52:55], v[228:231], v[84:87]\n"
"v_mul_f32 v245, v245, v221\n"
"v_mul_f32 v246, v246, v222\n"
"v_mfma_f32_16x16x32_bf16 v[80:83], a[52:55], v[224:227], v[80:83]\n"
"v_mul_f32 v247, v247, v223\n"
"v_cvt_pk_bf16_f32 v244, v244, v245\n"
"v_mfma_f32_16x16x32_bf16 v[96:99], a[56:59], v[224:227], v[96:99]\n"
"s_add_u32 s70, s90, 131072\n"
"buffer_load_dwordx4 a[164:167], v152, s[40:43], s70 offen offset:2048 sc1\n"
"v_cvt_pk_bf16_f32 v245, v246, v247\n"
"v_mfma_f32_16x16x32_bf16 v[100:103], a[56:59], v[228:231], v[100:103]\n"
"s_mov_b64 exec, s[22:23]\nbuffer_store_dwordx4 v[224:227], v152, s[44:47], 0 offen offset:3072 sc1\ns_mov_b64 exec, -1\n"
"v_and_b32 v254, 7, v158\n"
"v_mfma_f32_16x16x32_bf16 v[104:107], a[56:59], v[232:235], v[104:107]\n"
"v_lshlrev_b32 v254, 3, v254\n"
"v_add_u32 v254, s92, v254\n"
"v_mfma_f32_16x16x32_bf16 v[108:111], a[56:59], v[236:239], v[108:111]\n"
"s_add_u32 s70, s90, 262144\n"
"buffer_load_dwordx4 a[168:171], v152, s[40:43], s70 offen offset:2048 sc1\n"
"ds_write_b64 v254, v[244:245] offset:0\n"
"v_mfma_f32_16x16x32_bf16 v[124:127], a[60:63], v[236:239], v[124:127]\n"
"v_and_b32 v254, 48, v158\n"
"v_add_u32 v254, 131072, v254\n"
"v_mfma_f32_16x16x32_bf16 v[120:123], a[60:63], v[232:235], v[120:123]\n"
"v_mfma_f32_16x16x32_bf16 v[116:119], a[60:63], v[228:231], v[116:119]\n"
"s_add_u32 s70, s90, 393216\n"
"buffer_load_dwordx4 a[172:175], v152, s[40:43], s70 offen offset:2048 sc1\n"
"v_mfma_f32_16x16x32_bf16 v[112:115], a[60:63], v[224:227], v[112:115]\n"
"s_waitcnt lgkmcnt(0)\n"
"s_waitcnt vmcnt(11)\n"
"v_mfma_f32_16x16x32_bf16 v[0:3], a[0:3], a[128:131], v[0:3]\n"
"ds_read_b64_tr_b16 a[32:33], v144 offset:8192\n"
"s_mov_b64 exec, s[22:23]\nbuffer_store_dwordx4 v[132:135], v152, s[44:47], s97 offen offset:0 sc1\ns_mov_b64 exec, -1\n"
"v_mfma_f32_16x16x32_bf16 v[4:7], a[0:3], a[132:135], v[4:7]\n"
"s_add_u32 s70, s90, 0\n"
"ds_read_b64_tr_b16 a[34:35], v144 offset:8448\n"
"buffer_load_dwordx4 a[176:179], v152, s[40:43], s70 offen offset:3072 sc1\n"
"v_mfma_f32_16x16x32_bf16 v[8:11], a[0:3], a[136:139], v[8:11]\n"
"ds_read_b64_tr_b16 a[36:37], v145 offset:8192\n"
"v_mfma_f32_16x16x32_bf16 v[12:15], a[0:3], a[140:143], v[12:15]\n"
"ds_read_b64_tr_b16 a[38:39], v145 offset:8448\n"
"v_mfma_f32_16x16x32_bf16 v[28:31], a[4:7], a[140:143], v[28:31]\n"
"s_add_u32 s70, s90, 131072\n"
"ds_read_b64_tr_b16 a[40:41], v144 offset:8704\n"
"buffer_load_dwordx4 a[180:183], v152, s[40:43], s70 offen offset:3072 sc1\n"
"v_mfma_f32_16x16x32_bf16 v[24:27], a[4:7], a[136:139], v[24:27]\n"
"ds_read_b64_tr_b16 a[42:43], v144 offset:8960\n"
"v_mfma_f32_16x16x32_bf16 v[20:23], a[4:7], a[132:135], v[20:23]\n"
"ds_read_b64_tr_b16 a[44:45], v145 offset:8704\n"
"v_mfma_f32_16x16x32_bf16 v[16:19], a[4:7], a[128:131], v[16:19]\n"
"s_add_u32 s70, s90, 262144\n"
"ds_read_b64_tr_b16 a[46:47], v145 offset:8960\n"
"s_mov_b64 exec, s[22:23]\nbuffer_store_dwordx4 v[164:167], v152, s[44:47], s97 offen offset:1024 sc1\ns_mov_b64 exec, -1\n"
"v_mfma_f32_16x16x32_bf16 v[32:35], a[8:11], a[128:131], v[32:35]\n"
"ds_read_b64_tr_b16 a[48:49], v144 offset:9216\n"
"buffer_load_dwordx4 a[184:187], v152, s[40:43], s70 offen offset:3072 sc1\n"
"v_mfma_f32_16x16x32_bf16 v[36:39], a[8:11], a[132:135], v[36:39]\n"
"ds_read_b64_tr_b16 a[50:51], v144 offset:9472\n"
"v_mfma_f32_16x16x32_bf16 v[40:43], a[8:11], a[136:139], v[40:43]\n"
"ds_read_b64_tr_b16 a[52:53], v145 offset:9216\n"
"v_mfma_f32_16x16x32_bf16 v[44:47], a[8:11], a[140:143], v[44:47]\n"
"s_add_u32 s70, s90, 393216\n"
"ds_read_b64_tr_b16 a[54:55], v145 offset:9472\n"
"buffer_load_dwordx4 a[188:191], v152, s[40:43], s70 offen offset:3072 sc1\n"
"v_mfma_f32_16x16x32_bf16 v[60:63], a[12:15], a[140:143], v[60:63]\n"
"ds_read_b64_tr_b16 a[56:57], v144 offset:9728\n"
"v_mfma_f32_16x16x32_bf16 v[56:59], a[12:15], a[136:139], v[56:59]\n"
"ds_read_b64_tr_b16 a[58:59], v144 offset:9984\n"
"v_mfma_f32_16x16x32_bf16 v[52:55], a[12:15], a[132:135], v[52:55]\n"
"ds_read_b64_tr_b16 a[60:61], v145 offset:9728\n"
"s_mov_b64 exec, s[22:23]\nbuffer_store_dwordx4 v[196:199], v152, s[44:47], s97 offen offset:2048 sc1\ns_mov_b64 exec, -1\n"
"v_mfma_f32_16x16x32_bf16 v[48:51], a[12:15], a[128:131], v[48:51]\n"
"s_mov_b32 s54, 4194304\n"
"s_mov_b32 s55, 0x00020000\n"
"ds_read_b64_tr_b16 a[62:63], v145 offset:9984\n"
"v_mfma_f32_16x16x32_bf16 v[64:67], a[16:19], a[128:131], v[64:67]\n"
"s_mov_b32 s58, 4194304\n"
"s_mov_b32 s59, 0x00020000\n"
"s_lshl_b32 s72, s84, 17\n"
"v_mfma_f32_16x16x32_bf16 v[68:71], a[16:19], a[132:135], v[68:71]\n"
"s_lshl_b32 s73, s83, 8\n"
"s_add_u32 s72, s72, s73\n"
"s_lshl_b32 s73, s82, 6\n"
"v_mfma_f32_16x16x32_bf16 v[72:75], a[16:19], a[136:139], v[72:75]\n"
"s_add_u32 s72, s72, s73\n"
"v_lshrrev_b32 v250, 2, v158\n"
"v_and_b32 v251, 3, v158\n"
"v_mfma_f32_16x16x32_bf16 v[76:79], a[16:19], a[140:143], v[76:79]\n"
"v_lshlrev_b32 v157, 11, v250\n"
"v_lshl_add_u32 v157, v251, 4, v157\n"
"v_mfma_f32_16x16x32_bf16 v[92:95], a[20:23], a[140:143], v[92:95]\n"
"v_add_u32 v157, s72, v157\n"
"v_mfma_f32_16x16x32_bf16 v[88:91], a[20:23], a[136:139], v[88:91]\n"
"s_mov_b32 s26, 32768\n"
"s_mov_b32 s27, 65536\n"
"s_mov_b64 exec, s[22:23]\nbuffer_store_dwordx4 v[228:231], v152, s[44:47], s97 offen offset:3072 sc1\ns_mov_b64 exec, -1\n"
"v_mfma_f32_16x16x32_bf16 v[84:87], a[20:23], a[132:135], v[84:87]\n"
"s_mov_b32 s28, 98304\n"
"v_mfma_f32_16x16x32_bf16 v[80:83], a[20:23], a[128:131], v[80:83]\n"
"s_waitcnt vmcnt(23)\n"
"v_cmp_ne_u32 vcc, s77, v156\n"
"s_lshl_b32 s24, 1, s83\ns_andn2_b32 s24, 15, s24\ns_mov_b32 s25, 0\n"
"s_nop 0\n"
"s_and_b64 vcc, vcc, s[24:25]\n"
"s_nop 4\n"
"s_cbranch_vccnz LSO2_%=\n"
"LBO2_%=:\n"
"v_mfma_f32_16x16x32_bf16 v[96:99], a[24:27], a[128:131], v[96:99]\n"
"s_cmp_le_u32 s83, 1\ns_cselect_b32 s90, 2, 1\ns_lshl_b32 s90, s90, 12\n"
"s_add_u32 s70, s90, 0\n"
"buffer_load_dwordx4 a[192:195], v152, s[40:43], s70 offen offset:0 sc1\n"
"v_mfma_f32_16x16x32_bf16 v[100:103], a[24:27], a[132:135], v[100:103]\n"
"v_mfma_f32_16x16x32_bf16 v[104:107], a[24:27], a[136:139], v[104:107]\n"
"v_mfma_f32_16x16x32_bf16 v[108:111], a[24:27], a[140:143], v[108:111]\n"
"v_mfma_f32_16x16x32_bf16 v[124:127], a[28:31], a[140:143], v[124:127]\n"
"s_mov_b64 exec, s[22:23]\nbuffer_store_dwordx4 v[136:139], v152, s[44:47], s98 offen offset:0 sc1\ns_mov_b64 exec, -1\n"
"v_mfma_f32_16x16x32_bf16 v[120:123], a[28:31], a[136:139], v[120:123]\n"
"s_add_u32 s70, s90, 131072\n"
"buffer_load_dwordx4 a[196:199], v152, s[40:43], s70 offen offset:0 sc1\n"
"v_mfma_f32_16x16x32_bf16 v[116:119], a[28:31], a[132:135], v[116:119]\n"
"v_mfma_f32_16x16x32_bf16 v[112:115], a[28:31], a[128:131], v[112:115]\n"
"s_waitcnt lgkmcnt(0)\n"
"s_barrier\n"
"s_waitcnt vmcnt(16)\n"
"v_mfma_f32_16x16x32_bf16 v[0:3], a[32:35], a[144:147], v[0:3]\n"
"ds_read_b64_tr_b16 a[0:1], v144 offset:16384\n"
"v_mfma_f32_16x16x32_bf16 v[4:7], a[32:35], a[148:151], v[4:7]\n"
"ds_read_b64_tr_b16 a[2:3], v144 offset:16640\n"
"v_mfma_f32_16x16x32_bf16 v[8:11], a[32:35], a[152:155], v[8:11]\n"
"s_add_u32 s70, s90, 262144\n"
"ds_read_b64_tr_b16 a[4:5], v145 offset:16384\n"
"buffer_load_dwordx4 a[200:203], v152, s[40:43], s70 offen offset:0 sc1\n"
"v_mfma_f32_16x16x32_bf16 v[12:15], a[32:35], a[156:159], v[12:15]\n"
"ds_read_b64_tr_b16 a[6:7], v145 offset:16640\n"
"s_mov_b64 exec, s[22:23]\nbuffer_store_dwordx4 v[168:171], v152, s[44:47], s98 offen offset:1024 sc1\ns_mov_b64 exec, -1\n"
"v_mfma_f32_16x16x32_bf16 v[28:31], a[36:39], a[156:159], v[28:31]\n"
"ds_read_b64_tr_b16 a[8:9], v144 offset:16896\n"
"v_mfma_f32_16x16x32_bf16 v[24:27], a[36:39], a[152:155], v[24:27]\n"
"ds_read_b64_tr_b16 a[10:11], v144 offset:17152\n"
"v_mfma_f32_16x16x32_bf16 v[20:23], a[36:39], a[148:151], v[20:23]\n"
"ds_read_b64_tr_b16 a[12:13], v145 offset:16896\n"
"v_mfma_f32_16x16x32_bf16 v[16:19], a[36:39], a[144:147], v[16:19]\n"
"s_add_u32 s70, s90, 393216\n"
"ds_read_b64_tr_b16 a[14:15], v145 offset:17152\n"
"buffer_load_dwordx4 a[204:207], v152, s[40:43], s70 offen offset:0 sc1\n"
"v_mfma_f32_16x16x32_bf16 v[32:35], a[40:43], a[144:147], v[32:35]\n"
"ds_read_b64_tr_b16 a[16:17], v144 offset:17408\n"
"v_mfma_f32_16x16x32_bf16 v[36:39], a[40:43], a[148:151], v[36:39]\n"
"ds_read_b64_tr_b16 a[18:19], v144 offset:17664\n"
"v_mfma_f32_16x16x32_bf16 v[40:43], a[40:43], a[152:155], v[40:43]\n"
"ds_read_b64_tr_b16 a[20:21], v145 offset:17408\n"
"s_mov_b64 exec, s[22:23]\nbuffer_store_dwordx4 v[200:203], v152, s[44:47], s98 offen offset:2048 sc1\ns_mov_b64 exec, -1\n"
"v_mfma_f32_16x16x32_bf16 v[44:47], a[40:43], a[156:159], v[44:47]\n"
"ds_read_b64_tr_b16 a[22:23], v145 offset:17664\n"
"v_mfma_f32_16x16x32_bf16 v[60:63], a[44:47], a[156:159], v[60:63]\n"
"s_add_u32 s70, s90, 0\n"
"ds_read_b64_tr_b16 a[24:25], v144 offset:17920\n"
"buffer_load_dwordx4 a[208:211], v152, s[40:43], s70 offen offset:1024 sc1\n"
"v_mfma_f32_16x16x32_bf16 v[56:59], a[44:47], a[152:155], v[56:59]\n"
"ds_read_b64_tr_b16 a[26:27], v144 offset:18176\n"
"v_mfma_f32_16x16x32_bf16 v[52:55], a[44:47], a[148:151], v[52:55]\n"
"ds_read_b64_tr_b16 a[28:29], v145 offset:17920\n"
"v_mfma_f32_16x16x32_bf16 v[48:51], a[44:47], a[144:147], v[48:51]\n"
"ds_read_b64_tr_b16 a[30:31], v145 offset:18176\n"
"v_mfma_f32_16x16x32_bf16 v[64:67], a[48:51], a[144:147], v[64:67]\n"
"ds_read_b128 a[96:99], v254 offset:0\n"
"ds_read_b128 a[100:103], v254 offset:512\n"
"v_mfma_f32_16x16x32_bf16 v[68:71], a[48:51], a[148:151], v[68:71]\n"
"s_add_u32 s70, s90, 131072\n"
"s_mov_b64 exec, s[22:23]\nbuffer_store_dwordx4 v[232:235], v152, s[44:47], s98 offen offset:3072 sc1\ns_mov_b64 exec, -1\n"
"ds_read_b128 a[104:107], v254 offset:1024\n"
"v_mfma_f32_16x16x32_bf16 v[72:75], a[48:51], a[152:155], v[72:75]\n"
"ds_read_b128 a[108:111], v254 offset:1536\n"
"buffer_load_dwordx4 a[212:215], v152, s[40:43], s70 offen offset:1024 sc1\n"
"v_mfma_f32_16x16x32_bf16 v[76:79], a[48:51], a[156:159], v[76:79]\n"
"v_mfma_f32_16x16x32_bf16 v[92:95], a[52:55], a[156:159], v[92:95]\n"
"v_mfma_f32_16x16x32_bf16 v[88:91], a[52:55], a[152:155], v[88:91]\n"
"v_mfma_f32_16x16x32_bf16 v[84:87], a[52:55], a[148:151], v[84:87]\n"
"v_mfma_f32_16x16x32_bf16 v[80:83], a[52:55], a[144:147], v[80:83]\n"
"s_add_u32 s70, s90, 262144\n"
"buffer_load_dwordx4 a[216:219], v152, s[40:43], s70 offen offset:1024 sc1\n"
"v_mfma_f32_16x16x32_bf16 v[96:99], a[56:59], a[144:147], v[96:99]\n"
"s_mov_b64 exec, s[22:23]\nbuffer_store_dwordx4 v[140:143], v152, s[44:47], s99 offen offset:0 sc1\ns_mov_b64 exec, -1\n"
"v_mfma_f32_16x16x32_bf16 v[100:103], a[56:59], a[148:151], v[100:103]\n"
"v_mfma_f32_16x16x32_bf16 v[104:107], a[56:59], a[152:155], v[104:107]\n"
"v_mfma_f32_16x16x32_bf16 v[108:111], a[56:59], a[156:159], v[108:111]\n"
"v_mfma_f32_16x16x32_bf16 v[124:127], a[60:63], a[156:159], v[124:127]\n"
"s_add_u32 s70, s90, 393216\n"
"buffer_load_dwordx4 a[220:223], v152, s[40:43], s70 offen offset:1024 sc1\n"
"v_mfma_f32_16x16x32_bf16 v[120:123], a[60:63], a[152:155], v[120:123]\n"
"v_mfma_f32_16x16x32_bf16 v[116:119], a[60:63], a[148:151], v[116:119]\n"
"v_mfma_f32_16x16x32_bf16 v[112:115], a[60:63], a[144:147], v[112:115]\n"
"s_mov_b64 exec, s[22:23]\nbuffer_store_dwordx4 v[172:175], v152, s[44:47], s99 offen offset:1024 sc1\ns_mov_b64 exec, -1\n"
"s_waitcnt lgkmcnt(0)\n"
"s_waitcnt vmcnt(22)\n"
"v_mfma_f32_16x16x32_bf16 a[112:115], a[64:67], a[96:99], 0\n"
"ds_read_b64_tr_b16 a[32:33], v144 offset:24576\n"
"v_mfma_f32_16x16x32_bf16 a[116:119], a[80:83], a[96:99], 0\n"
"s_add_u32 s70, s90, 0\n"
"ds_read_b64_tr_b16 a[34:35], v144 offset:24832\n"
"buffer_load_dwordx4 a[224:227], v152, s[40:43], s70 offen offset:2048 sc1\n"
"v_mfma_f32_16x16x32_bf16 a[112:115], a[68:71], a[100:103], a[112:115]\n"
"ds_read_b64_tr_b16 a[36:37], v145 offset:24576\n"
"v_mfma_f32_16x16x32_bf16 a[116:119], a[84:87], a[100:103], a[116:119]\n"
"ds_read_b64_tr_b16 a[38:39], v145 offset:24832\n"
"v_mfma_f32_16x16x32_bf16 a[112:115], a[72:75], a[104:107], a[112:115]\n"
"ds_read_b64_tr_b16 a[40:41], v144 offset:25088\n"
"v_mfma_f32_16x16x32_bf16 a[116:119], a[88:91], a[104:107], a[116:119]\n"
"ds_read_b64_tr_b16 a[42:43], v144 offset:25344\n"
"v_mfma_f32_16x16x32_bf16 a[112:115], a[76:79], a[108:111], a[112:115]\n"
"s_add_u32 s70, s90, 131072\n"
"ds_read_b64_tr_b16 a[44:45], v145 offset:25088\n"
"s_mov_b64 exec, s[22:23]\nbuffer_store_dwordx4 v[204:207], v152, s[44:47], s99 offen offset:2048 sc1\ns_mov_b64 exec, -1\n"
"v_mfma_f32_16x16x32_bf16 a[116:119], a[92:95], a[108:111], a[116:119]\n"
"ds_read_b64_tr_b16 a[46:47], v145 offset:25344\n"
"buffer_load_dwordx4 a[228:231], v152, s[40:43], s70 offen offset:2048 sc1\n"
"v_mfma_f32_16x16x32_bf16 v[0:3], a[0:3], a[160:163], v[0:3]\n"
"ds_read_b64_tr_b16 a[48:49], v144 offset:25600\n"
"v_mfma_f32_16x16x32_bf16 v[4:7], a[0:3], a[164:167], v[4:7]\n"
"ds_read_b64_tr_b16 a[50:51], v144 offset:25856\n"
"v_mfma_f32_16x16x32_bf16 v[8:11], a[0:3], a[168:171], v[8:11]\n"
"ds_read_b64_tr_b16 a[52:53], v145 offset:25600\n"
"v_mfma_f32_16x16x32_bf16 v[12:15], a[0:3], a[172:175], v[12:15]\n"
"ds_read_b64_tr_b16 a[54:55], v145 offset:25856\n"
"v_mfma_f32_16x16x32_bf16 v[28:31], a[4:7], a[172:175], v[28:31]\n"
"s_lshl_b32 s30, s78, 2\n"
"s_add_u32 s30, s30, s79\n"
"ds_read_b64_tr_b16 a[56:57], v144 offset:26112\n"
"v_mfma_f32_16x16x32_bf16 v[24:27], a[4:7], a[168:171], v[24:27]\n"
"s_lshl_b32 s93, s30, 4\n"
"ds_read_b64_tr_b16 a[58:59], v144 offset:26368\n"
"s_mov_b64 exec, s[22:23]\nbuffer_store_dwordx4 v[236:239], v152, s[44:47], s99 offen offset:3072 sc1\ns_mov_b64 exec, -1\n"
"v_mfma_f32_16x16x32_bf16 v[20:23], a[4:7], a[164:167], v[20:23]\n"
"s_mov_b32 s94, s95\n"
"s_add_u32 s93, s93, 688128\n"
"ds_read_b64_tr_b16 a[60:61], v145 offset:26112\n"
"v_mfma_f32_16x16x32_bf16 v[16:19], a[4:7], a[160:163], v[16:19]\n"
"s_lshl_b32 s30, s30, 7\n"
"s_add_u32 s30, s30, 704512\n"
"ds_read_b64_tr_b16 a[62:63], v145 offset:26368\n"
"v_mfma_f32_16x16x32_bf16 v[32:35], a[8:11], a[160:163], v[32:35]\n"
"s_add_u32 s70, s90, 262144\n"
"v_and_b32 v254, 15, v158\n"
"v_lshlrev_b32 v254, 24, v254\n"
"v_mfma_f32_16x16x32_bf16 v[36:39], a[8:11], a[164:167], v[36:39]\n"
"v_and_b32 v244, 48, v158\n"
"v_add_u32 v254, v254, v244\n"
"v_mfma_f32_16x16x32_bf16 v[40:43], a[8:11], a[168:171], v[40:43]\n"
"buffer_store_dwordx4 a[112:115], v254, s[48:51], s30 offen offset:0 sc1\n"
"buffer_store_dwordx4 a[116:119], v254, s[48:51], s30 offen offset:64 sc1\n"
"v_mfma_f32_16x16x32_bf16 v[44:47], a[8:11], a[172:175], v[44:47]\n"
"buffer_load_dwordx4 a[232:235], v152, s[40:43], s70 offen offset:2048 sc1\n"
"v_mfma_f32_16x16x32_bf16 v[60:63], a[12:15], a[172:175], v[60:63]\n"
"v_mfma_f32_16x16x32_bf16 v[56:59], a[12:15], a[168:171], v[56:59]\n"
"v_mfma_f32_16x16x32_bf16 v[52:55], a[12:15], a[164:167], v[52:55]\n"
"v_mfma_f32_16x16x32_bf16 v[48:51], a[12:15], a[160:163], v[48:51]\n"
"v_mfma_f32_16x16x32_bf16 v[64:67], a[16:19], a[160:163], v[64:67]\n"
"s_add_u32 s70, s90, 393216\n"
"buffer_load_dwordx4 a[236:239], v152, s[40:43], s70 offen offset:2048 sc1\n"
"v_mfma_f32_16x16x32_bf16 v[68:71], a[16:19], a[164:167], v[68:71]\n"
"v_mfma_f32_16x16x32_bf16 v[72:75], a[16:19], a[168:171], v[72:75]\n"
"v_mfma_f32_16x16x32_bf16 v[76:79], a[16:19], a[172:175], v[76:79]\n"
"v_mfma_f32_16x16x32_bf16 v[92:95], a[20:23], a[172:175], v[92:95]\n"
"v_mfma_f32_16x16x32_bf16 v[88:91], a[20:23], a[168:171], v[88:91]\n"
"s_add_u32 s70, s90, 0\n"
"buffer_load_dwordx4 a[240:243], v152, s[40:43], s70 offen offset:3072 sc1\n"
"v_mfma_f32_16x16x32_bf16 v[84:87], a[20:23], a[164:167], v[84:87]\n"
"v_mfma_f32_16x16x32_bf16 v[80:83], a[20:23], a[160:163], v[80:83]\n"
"v_mfma_f32_16x16x32_bf16 v[96:99], a[24:27], a[160:163], v[96:99]\n"
"v_mfma_f32_16x16x32_bf16 v[100:103], a[24:27], a[164:167], v[100:103]\n"
"v_mfma_f32_16x16x32_bf16 v[104:107], a[24:27], a[168:171], v[104:107]\n"
"s_add_u32 s70, s90, 131072\n"
"buffer_load_dwordx4 a[244:247], v152, s[40:43], s70 offen offset:3072 sc1\n"
"v_mfma_f32_16x16x32_bf16 v[108:111], a[24:27], a[172:175], v[108:111]\n"
"v_mfma_f32_16x16x32_bf16 v[124:127], a[28:31], a[172:175], v[124:127]\n"
"v_mfma_f32_16x16x32_bf16 v[120:123], a[28:31], a[168:171], v[120:123]\n"
"v_mfma_f32_16x16x32_bf16 v[116:119], a[28:31], a[164:167], v[116:119]\n"
"v_mfma_f32_16x16x32_bf16 v[112:115], a[28:31], a[160:163], v[112:115]\n"
"s_add_u32 s70, s90, 262144\n"
"buffer_load_dwordx4 a[248:251], v152, s[40:43], s70 offen offset:3072 sc1\n"
"s_waitcnt lgkmcnt(0)\n"
"s_barrier\n"
"s_waitcnt vmcnt(27)\n"
"v_mfma_f32_16x16x32_bf16 v[0:3], a[32:35], a[176:179], v[0:3]\n"
"ds_read_b64_tr_b16 a[0:1], v144 offset:32768\n"
"v_mfma_f32_16x16x32_bf16 v[4:7], a[32:35], a[180:183], v[4:7]\n"
"ds_read_b64_tr_b16 a[2:3], v144 offset:33024\n"
"v_mfma_f32_16x16x32_bf16 v[8:11], a[32:35], a[184:187], v[8:11]\n"
"ds_read_b64_tr_b16 a[4:5], v145 offset:32768\n"
"v_mfma_f32_16x16x32_bf16 v[12:15], a[32:35], a[188:191], v[12:15]\n"
"ds_read_b64_tr_b16 a[6:7], v145 offset:33024\n"
"v_mfma_f32_16x16x32_bf16 v[28:31], a[36:39], a[188:191], v[28:31]\n"
"s_add_u32 s70, s90, 393216\n"
"ds_read_b64_tr_b16 a[8:9], v144 offset:33280\n"
"buffer_load_dwordx4 a[252:255], v152, s[40:43], s70 offen offset:3072 sc1\n"
"v_mfma_f32_16x16x32_bf16 v[24:27], a[36:39], a[184:187], v[24:27]\n"
"ds_read_b64_tr_b16 a[10:11], v144 offset:33536\n"
"v_mfma_f32_16x16x32_bf16 v[20:23], a[36:39], a[180:183], v[20:23]\n"
"ds_read_b64_tr_b16 a[12:13], v145 offset:33280\n"
"v_mfma_f32_16x16x32_bf16 v[16:19], a[36:39], a[176:179], v[16:19]\n"
"ds_read_b64_tr_b16 a[14:15], v145 offset:33536\n"
"v_mfma_f32_16x16x32_bf16 v[32:35], a[40:43], a[176:179], v[32:35]\n"
"ds_read_b64_tr_b16 a[16:17], v144 offset:33792\n"
"v_mfma_f32_16x16x32_bf16 v[36:39], a[40:43], a[180:183], v[36:39]\n"
"ds_read_b64_tr_b16 a[18:19], v144 offset:34048\n"
"v_mfma_f32_16x16x32_bf16 v[40:43], a[40:43], a[184:187], v[40:43]\n"
"s_lshr_b32 s71, s78, 3\n"
"s_mul_i32 s71, s71, 384\n"
"ds_read_b64_tr_b16 a[20:21], v145 offset:33792\n"
"v_mfma_f32_16x16x32_bf16 v[44:47], a[40:43], a[188:191], v[44:47]\n"
"s_add_u32 s71, s71, 2048\n"
"ds_read_b64_tr_b16 a[22:23], v145 offset:34048\n"
"v_min_u32 v159, 2, v158\n"
"v_mfma_f32_16x16x32_bf16 v[60:63], a[44:47], a[188:191], v[60:63]\n"
"ds_read_b64_tr_b16 a[24:25], v144 offset:34304\n"
"v_lshlrev_b32 v159, 7, v159\n"
"v_mfma_f32_16x16x32_bf16 v[56:59], a[44:47], a[184:187], v[56:59]\n"
"ds_read_b64_tr_b16 a[26:27], v144 offset:34560\n"
"v_add_u32 v159, s71, v159\n"
"v_mfma_f32_16x16x32_bf16 v[52:55], a[44:47], a[180:183], v[52:55]\n"
"s_getpc_b64 s[20:21]\n"
"ds_read_b64_tr_b16 a[28:29], v145 offset:34304\n"
"global_load_dword v253, v159, s[20:21]\n"
"v_mfma_f32_16x16x32_bf16 v[48:51], a[44:47], a[176:179], v[48:51]\n"
"s_cmp_le_u32 s83, 2\ns_cselect_b32 s89, 3, 2\ns_lshl_b32 s89, s89, 18\n"
"s_add_u32 m0, s68, 65536\n"
"ds_read_b64_tr_b16 a[30:31], v145 offset:34560\n"
"v_mfma_f32_16x16x32_bf16 v[64:67], a[48:51], a[176:179], v[64:67]\n"
"s_add_u32 s69, s89, 0\n"
"buffer_load_dwordx4 v150, s[36:39], s69 offen offset:0 lds\n"
"v_mfma_f32_16x16x32_bf16 v[68:71], a[48:51], a[180:183], v[68:71]\n"
"v_mfma_f32_16x16x32_bf16 v[72:75], a[48:51], a[184:187], v[72:75]\n"
"s_waitcnt vmcnt(8)\n"
"buffer_store_dword v153, v154, s[48:51], s93 offen offset:0 sc1\n"
"v_mfma_f32_16x16x32_bf16 v[76:79], a[48:51], a[188:191], v[76:79]\n"
"v_mfma_f32_16x16x32_bf16 v[92:95], a[52:55], a[188:191], v[92:95]\n"
"v_lshlrev_b32 v208, 16, v240\n"
"v_and_b32 v212, 0xffff0000, v240\n"
"v_mfma_f32_16x16x32_bf16 v[88:91], a[52:55], a[184:187], v[88:91]\n"
"buffer_load_dwordx4 v151, s[36:39], s69 offen offset:1024 lds\n"
"s_waitcnt vmcnt(12)\n"
"buffer_store_dword v153, v154, s[48:51], s94 offen offset:0 sc1\n"
"v_mfma_f32_16x16x32_bf16 v[84:87], a[52:55], a[180:183], v[84:87]\n"
"v_rcp_f32 v208, v208\n"
"v_mfma_f32_16x16x32_bf16 v[80:83], a[52:55], a[176:179], v[80:83]\n"
"v_lshlrev_b32 v209, 16, v241\n"
"v_and_b32 v213, 0xffff0000, v241\n"
"v_mfma_f32_16x16x32_bf16 v[96:99], a[56:59], a[176:179], v[96:99]\n"
"v_rcp_f32 v209, v209\n"
"v_mfma_f32_16x16x32_bf16 v[100:103], a[56:59], a[180:183], v[100:103]\n"
"s_add_u32 m0, s68, 73728\n"
"v_lshlrev_b32 v210, 16, v242\n"
"v_and_b32 v214, 0xffff0000, v242\n"
"v_mfma_f32_16x16x32_bf16 v[104:107], a[56:59], a[184:187], v[104:107]\n"
"s_add_u32 s69, s89, 65536\n"
"buffer_load_dwordx4 v150, s[36:39], s69 offen offset:0 lds\n"
"v_rcp_f32 v210, v210\n"
"v_mfma_f32_16x16x32_bf16 v[108:111], a[56:59], a[188:191], v[108:111]\n"
"v_lshlrev_b32 v211, 16, v243\n"
"v_and_b32 v215, 0xffff0000, v243\n"
"v_mfma_f32_16x16x32_bf16 v[124:127], a[60:63], a[188:191], v[124:127]\n"
"v_rcp_f32 v211, v211\n"
"v_mfma_f32_16x16x32_bf16 v[120:123], a[60:63], a[184:187], v[120:123]\n"
"v_mul_f32 v212, v212, v208\n"
"v_mul_f32 v216, 0xbfb8aa3b, v208\n"
"v_mfma_f32_16x16x32_bf16 v[116:119], a[60:63], a[180:183], v[116:119]\n"
"v_mul_f32 v220, 0x3fb8aa3b, v212\n"
"v_mul_f32 v213, v213, v209\n"
"v_mfma_f32_16x16x32_bf16 v[112:115], a[60:63], a[176:179], v[112:115]\n"
"buffer_load_dwordx4 v151, s[36:39], s69 offen offset:1024 lds\n"
"s_waitcnt lgkmcnt(0)\n"
"s_waitcnt vmcnt(27)\n"
"v_mfma_f32_16x16x32_bf16 v[0:3], a[0:3], a[192:195], v[0:3]\n"
"ds_read_b64_tr_b16 a[32:33], v144 offset:40960\n"
"v_mul_f32 v217, 0xbfb8aa3b, v209\n"
"v_mfma_f32_16x16x32_bf16 v[4:7], a[0:3], a[196:199], v[4:7]\n"
"ds_read_b64_tr_b16 a[34:35], v144 offset:41216\n"
"v_mul_f32 v221, 0x3fb8aa3b, v213\n"
"v_mfma_f32_16x16x32_bf16 v[8:11], a[0:3], a[200:203], v[8:11]\n"
"ds_read_b64_tr_b16 a[36:37], v145 offset:40960\n"
"v_mul_f32 v214, v214, v210\n"
"v_mfma_f32_16x16x32_bf16 v[12:15], a[0:3], a[204:207], v[12:15]\n"
"s_add_u32 m0, s68, 81920\n"
"ds_read_b64_tr_b16 a[38:39], v145 offset:41216\n"
"v_mul_f32 v218, 0xbfb8aa3b, v210\n"
"v_mfma_f32_16x16x32_bf16 v[28:31], a[4:7], a[204:207], v[28:31]\n"
"s_add_u32 s69, s89, 131072\n"
"ds_read_b64_tr_b16 a[40:41], v144 offset:41472\n"
"buffer_load_dwordx4 v150, s[36:39], s69 offen offset:0 lds\n"
"v_mfma_f32_16x16x32_bf16 v[24:27], a[4:7], a[200:203], v[24:27]\n"
"ds_read_b64_tr_b16 a[42:43], v144 offset:41728\n"
"v_mul_f32 v222, 0x3fb8aa3b, v214\n"
"v_mfma_f32_16x16x32_bf16 v[20:23], a[4:7], a[196:199], v[20:23]\n"
"ds_read_b64_tr_b16 a[44:45], v145 offset:41472\n"
"v_mul_f32 v215, v215, v211\n"
"v_mfma_f32_16x16x32_bf16 v[16:19], a[4:7], a[192:195], v[16:19]\n"
"ds_read_b64_tr_b16 a[46:47], v145 offset:41728\n"
"v_mul_f32 v219, 0xbfb8aa3b, v211\n"
"v_mfma_f32_16x16x32_bf16 v[32:35], a[8:11], a[192:195], v[32:35]\n"
"ds_read_b64_tr_b16 a[48:49], v144 offset:41984\n"
"v_mul_f32 v223, 0x3fb8aa3b, v215\n"
"v_mfma_f32_16x16x32_bf16 v[36:39], a[8:11], a[196:199], v[36:39]\n"
"ds_read_b64_tr_b16 a[50:51], v144 offset:42240\n"
"buffer_load_dwordx4 v151, s[36:39], s69 offen offset:1024 lds\n"
"v_mfma_f32_16x16x32_bf16 v[40:43], a[8:11], a[200:203], v[40:43]\n"
"ds_read_b64_tr_b16 a[52:53], v145 offset:41984\n"
"v_lshlrev_b32 v128, 16, v176\n"
"v_mfma_f32_16x16x32_bf16 v[44:47], a[8:11], a[204:207], v[44:47]\n"
"ds_read_b64_tr_b16 a[54:55], v145 offset:42240\n"
"v_and_b32 v129, 0xffff0000, v176\n"
"v_mfma_f32_16x16x32_bf16 v[60:63], a[12:15], a[204:207], v[60:63]\n"
"ds_read_b64_tr_b16 a[56:57], v144 offset:42496\n"
"v_fma_f32 v244, v128, v216, v220\n"
"v_mfma_f32_16x16x32_bf16 v[56:59], a[12:15], a[200:203], v[56:59]\n"
"s_add_u32 m0, s68, 90112\n"
"ds_read_b64_tr_b16 a[58:59], v144 offset:42752\n"
"v_fma_f32 v245, v129, v216, v220\n"
"v_mfma_f32_16x16x32_bf16 v[52:55], a[12:15], a[196:199], v[52:55]\n"
"s_add_u32 s69, s89, 196608\n"
"ds_read_b64_tr_b16 a[60:61], v145 offset:42496\n"
"buffer_load_dwordx4 v150, s[36:39], s69 offen offset:0 lds\n"
"v_mfma_f32_16x16x32_bf16 v[48:51], a[12:15], a[192:195], v[48:51]\n"
"ds_read_b64_tr_b16 a[62:63], v145 offset:42752\n"
"v_exp_f32 v244, v244\n"
"v_mfma_f32_16x16x32_bf16 v[64:67], a[16:19], a[192:195], v[64:67]\n"
"v_exp_f32 v245, v245\n"
"v_mfma_f32_16x16x32_bf16 v[68:71], a[16:19], a[196:199], v[68:71]\n"
"v_fma_f32 v136, v128, v208, -v212\n"
"v_fma_f32 v137, v129, v208, -v212\n"
"v_mfma_f32_16x16x32_bf16 v[72:75], a[16:19], a[200:203], v[72:75]\n"
"v_add_f32 v244, 1.0, v244\n"
"v_add_f32 v245, 1.0, v245\n"
"v_mfma_f32_16x16x32_bf16 v[76:79], a[16:19], a[204:207], v[76:79]\n"
"buffer_load_dwordx4 v151, s[36:39], s69 offen offset:1024 lds\n"
"v_rcp_f32 v244, v244\n"
"v_mfma_f32_16x16x32_bf16 v[92:95], a[20:23], a[204:207], v[92:95]\n"
"v_rcp_f32 v245, v245\n"
"v_mfma_f32_16x16x32_bf16 v[88:91], a[20:23], a[200:203], v[88:91]\n"
"v_mul_f32 v136, v136, v244\n"
"v_mul_f32 v137, v137, v245\n"
"v_mfma_f32_16x16x32_bf16 v[84:87], a[20:23], a[196:199], v[84:87]\n"
"v_lshlrev_b32 v130, 16, v177\n"
"v_and_b32 v131, 0xffff0000, v177\n"
"v_mfma_f32_16x16x32_bf16 v[80:83], a[20:23], a[192:195], v[80:83]\n"
"v_fma_f32 v244, v130, v216, v220\n"
"v_fma_f32 v245, v131, v216, v220\n"
"v_mfma_f32_16x16x32_bf16 v[96:99], a[24:27], a[192:195], v[96:99]\n"
"s_cmp_le_u32 s83, 2\ns_cselect_b32 s90, 3, 2\ns_lshl_b32 s90, s90, 12\n"
"s_add_u32 s70, s90, 0\n"
"buffer_load_dwordx4 a[64:67], v152, s[40:43], s70 offen offset:0 sc1\n"
"v_mfma_f32_16x16x32_bf16 v[100:103], a[24:27], a[196:199], v[100:103]\n"
"v_exp_f32 v244, v244\n"
"v_mfma_f32_16x16x32_bf16 v[104:107], a[24:27], a[200:203], v[104:107]\n"
"v_exp_f32 v245, v245\n"
"v_mfma_f32_16x16x32_bf16 v[108:111], a[24:27], a[204:207], v[108:111]\n"
"v_fma_f32 v138, v130, v208, -v212\n"
"v_fma_f32 v139, v131, v208, -v212\n"
"v_mfma_f32_16x16x32_bf16 v[124:127], a[28:31], a[204:207], v[124:127]\n"
"v_add_f32 v244, 1.0, v244\n"
"v_add_f32 v245, 1.0, v245\n"
"v_mfma_f32_16x16x32_bf16 v[120:123], a[28:31], a[200:203], v[120:123]\n"
"s_add_u32 s70, s90, 131072\n"
"buffer_load_dwordx4 a[68:71], v152, s[40:43], s70 offen offset:0 sc1\n"
"v_rcp_f32 v244, v244\n"
"v_mfma_f32_16x16x32_bf16 v[116:119], a[28:31], a[196:199], v[116:119]\n"
"v_rcp_f32 v245, v245\n"
"v_mfma_f32_16x16x32_bf16 v[112:115], a[28:31], a[192:195], v[112:115]\n"
"v_mul_f32 v138, v138, v244\n"
"s_waitcnt lgkmcnt(0)\n"
"s_waitcnt vmcnt(26)\n"
"v_mfma_f32_16x16x32_bf16 v[0:3], a[32:35], a[208:211], v[0:3]\n"
"ds_read_b64_tr_b16 a[0:1], v144 offset:49152\n"
"v_mul_f32 v139, v139, v245\n"
"v_mfma_f32_16x16x32_bf16 v[4:7], a[32:35], a[212:215], v[4:7]\n"
"ds_read_b64_tr_b16 a[2:3], v144 offset:49408\n"
"v_lshlrev_b32 v160, 16, v178\n"
"v_mfma_f32_16x16x32_bf16 v[8:11], a[32:35], a[216:219], v[8:11]\n"
"s_add_u32 s70, s90, 262144\n"
"ds_read_b64_tr_b16 a[4:5], v145 offset:49152\n"
"buffer_load_dwordx4 a[72:75], v152, s[40:43], s70 offen offset:0 sc1\n"
"v_mfma_f32_16x16x32_bf16 v[12:15], a[32:35], a[220:223], v[12:15]\n"
"ds_read_b64_tr_b16 a[6:7], v145 offset:49408\n"
"v_and_b32 v161, 0xffff0000, v178\n"
"v_mfma_f32_16x16x32_bf16 v[28:31], a[36:39], a[220:223], v[28:31]\n"
"ds_read_b64_tr_b16 a[8:9], v144 offset:49664\n"
"v_fma_f32 v244, v160, v217, v221\n"
"v_mfma_f32_16x16x32_bf16 v[24:27], a[36:39], a[216:219], v[24:27]\n"
"ds_read_b64_tr_b16 a[10:11], v144 offset:49920\n"
"v_fma_f32 v245, v161, v217, v221\n"
"v_mfma_f32_16x16x32_bf16 v[20:23], a[36:39], a[212:215], v[20:23]\n"
"ds_read_b64_tr_b16 a[12:13], v145 offset:49664\n"
"v_exp_f32 v244, v244\n"
"v_mfma_f32_16x16x32_bf16 v[16:19], a[36:39], a[208:211], v[16:19]\n"
"s_add_u32 s70, s90, 393216\n"
"ds_read_b64_tr_b16 a[14:15], v145 offset:49920\n"
"buffer_load_dwordx4 a[76:79], v152, s[40:43], s70 offen offset:0 sc1\n"
"v_mfma_f32_16x16x32_bf16 v[32:35], a[40:43], a[208:211], v[32:35]\n"
"ds_read_b64_tr_b16 a[16:17], v144 offset:50176\n"
"v_exp_f32 v245, v245\n"
"v_mfma_f32_16x16x32_bf16 v[36:39], a[40:43], a[212:215], v[36:39]\n"
"ds_read_b64_tr_b16 a[18:19], v144 offset:50432\n"
"v_fma_f32 v168, v160, v209, -v213\n"
"v_mfma_f32_16x16x32_bf16 v[40:43], a[40:43], a[216:219], v[40:43]\n"
"ds_read_b64_tr_b16 a[20:21], v145 offset:50176\n"
"v_fma_f32 v169, v161, v209, -v213\n"
"v_mfma_f32_16x16x32_bf16 v[44:47], a[40:43], a[220:223], v[44:47]\n"
"ds_read_b64_tr_b16 a[22:23], v145 offset:50432\n"
"v_add_f32 v244, 1.0, v244\n"
"v_mfma_f32_16x16x32_bf16 v[60:63], a[44:47], a[220:223], v[60:63]\n"
"s_add_u32 s70, s90, 0\n"
"ds_read_b64_tr_b16 a[24:25], v144 offset:50688\n"
"buffer_load_dwordx4 a[80:83], v152, s[40:43], s70 offen offset:1024 sc1\n"
"v_mfma_f32_16x16x32_bf16 v[56:59], a[44:47], a[216:219], v[56:59]\n"
"ds_read_b64_tr_b16 a[26:27], v144 offset:50944\n"
"v_add_f32 v245, 1.0, v245\n"
"v_mfma_f32_16x16x32_bf16 v[52:55], a[44:47], a[212:215], v[52:55]\n"
"ds_read_b64_tr_b16 a[28:29], v145 offset:50688\n"
"v_rcp_f32 v244, v244\n"
"v_mfma_f32_16x16x32_bf16 v[48:51], a[44:47], a[208:211], v[48:51]\n"
"ds_read_b64_tr_b16 a[30:31], v145 offset:50944\n"
"v_rcp_f32 v245, v245\n"
"v_mfma_f32_16x16x32_bf16 v[64:67], a[48:51], a[208:211], v[64:67]\n"
"v_mul_f32 v168, v168, v244\n"
"v_mul_f32 v169, v169, v245\n"
"v_mfma_f32_16x16x32_bf16 v[68:71], a[48:51], a[212:215], v[68:71]\n"
"s_add_u32 s70, s90, 131072\n"
"buffer_load_dwordx4 a[84:87], v152, s[40:43], s70 offen offset:1024 sc1\n"
"v_lshlrev_b32 v162, 16, v179\n"
"v_mfma_f32_16x16x32_bf16 v[72:75], a[48:51], a[216:219], v[72:75]\n"
"v_and_b32 v163, 0xffff0000, v179\n"
"v_fma_f32 v244, v162, v217, v221\n"
"v_mfma_f32_16x16x32_bf16 v[76:79], a[48:51], a[220:223], v[76:79]\n"
"v_fma_f32 v245, v163, v217, v221\n"
"v_mfma_f32_16x16x32_bf16 v[92:95], a[52:55], a[220:223], v[92:95]\n"
"v_exp_f32 v244, v244\n"
"v_mfma_f32_16x16x32_bf16 v[88:91], a[52:55], a[216:219], v[88:91]\n"
"v_exp_f32 v245, v245\n"
"v_mfma_f32_16x16x32_bf16 v[84:87], a[52:55], a[212:215], v[84:87]\n"
"s_add_u32 s70, s90, 262144\n"
"buffer_load_dwordx4 a[88:91], v152, s[40:43], s70 offen offset:1024 sc1\n"
"v_fma_f32 v170, v162, v209, -v213\n"
"v_mfma_f32_16x16x32_bf16 v[80:83], a[52:55], a[208:211], v[80:83]\n"
"v_fma_f32 v171, v163, v209, -v213\n"
"v_add_f32 v244, 1.0, v244\n"
"v_mfma_f32_16x16x32_bf16 v[96:99], a[56:59], a[208:211], v[96:99]\n"
"v_add_f32 v245, 1.0, v245\n"
"v_mfma_f32_16x16x32_bf16 v[100:103], a[56:59], a[212:215], v[100:103]\n"
"v_rcp_f32 v244, v244\n"
"v_mfma_f32_16x16x32_bf16 v[104:107], a[56:59], a[216:219], v[104:107]\n"
"v_rcp_f32 v245, v245\n"
"v_mfma_f32_16x16x32_bf16 v[108:111], a[56:59], a[220:223], v[108:111]\n"
"s_add_u32 s70, s90, 393216\n"
"buffer_load_dwordx4 a[92:95], v152, s[40:43], s70 offen offset:1024 sc1\n"
"v_mul_f32 v170, v170, v244\n"
"v_mfma_f32_16x16x32_bf16 v[124:127], a[60:63], a[220:223], v[124:127]\n"
"v_mul_f32 v171, v171, v245\n"
"v_lshlrev_b32 v192, 16, v180\n"
"v_mfma_f32_16x16x32_bf16 v[120:123], a[60:63], a[216:219], v[120:123]\n"
"v_and_b32 v193, 0xffff0000, v180\n"
"v_fma_f32 v244, v192, v218, v222\n"
"v_mfma_f32_16x16x32_bf16 v[116:119], a[60:63], a[212:215], v[116:119]\n"
"v_fma_f32 v245, v193, v218, v222\n"
"v_mfma_f32_16x16x32_bf16 v[112:115], a[60:63], a[208:211], v[112:115]\n"
"v_exp_f32 v244, v244\n"
"s_waitcnt lgkmcnt(0)\n"
"s_waitcnt vmcnt(23)\n"
"v_mfma_f32_16x16x32_bf16 v[0:3], a[0:3], a[224:227], v[0:3]\n"
"s_add_u32 s70, s90, 0\n"
"ds_read_b64_tr_b16 a[32:33], v144 offset:57344\n"
"buffer_load_dwordx4 a[96:99], v152, s[40:43], s70 offen offset:2048 sc1\n"
"v_mfma_f32_16x16x32_bf16 v[4:7], a[0:3], a[228:231], v[4:7]\n"
"ds_read_b64_tr_b16 a[34:35], v144 offset:57600\n"
"v_exp_f32 v245, v245\n"
"v_mfma_f32_16x16x32_bf16 v[8:11], a[0:3], a[232:235], v[8:11]\n"
"ds_read_b64_tr_b16 a[36:37], v145 offset:57344\n"
"v_fma_f32 v200, v192, v210, -v214\n"
"v_mfma_f32_16x16x32_bf16 v[12:15], a[0:3], a[236:239], v[12:15]\n"
"ds_read_b64_tr_b16 a[38:39], v145 offset:57600\n"
"v_fma_f32 v201, v193, v210, -v214\n"
"v_mfma_f32_16x16x32_bf16 v[28:31], a[4:7], a[236:239], v[28:31]\n"
"ds_read_b64_tr_b16 a[40:41], v144 offset:57856\n"
"v_add_f32 v244, 1.0, v244\n"
"v_mfma_f32_16x16x32_bf16 v[24:27], a[4:7], a[232:235], v[24:27]\n"
"s_add_u32 s70, s90, 131072\n"
"ds_read_b64_tr_b16 a[42:43], v144 offset:58112\n"
"buffer_load_dwordx4 a[100:103], v152, s[40:43], s70 offen offset:2048 sc1\n"
"v_mfma_f32_16x16x32_bf16 v[20:23], a[4:7], a[228:231], v[20:23]\n"
"ds_read_b64_tr_b16 a[44:45], v145 offset:57856\n"
"v_add_f32 v245, 1.0, v245\n"
"v_mfma_f32_16x16x32_bf16 v[16:19], a[4:7], a[224:227], v[16:19]\n"
"ds_read_b64_tr_b16 a[46:47], v145 offset:58112\n"
"v_rcp_f32 v244, v244\n"
"v_mfma_f32_16x16x32_bf16 v[32:35], a[8:11], a[224:227], v[32:35]\n"
"ds_read_b64_tr_b16 a[48:49], v144 offset:58368\n"
"v_rcp_f32 v245, v245\n"
"v_mfma_f32_16x16x32_bf16 v[36:39], a[8:11], a[228:231], v[36:39]\n"
"ds_read_b64_tr_b16 a[50:51], v144 offset:58624\n"
"v_mul_f32 v200, v200, v244\n"
"v_mfma_f32_16x16x32_bf16 v[40:43], a[8:11], a[232:235], v[40:43]\n"
"s_add_u32 s70, s90, 262144\n"
"ds_read_b64_tr_b16 a[52:53], v145 offset:58368\n"
"buffer_load_dwordx4 a[104:107], v152, s[40:43], s70 offen offset:2048 sc1\n"
"v_mfma_f32_16x16x32_bf16 v[44:47], a[8:11], a[236:239], v[44:47]\n"
"ds_read_b64_tr_b16 a[54:55], v145 offset:58624\n"
"v_mul_f32 v201, v201, v245\n"
"v_mfma_f32_16x16x32_bf16 v[60:63], a[12:15], a[236:239], v[60:63]\n"
"ds_read_b64_tr_b16 a[56:57], v144 offset:58880\n"
"v_lshlrev_b32 v194, 16, v181\n"
"v_mfma_f32_16x16x32_bf16 v[56:59], a[12:15], a[232:235], v[56:59]\n"
"ds_read_b64_tr_b16 a[58:59], v144 offset:59136\n"
"v_and_b32 v195, 0xffff0000, v181\n"
"v_mfma_f32_16x16x32_bf16 v[52:55], a[12:15], a[228:231], v[52:55]\n"
"ds_read_b64_tr_b16 a[60:61], v145 offset:58880\n"
"v_fma_f32 v244, v194, v218, v222\n"
"v_mfma_f32_16x16x32_bf16 v[48:51], a[12:15], a[224:227], v[48:51]\n"
"s_add_u32 s70, s90, 393216\n"
"ds_read_b64_tr_b16 a[62:63], v145 offset:59136\n"
"buffer_load_dwordx4 a[108:111], v152, s[40:43], s70 offen offset:2048 sc1\n"
"v_mfma_f32_16x16x32_bf16 v[64:67], a[16:19], a[224:227], v[64:67]\n"
"v_fma_f32 v245, v195, v218, v222\n"
"v_mfma_f32_16x16x32_bf16 v[68:71], a[16:19], a[228:231], v[68:71]\n"
"v_exp_f32 v244, v244\n"
"v_mfma_f32_16x16x32_bf16 v[72:75], a[16:19], a[232:235], v[72:75]\n"
"buffer_load_dword v156, v155, s[48:51], s75 offen offset:0 sc1\n"
"v_exp_f32 v245, v245\n"
"v_mfma_f32_16x16x32_bf16 v[76:79], a[16:19], a[236:239], v[76:79]\n"
"v_fma_f32 v202, v194, v210, -v214\n"
"v_fma_f32 v203, v195, v210, -v214\n"
"v_mfma_f32_16x16x32_bf16 v[92:95], a[20:23], a[236:239], v[92:95]\n"
"s_add_u32 s70, s90, 0\n"
"buffer_load_dwordx4 a[112:115], v152, s[40:43], s70 offen offset:3072 sc1\n"
"v_add_f32 v244, 1.0, v244\n"
"v_mfma_f32_16x16x32_bf16 v[88:91], a[20:23], a[232:235], v[88:91]\n"
"v_add_f32 v245, 1.0, v245\n"
"v_mfma_f32_16x16x32_bf16 v[84:87], a[20:23], a[228:231], v[84:87]\n"
"v_rcp_f32 v244, v244\n"
"v_mfma_f32_16x16x32_bf16 v[80:83], a[20:23], a[224:227], v[80:83]\n"
"v_rcp_f32 v245, v245\n"
"v_mfma_f32_16x16x32_bf16 v[96:99], a[24:27], a[224:227], v[96:99]\n"
"v_mul_f32 v202, v202, v244\n"
"v_mul_f32 v203, v203, v245\n"
"v_mfma_f32_16x16x32_bf16 v[100:103], a[24:27], a[228:231], v[100:103]\n"
"s_add_u32 s70, s90, 131072\n"
"buffer_load_dwordx4 a[116:119], v152, s[40:43], s70 offen offset:3072 sc1\n"
"v_lshlrev_b32 v224, 16, v182\n"
"v_mfma_f32_16x16x32_bf16 v[104:107], a[24:27], a[232:235], v[104:107]\n"
"v_and_b32 v225, 0xffff0000, v182\n"
"v_fma_f32 v244, v224, v219, v223\n"
"v_mfma_f32_16x16x32_bf16 v[108:111], a[24:27], a[236:239], v[108:111]\n"
"v_fma_f32 v245, v225, v219, v223\n"
"v_mfma_f32_16x16x32_bf16 v[124:127], a[28:31], a[236:239], v[124:127]\n"
"v_exp_f32 v244, v244\n"
"v_mfma_f32_16x16x32_bf16 v[120:123], a[28:31], a[232:235], v[120:123]\n"
"v_exp_f32 v245, v245\n"
"v_mfma_f32_16x16x32_bf16 v[116:119], a[28:31], a[228:231], v[116:119]\n"
"s_add_u32 s70, s90, 262144\n"
"buffer_load_dwordx4 a[120:123], v152, s[40:43], s70 offen offset:3072 sc1\n"
"v_fma_f32 v232, v224, v211, -v215\n"
"v_mfma_f32_16x16x32_bf16 v[112:115], a[28:31], a[224:227], v[112:115]\n"
"s_waitcnt lgkmcnt(0)\n"
"s_waitcnt vmcnt(27)\n"
"s_waitcnt vmcnt(16)\n"
"s_barrier\n"
"v_mfma_f32_16x16x32_bf16 v[0:3], a[32:35], a[240:243], v[0:3]\n"
"ds_read_b64_tr_b16 a[0:1], v146 offset:0\n"
"v_fma_f32 v233, v225, v211, -v215\n"
"v_mfma_f32_16x16x32_bf16 v[4:7], a[32:35], a[244:247], v[4:7]\n"
"ds_read_b64_tr_b16 a[2:3], v146 offset:256\n"
"v_add_f32 v244, 1.0, v244\n"
"v_mfma_f32_16x16x32_bf16 v[8:11], a[32:35], a[248:251], v[8:11]\n"
"ds_read_b64_tr_b16 a[4:5], v147 offset:0\n"
"v_add_f32 v245, 1.0, v245\n"
"v_mfma_f32_16x16x32_bf16 v[12:15], a[32:35], a[252:255], v[12:15]\n"
"s_add_u32 s70, s90, 393216\n"
"ds_read_b64_tr_b16 a[6:7], v147 offset:256\n"
"buffer_load_dwordx4 a[124:127], v152, s[40:43], s70 offen offset:3072 sc1\n"
"v_mfma_f32_16x16x32_bf16 v[28:31], a[36:39], a[252:255], v[28:31]\n"
"ds_read_b64_tr_b16 a[8:9], v146 offset:512\n"
"v_rcp_f32 v244, v244\n"
"v_mfma_f32_16x16x32_bf16 v[24:27], a[36:39], a[248:251], v[24:27]\n"
"ds_read_b64_tr_b16 a[10:11], v146 offset:768\n"
"v_rcp_f32 v245, v245\n"
"v_mfma_f32_16x16x32_bf16 v[20:23], a[36:39], a[244:247], v[20:23]\n"
"ds_read_b64_tr_b16 a[12:13], v147 offset:512\n"
"v_mul_f32 v232, v232, v244\n"
"v_mfma_f32_16x16x32_bf16 v[16:19], a[36:39], a[240:243], v[16:19]\n"
"ds_read_b64_tr_b16 a[14:15], v147 offset:768\n"
"v_mul_f32 v233, v233, v245\n"
"v_mfma_f32_16x16x32_bf16 v[32:35], a[40:43], a[240:243], v[32:35]\n"
"ds_read_b64_tr_b16 a[16:17], v146 offset:1024\n"
"v_lshlrev_b32 v226, 16, v183\n"
"v_mfma_f32_16x16x32_bf16 v[36:39], a[40:43], a[244:247], v[36:39]\n"
"s_cmp_le_u32 s83, 3\ns_cselect_b32 s89, 4, 3\ns_lshl_b32 s89, s89, 18\n"
"s_add_u32 m0, s68, 131072\n"
"ds_read_b64_tr_b16 a[18:19], v146 offset:1280\n"
"v_mfma_f32_16x16x32_bf16 v[40:43], a[40:43], a[248:251], v[40:43]\n"
"s_add_u32 s69, s89, 0\n"
"ds_read_b64_tr_b16 a[20:21], v147 offset:1024\n"
"buffer_load_dwordx4 v150, s[36:39], s69 offen offset:0 lds\n"
"v_mfma_f32_16x16x32_bf16 v[44:47], a[40:43], a[252:255], v[44:47]\n"
"ds_read_b64_tr_b16 a[22:23], v147 offset:1280\n"
"v_and_b32 v227, 0xffff0000, v183\n"
"v_mfma_f32_16x16x32_bf16 v[60:63], a[44:47], a[252:255], v[60:63]\n"
"ds_read_b64_tr_b16 a[24:25], v146 offset:1536\n"
"v_fma_f32 v244, v226, v219, v223\n"
"v_mfma_f32_16x16x32_bf16 v[56:59], a[44:47], a[248:251], v[56:59]\n"
"ds_read_b64_tr_b16 a[26:27], v146 offset:1792\n"
"v_fma_f32 v245, v227, v219, v223\n"
"v_mfma_f32_16x16x32_bf16 v[52:55], a[44:47], a[244:247], v[52:55]\n"
"ds_read_b64_tr_b16 a[28:29], v147 offset:1536\n"
"v_exp_f32 v244, v244\n"
"v_mfma_f32_16x16x32_bf16 v[48:51], a[44:47], a[240:243], v[48:51]\n"
"ds_read_b64_tr_b16 a[30:31], v147 offset:1792\n"
"buffer_load_dwordx4 v151, s[36:39], s69 offen offset:1024 lds\n"
"v_mfma_f32_16x16x32_bf16 v[64:67], a[48:51], a[240:243], v[64:67]\n"
"v_exp_f32 v245, v245\n"
"v_mfma_f32_16x16x32_bf16 v[68:71], a[48:51], a[244:247], v[68:71]\n"
"v_fma_f32 v234, v226, v211, -v215\n"
"v_fma_f32 v235, v227, v211, -v215\n"
"v_mfma_f32_16x16x32_bf16 v[72:75], a[48:51], a[248:251], v[72:75]\n"
"v_add_f32 v244, 1.0, v244\n"
"v_add_f32 v245, 1.0, v245\n"
"v_mfma_f32_16x16x32_bf16 v[76:79], a[48:51], a[252:255], v[76:79]\n"
"s_add_u32 m0, s68, 139264\n"
"v_rcp_f32 v244, v244\n"
"v_mfma_f32_16x16x32_bf16 v[92:95], a[52:55], a[252:255], v[92:95]\n"
"s_add_u32 s69, s89, 65536\n"
"buffer_load_dwordx4 v150, s[36:39], s69 offen offset:0 lds\n"
"v_rcp_f32 v245, v245\n"
"v_mfma_f32_16x16x32_bf16 v[88:91], a[52:55], a[248:251], v[88:91]\n"
"v_mul_f32 v234, v234, v244\n"
"v_mul_f32 v235, v235, v245\n"
"v_mfma_f32_16x16x32_bf16 v[84:87], a[52:55], a[244:247], v[84:87]\n"
"v_lshlrev_b32 v132, 16, v184\n"
"v_and_b32 v133, 0xffff0000, v184\n"
"v_mfma_f32_16x16x32_bf16 v[80:83], a[52:55], a[240:243], v[80:83]\n"
"v_fma_f32 v244, v132, v216, v220\n"
"v_fma_f32 v245, v133, v216, v220\n"
"v_mfma_f32_16x16x32_bf16 v[96:99], a[56:59], a[240:243], v[96:99]\n"
"v_exp_f32 v244, v244\n"
"v_mfma_f32_16x16x32_bf16 v[100:103], a[56:59], a[244:247], v[100:103]\n"
"buffer_load_dwordx4 v151, s[36:39], s69 offen offset:1024 lds\n"
"v_exp_f32 v245, v245\n"
"v_mfma_f32_16x16x32_bf16 v[104:107], a[56:59], a[248:251], v[104:107]\n"
"v_fma_f32 v140, v132, v208, -v212\n"
"v_fma_f32 v141, v133, v208, -v212\n"
"v_mfma_f32_16x16x32_bf16 v[108:111], a[56:59], a[252:255], v[108:111]\n"
"v_add_f32 v244, 1.0, v244\n"
"v_add_f32 v245, 1.0, v245\n"
"v_mfma_f32_16x16x32_bf16 v[124:127], a[60:63], a[252:255], v[124:127]\n"
"v_rcp_f32 v244, v244\n"
"v_mfma_f32_16x16x32_bf16 v[120:123], a[60:63], a[248:251], v[120:123]\n"
"s_add_u32 m0, s68, 147456\n"
"v_rcp_f32 v245, v245\n"
"v_mfma_f32_16x16x32_bf16 v[116:119], a[60:63], a[244:247], v[116:119]\n"
"s_add_u32 s69, s89, 131072\n"
"buffer_load_dwordx4 v150, s[36:39], s69 offen offset:0 lds\n"
"v_mul_f32 v140, v140, v244\n"
"v_mfma_f32_16x16x32_bf16 v[112:115], a[60:63], a[240:243], v[112:115]\n"
"v_mul_f32 v141, v141, v245\n"
"s_waitcnt lgkmcnt(0)\n"
"s_waitcnt vmcnt(18)\n"
"v_mfma_f32_16x16x32_bf16 v[0:3], a[0:3], a[64:67], v[0:3]\n"
"ds_read_b64_tr_b16 a[32:33], v146 offset:8192\n"
"v_lshlrev_b32 v134, 16, v185\n"
"v_mfma_f32_16x16x32_bf16 v[4:7], a[0:3], a[68:71], v[4:7]\n"
"ds_read_b64_tr_b16 a[34:35], v146 offset:8448\n"
"v_and_b32 v135, 0xffff0000, v185\n"
"v_mfma_f32_16x16x32_bf16 v[8:11], a[0:3], a[72:75], v[8:11]\n"
"ds_read_b64_tr_b16 a[36:37], v147 offset:8192\n"
"v_fma_f32 v244, v134, v216, v220\n"
"v_mfma_f32_16x16x32_bf16 v[12:15], a[0:3], a[76:79], v[12:15]\n"
"ds_read_b64_tr_b16 a[38:39], v147 offset:8448\n"
"buffer_load_dwordx4 v151, s[36:39], s69 offen offset:1024 lds\n"
"v_mfma_f32_16x16x32_bf16 v[28:31], a[4:7], a[76:79], v[28:31]\n"
"ds_read_b64_tr_b16 a[40:41], v146 offset:8704\n"
"v_fma_f32 v245, v135, v216, v220\n"
"v_mfma_f32_16x16x32_bf16 v[24:27], a[4:7], a[72:75], v[24:27]\n"
"ds_read_b64_tr_b16 a[42:43], v146 offset:8960\n"
"v_exp_f32 v244, v244\n"
"v_mfma_f32_16x16x32_bf16 v[20:23], a[4:7], a[68:71], v[20:23]\n"
"ds_read_b64_tr_b16 a[44:45], v147 offset:8704\n"
"v_exp_f32 v245, v245\n"
"v_mfma_f32_16x16x32_bf16 v[16:19], a[4:7], a[64:67], v[16:19]\n"
"s_add_u32 m0, s68, 155648\n"
"ds_read_b64_tr_b16 a[46:47], v147 offset:8960\n"
"v_fma_f32 v142, v134, v208, -v212\n"
"v_mfma_f32_16x16x32_bf16 v[32:35], a[8:11], a[64:67], v[32:35]\n"
"s_add_u32 s69, s89, 196608\n"
"ds_read_b64_tr_b16 a[48:49], v146 offset:9216\n"
"buffer_load_dwordx4 v150, s[36:39], s69 offen offset:0 lds\n"
"v_mfma_f32_16x16x32_bf16 v[36:39], a[8:11], a[68:71], v[36:39]\n"
"ds_read_b64_tr_b16 a[50:51], v146 offset:9472\n"
"v_fma_f32 v143, v135, v208, -v212\n"
"v_mfma_f32_16x16x32_bf16 v[40:43], a[8:11], a[72:75], v[40:43]\n"
"ds_read_b64_tr_b16 a[52:53], v147 offset:9216\n"
"v_add_f32 v244, 1.0, v244\n"
"v_mfma_f32_16x16x32_bf16 v[44:47], a[8:11], a[76:79], v[44:47]\n"
"ds_read_b64_tr_b16 a[54:55], v147 offset:9472\n"
"v_add_f32 v245, 1.0, v245\n"
"v_mfma_f32_16x16x32_bf16 v[60:63], a[12:15], a[76:79], v[60:63]\n"
"ds_read_b64_tr_b16 a[56:57], v146 offset:9728\n"
"v_rcp_f32 v244, v244\n"
"v_mfma_f32_16x16x32_bf16 v[56:59], a[12:15], a[72:75], v[56:59]\n"
"ds_read_b64_tr_b16 a[58:59], v146 offset:9984\n"
"buffer_load_dwordx4 v151, s[36:39], s69 offen offset:1024 lds\n"
"v_mfma_f32_16x16x32_bf16 v[52:55], a[12:15], a[68:71], v[52:55]\n"
"ds_read_b64_tr_b16 a[60:61], v147 offset:9728\n"
"v_rcp_f32 v245, v245\n"
"v_mfma_f32_16x16x32_bf16 v[48:51], a[12:15], a[64:67], v[48:51]\n"
"ds_read_b64_tr_b16 a[62:63], v147 offset:9984\n"
"v_mul_f32 v142, v142, v244\n"
"v_mfma_f32_16x16x32_bf16 v[64:67], a[16:19], a[64:67], v[64:67]\n"
"v_mul_f32 v143, v143, v245\n"
"v_lshlrev_b32 v164, 16, v186\n"
"v_mfma_f32_16x16x32_bf16 v[68:71], a[16:19], a[68:71], v[68:71]\n"
"v_and_b32 v165, 0xffff0000, v186\n"
"v_fma_f32 v244, v164, v217, v221\n"
"v_mfma_f32_16x16x32_bf16 v[72:75], a[16:19], a[72:75], v[72:75]\n"
"s_waitcnt vmcnt(12)\n"
"v_cmp_ne_u32 vcc, s77, v156\n"
"s_lshl_b32 s24, 1, s83\ns_andn2_b32 s24, 255, s24\ns_mov_b32 s25, 0\n"
"s_nop 0\n"
"s_and_b64 vcc, vcc, s[24:25]\n"
"s_nop 4\n"
"s_cbranch_vccnz LSO4_%=\n"
"LBO4_%=:\n"
"v_mfma_f32_16x16x32_bf16 v[76:79], a[16:19], a[76:79], v[76:79]\n"
"s_cmp_le_u32 s83, 3\ns_cselect_b32 s90, 4, 3\ns_lshl_b32 s90, s90, 12\n"
"s_add_u32 s70, s90, 0\n"
"buffer_load_dwordx4 a[128:131], v152, s[40:43], s70 offen offset:0 sc1\n"
"v_mfma_f32_16x16x32_bf16 v[92:95], a[20:23], a[76:79], v[92:95]\n"
"v_fma_f32 v245, v165, v217, v221\n"
"v_mfma_f32_16x16x32_bf16 v[88:91], a[20:23], a[72:75], v[88:91]\n"
"v_exp_f32 v244, v244\n"
"v_mfma_f32_16x16x32_bf16 v[84:87], a[20:23], a[68:71], v[84:87]\n"
"v_exp_f32 v245, v245\n"
"v_mfma_f32_16x16x32_bf16 v[80:83], a[20:23], a[64:67], v[80:83]\n"
"v_fma_f32 v172, v164, v209, -v213\n"
"v_fma_f32 v173, v165, v209, -v213\n"
"v_mfma_f32_16x16x32_bf16 v[96:99], a[24:27], a[64:67], v[96:99]\n"
"s_add_u32 s70, s90, 131072\n"
"buffer_load_dwordx4 a[132:135], v152, s[40:43], s70 offen offset:0 sc1\n"
"v_add_f32 v244, 1.0, v244\n"
"v_mfma_f32_16x16x32_bf16 v[100:103], a[24:27], a[68:71], v[100:103]\n"
"v_add_f32 v245, 1.0, v245\n"
"v_mfma_f32_16x16x32_bf16 v[104:107], a[24:27], a[72:75], v[104:107]\n"
"v_rcp_f32 v244, v244\n"
"v_mfma_f32_16x16x32_bf16 v[108:111], a[24:27], a[76:79], v[108:111]\n"
"v_rcp_f32 v245, v245\n"
"v_mfma_f32_16x16x32_bf16 v[124:127], a[28:31], a[76:79], v[124:127]\n"
"v_mul_f32 v172, v172, v244\n"
"v_mul_f32 v173, v173, v245\n"
"v_mfma_f32_16x16x32_bf16 v[120:123], a[28:31], a[72:75], v[120:123]\n"
"s_add_u32 s70, s90, 262144\n"
"buffer_load_dwordx4 a[136:139], v152, s[40:43], s70 offen offset:0 sc1\n"
"v_lshlrev_b32 v166, 16, v187\n"
"v_mfma_f32_16x16x32_bf16 v[116:119], a[28:31], a[68:71], v[116:119]\n"
"v_and_b32 v167, 0xffff0000, v187\n"
"v_fma_f32 v244, v166, v217, v221\n"
"v_mfma_f32_16x16x32_bf16 v[112:115], a[28:31], a[64:67], v[112:115]\n"
"v_fma_f32 v245, v167, v217, v221\n"
"s_waitcnt lgkmcnt(0)\n"
"s_waitcnt vmcnt(20)\n"
"v_mfma_f32_16x16x32_bf16 v[0:3], a[32:35], a[80:83], v[0:3]\n"
"ds_read_b64_tr_b16 a[0:1], v146 offset:16384\n"
"v_exp_f32 v244, v244\n"
"v_mfma_f32_16x16x32_bf16 v[4:7], a[32:35], a[84:87], v[4:7]\n"
"ds_read_b64_tr_b16 a[2:3], v146 offset:16640\n"
"v_exp_f32 v245, v245\n"
"v_mfma_f32_16x16x32_bf16 v[8:11], a[32:35], a[88:91], v[8:11]\n"
"s_add_u32 s70, s90, 393216\n"
"ds_read_b64_tr_b16 a[4:5], v147 offset:16384\n"
"buffer_load_dwordx4 a[140:143], v152, s[40:43], s70 offen offset:0 sc1\n"
"v_mfma_f32_16x16x32_bf16 v[12:15], a[32:35], a[92:95], v[12:15]\n"
"ds_read_b64_tr_b16 a[6:7], v147 offset:16640\n"
"v_fma_f32 v174, v166, v209, -v213\n"
"v_mfma_f32_16x16x32_bf16 v[28:31], a[36:39], a[92:95], v[28:31]\n"
"ds_read_b64_tr_b16 a[8:9], v146 offset:16896\n"
"v_fma_f32 v175, v167, v209, -v213\n"
"v_mfma_f32_16x16x32_bf16 v[24:27], a[36:39], a[88:91], v[24:27]\n"
"ds_read_b64_tr_b16 a[10:11], v146 offset:17152\n"
"v_add_f32 v244, 1.0, v244\n"
"v_mfma_f32_16x16x32_bf16 v[20:23], a[36:39], a[84:87], v[20:23]\n"
"ds_read_b64_tr_b16 a[12:13], v147 offset:16896\n"
"v_add_f32 v245, 1.0, v245\n"
"v_mfma_f32_16x16x32_bf16 v[16:19], a[36:39], a[80:83], v[16:19]\n"
"s_add_u32 s70, s90, 0\n"
"ds_read_b64_tr_b16 a[14:15], v147 offset:17152\n"
"buffer_load_dwordx4 a[144:147], v152, s[40:43], s70 offen offset:1024 sc1\n"
"v_mfma_f32_16x16x32_bf16 v[32:35], a[40:43], a[80:83], v[32:35]\n"
"ds_read_b64_tr_b16 a[16:17], v146 offset:17408\n"
"v_rcp_f32 v244, v244\n"
"v_mfma_f32_16x16x32_bf16 v[36:39], a[40:43], a[84:87], v[36:39]\n"
"ds_read_b64_tr_b16 a[18:19], v146 offset:17664\n"
"v_rcp_f32 v245, v245\n"
"v_mfma_f32_16x16x32_bf16 v[40:43], a[40:43], a[88:91], v[40:43]\n"
"ds_read_b64_tr_b16 a[20:21], v147 offset:17408\n"
"v_mul_f32 v174, v174, v244\n"
"v_mfma_f32_16x16x32_bf16 v[44:47], a[40:43], a[92:95], v[44:47]\n"
"ds_read_b64_tr_b16 a[22:23], v147 offset:17664\n"
"v_mul_f32 v175, v175, v245\n"
"v_mfma_f32_16x16x32_bf16 v[60:63], a[44:47], a[92:95], v[60:63]\n"
"s_add_u32 s70, s90, 131072\n"
"ds_read_b64_tr_b16 a[24:25], v146 offset:17920\n"
"buffer_load_dwordx4 a[148:151], v152, s[40:43], s70 offen offset:1024 sc1\n"
"v_mfma_f32_16x16x32_bf16 v[56:59], a[44:47], a[88:91], v[56:59]\n"
"ds_read_b64_tr_b16 a[26:27], v146 offset:18176\n"
"v_lshlrev_b32 v196, 16, v188\n"
"v_mfma_f32_16x16x32_bf16 v[52:55], a[44:47], a[84:87], v[52:55]\n"
"ds_read_b64_tr_b16 a[28:29], v147 offset:17920\n"
"v_and_b32 v197, 0xffff0000, v188\n"
"v_mfma_f32_16x16x32_bf16 v[48:51], a[44:47], a[80:83], v[48:51]\n"
"ds_read_b64_tr_b16 a[30:31], v147 offset:18176\n"
"v_fma_f32 v244, v196, v218, v222\n"
"v_mfma_f32_16x16x32_bf16 v[64:67], a[48:51], a[80:83], v[64:67]\n"
"v_fma_f32 v245, v197, v218, v222\n"
"v_mfma_f32_16x16x32_bf16 v[68:71], a[48:51], a[84:87], v[68:71]\n"
"s_add_u32 s70, s90, 262144\n"
"buffer_load_dwordx4 a[152:155], v152, s[40:43], s70 offen offset:1024 sc1\n"
"v_exp_f32 v244, v244\n"
"v_mfma_f32_16x16x32_bf16 v[72:75], a[48:51], a[88:91], v[72:75]\n"
"v_exp_f32 v245, v245\n"
"v_mfma_f32_16x16x32_bf16 v[76:79], a[48:51], a[92:95], v[76:79]\n"
"v_fma_f32 v204, v196, v210, -v214\n"
"v_fma_f32 v205, v197, v210, -v214\n"
"v_mfma_f32_16x16x32_bf16 v[92:95], a[52:55], a[92:95], v[92:95]\n"
"v_add_f32 v244, 1.0, v244\n"
"v_add_f32 v245, 1.0, v245\n"
"v_mfma_f32_16x16x32_bf16 v[88:91], a[52:55], a[88:91], v[88:91]\n"
"v_rcp_f32 v244, v244\n"
"v_mfma_f32_16x16x32_bf16 v[84:87], a[52:55], a[84:87], v[84:87]\n"
"s_add_u32 s70, s90, 393216\n"
"buffer_load_dwordx4 a[156:159], v152, s[40:43], s70 offen offset:1024 sc1\n"
"v_rcp_f32 v245, v245\n"
"v_mfma_f32_16x16x32_bf16 v[80:83], a[52:55], a[80:83], v[80:83]\n"
"v_mul_f32 v204, v204, v244\n"
"v_mul_f32 v205, v205, v245\n"
"v_mfma_f32_16x16x32_bf16 v[96:99], a[56:59], a[80:83], v[96:99]\n"
"v_lshlrev_b32 v198, 16, v189\n"
"v_and_b32 v199, 0xffff0000, v189\n"
"v_mfma_f32_16x16x32_bf16 v[100:103], a[56:59], a[84:87], v[100:103]\n"
"v_fma_f32 v244, v198, v218, v222\n"
"v_fma_f32 v245, v199, v218, v222\n"
"v_mfma_f32_16x16x32_bf16 v[104:107], a[56:59], a[88:91], v[104:107]\n"
"v_exp_f32 v244, v244\n"
"v_mfma_f32_16x16x32_bf16 v[108:111], a[56:59], a[92:95], v[108:111]\n"
"s_add_u32 s70, s90, 0\n"
"buffer_load_dwordx4 a[160:163], v152, s[40:43], s70 offen offset:2048 sc1\n"
"v_exp_f32 v245, v245\n"
"v_mfma_f32_16x16x32_bf16 v[124:127], a[60:63], a[92:95], v[124:127]\n"
"v_fma_f32 v206, v198, v210, -v214\n"
"v_fma_f32 v207, v199, v210, -v214\n"
"v_mfma_f32_16x16x32_bf16 v[120:123], a[60:63], a[88:91], v[120:123]\n"
"v_add_f32 v244, 1.0, v244\n"
"v_add_f32 v245, 1.0, v245\n"
"v_mfma_f32_16x16x32_bf16 v[116:119], a[60:63], a[84:87], v[116:119]\n"
"v_rcp_f32 v244, v244\n"
"v_mfma_f32_16x16x32_bf16 v[112:115], a[60:63], a[80:83], v[112:115]\n"
"v_rcp_f32 v245, v245\n"
"s_waitcnt lgkmcnt(0)\n"
"s_waitcnt vmcnt(22)\n"
"v_mfma_f32_16x16x32_bf16 v[0:3], a[0:3], a[96:99], v[0:3]\n"
"s_add_u32 s70, s90, 131072\n"
"ds_read_b64_tr_b16 a[32:33], v146 offset:24576\n"
"buffer_load_dwordx4 a[164:167], v152, s[40:43], s70 offen offset:2048 sc1\n"
"v_mfma_f32_16x16x32_bf16 v[4:7], a[0:3], a[100:103], v[4:7]\n"
"ds_read_b64_tr_b16 a[34:35], v146 offset:24832\n"
"v_mul_f32 v206, v206, v244\n"
"v_mfma_f32_16x16x32_bf16 v[8:11], a[0:3], a[104:107], v[8:11]\n"
"ds_read_b64_tr_b16 a[36:37], v147 offset:24576\n"
"v_mul_f32 v207, v207, v245\n"
"v_mfma_f32_16x16x32_bf16 v[12:15], a[0:3], a[108:111], v[12:15]\n"
"ds_read_b64_tr_b16 a[38:39], v147 offset:24832\n"
"v_lshlrev_b32 v228, 16, v190\n"
"v_mfma_f32_16x16x32_bf16 v[28:31], a[4:7], a[108:111], v[28:31]\n"
"ds_read_b64_tr_b16 a[40:41], v146 offset:25088\n"
"v_and_b32 v229, 0xffff0000, v190\n"
"v_mfma_f32_16x16x32_bf16 v[24:27], a[4:7], a[104:107], v[24:27]\n"
"s_add_u32 s70, s90, 262144\n"
"ds_read_b64_tr_b16 a[42:43], v146 offset:25344\n"
"buffer_load_dwordx4 a[168:171], v152, s[40:43], s70 offen offset:2048 sc1\n"
"v_mfma_f32_16x16x32_bf16 v[20:23], a[4:7], a[100:103], v[20:23]\n"
"ds_read_b64_tr_b16 a[44:45], v147 offset:25088\n"
"v_fma_f32 v244, v228, v219, v223\n"
"v_mfma_f32_16x16x32_bf16 v[16:19], a[4:7], a[96:99], v[16:19]\n"
"ds_read_b64_tr_b16 a[46:47], v147 offset:25344\n"
"v_fma_f32 v245, v229, v219, v223\n"
"v_mfma_f32_16x16x32_bf16 v[32:35], a[8:11], a[96:99], v[32:35]\n"
"ds_read_b64_tr_b16 a[48:49], v146 offset:25600\n"
"v_exp_f32 v244, v244\n"
"v_mfma_f32_16x16x32_bf16 v[36:39], a[8:11], a[100:103], v[36:39]\n"
"ds_read_b64_tr_b16 a[50:51], v146 offset:25856\n"
"v_exp_f32 v245, v245\n"
"v_mfma_f32_16x16x32_bf16 v[40:43], a[8:11], a[104:107], v[40:43]\n"
"s_add_u32 s70, s90, 393216\n"
"ds_read_b64_tr_b16 a[52:53], v147 offset:25600\n"
"buffer_load_dwordx4 a[172:175], v152, s[40:43], s70 offen offset:2048 sc1\n"
"v_mfma_f32_16x16x32_bf16 v[44:47], a[8:11], a[108:111], v[44:47]\n"
"ds_read_b64_tr_b16 a[54:55], v147 offset:25856\n"
"v_fma_f32 v236, v228, v211, -v215\n"
"v_mfma_f32_16x16x32_bf16 v[60:63], a[12:15], a[108:111], v[60:63]\n"
"ds_read_b64_tr_b16 a[56:57], v146 offset:26112\n"
"v_fma_f32 v237, v229, v211, -v215\n"
"v_mfma_f32_16x16x32_bf16 v[56:59], a[12:15], a[104:107], v[56:59]\n"
"ds_read_b64_tr_b16 a[58:59], v146 offset:26368\n"
"v_add_f32 v244, 1.0, v244\n"
"v_mfma_f32_16x16x32_bf16 v[52:55], a[12:15], a[100:103], v[52:55]\n"
"ds_read_b64_tr_b16 a[60:61], v147 offset:26112\n"
"v_add_f32 v245, 1.0, v245\n"
"v_mfma_f32_16x16x32_bf16 v[48:51], a[12:15], a[96:99], v[48:51]\n"
"s_add_u32 s70, s90, 0\n"
"ds_read_b64_tr_b16 a[62:63], v147 offset:26368\n"
"buffer_load_dwordx4 a[176:179], v152, s[40:43], s70 offen offset:3072 sc1\n"
"v_mfma_f32_16x16x32_bf16 v[64:67], a[16:19], a[96:99], v[64:67]\n"
"v_rcp_f32 v244, v244\n"
"v_mfma_f32_16x16x32_bf16 v[68:71], a[16:19], a[100:103], v[68:71]\n"
"v_rcp_f32 v245, v245\n"
"v_mfma_f32_16x16x32_bf16 v[72:75], a[16:19], a[104:107], v[72:75]\n"
"v_mul_f32 v236, v236, v244\n"
"v_mul_f32 v237, v237, v245\n"
"v_mfma_f32_16x16x32_bf16 v[76:79], a[16:19], a[108:111], v[76:79]\n"
"v_lshlrev_b32 v230, 16, v191\n"
"v_and_b32 v231, 0xffff0000, v191\n"
"v_mfma_f32_16x16x32_bf16 v[92:95], a[20:23], a[108:111], v[92:95]\n"
"s_add_u32 s70, s90, 131072\n"
"buffer_load_dwordx4 a[180:183], v152, s[40:43], s70 offen offset:3072 sc1\n"
"v_fma_f32 v244, v230, v219, v223\n"
"v_mfma_f32_16x16x32_bf16 v[88:91], a[20:23], a[104:107], v[88:91]\n"
"v_fma_f32 v245, v231, v219, v223\n"
"v_mfma_f32_16x16x32_bf16 v[84:87], a[20:23], a[100:103], v[84:87]\n"
"v_exp_f32 v244, v244\n"
"v_mfma_f32_16x16x32_bf16 v[80:83], a[20:23], a[96:99], v[80:83]\n"
"v_exp_f32 v245, v245\n"
"v_mfma_f32_16x16x32_bf16 v[96:99], a[24:27], a[96:99], v[96:99]\n"
"v_fma_f32 v238, v230, v211, -v215\n"
"v_fma_f32 v239, v231, v211, -v215\n"
"v_mfma_f32_16x16x32_bf16 v[100:103], a[24:27], a[100:103], v[100:103]\n"
"s_add_u32 s70, s90, 262144\n"
"buffer_load_dwordx4 a[184:187], v152, s[40:43], s70 offen offset:3072 sc1\n"
"v_add_f32 v244, 1.0, v244\n"
"v_mfma_f32_16x16x32_bf16 v[104:107], a[24:27], a[104:107], v[104:107]\n"
"v_add_f32 v245, 1.0, v245\n"
"v_mfma_f32_16x16x32_bf16 v[108:111], a[24:27], a[108:111], v[108:111]\n"
"v_rcp_f32 v244, v244\n"
"v_mfma_f32_16x16x32_bf16 v[124:127], a[28:31], a[108:111], v[124:127]\n"
"v_rcp_f32 v245, v245\n"
"v_mfma_f32_16x16x32_bf16 v[120:123], a[28:31], a[104:107], v[120:123]\n"
"v_mul_f32 v238, v238, v244\n"
"v_mul_f32 v239, v239, v245\n"
"v_mfma_f32_16x16x32_bf16 v[116:119], a[28:31], a[100:103], v[116:119]\n"
"s_add_u32 s70, s90, 393216\n"
"s_and_b32 s85, %[key], 2047\n"
"buffer_load_dwordx4 a[188:191], v152, s[40:43], s70 offen offset:3072 sc1\n"
"v_mfma_f32_16x16x32_bf16 v[112:115], a[28:31], a[96:99], v[112:115]\n"
"s_waitcnt lgkmcnt(0)\n"
"s_waitcnt vmcnt(24)\n"
"s_waitcnt vmcnt(16)\n"
"s_barrier\n"
"v_mfma_f32_16x16x32_bf16 v[0:3], a[32:35], a[112:115], v[0:3]\n"
"s_sub_u32 s85, 2047, s85\n"
"s_lshr_b32 s71, s85, 6\n"
"ds_read_b64_tr_b16 a[0:1], v148 offset:0\n"
"v_mfma_f32_16x16x32_bf16 v[4:7], a[32:35], a[116:119], v[4:7]\n"
"s_cmp_eq_u32 s71, s84\ns_cselect_b32 s22, 0xfffffff, 0\ns_cselect_b32 s95, 0x10001, 0\n"
"s_mov_b32 s23, 0\n"
"ds_read_b64_tr_b16 a[2:3], v148 offset:256\n"
"v_mfma_f32_16x16x32_bf16 v[8:11], a[32:35], a[120:123], v[8:11]\n"
"s_and_b32 s71, s85, 15\n"
"s_lshl_b32 s95, s95, s71\n"
"ds_read_b64_tr_b16 a[4:5], v149 offset:0\n"
"v_mfma_f32_16x16x32_bf16 v[12:15], a[32:35], a[124:127], v[12:15]\n"
"s_not_b32 s95, s95\n"
"s_bfe_u32 s94, s85, 0x20004\n"
"ds_read_b64_tr_b16 a[6:7], v149 offset:256\n"
"v_mfma_f32_16x16x32_bf16 v[28:31], a[36:39], a[124:127], v[28:31]\n"
"s_cmp_le_u32 s83, 4\ns_cselect_b32 s89, 5, 4\ns_lshl_b32 s89, s89, 18\n"
"s_add_u32 m0, s68, 98304\n"
"ds_read_b64_tr_b16 a[8:9], v148 offset:512\n"
"v_mfma_f32_16x16x32_bf16 v[24:27], a[36:39], a[120:123], v[24:27]\n"
"s_add_u32 s69, s89, 0\n"
"ds_read_b64_tr_b16 a[10:11], v148 offset:768\n"
"buffer_load_dwordx4 v150, s[36:39], s69 offen offset:0 lds\n"
"v_mfma_f32_16x16x32_bf16 v[20:23], a[36:39], a[116:119], v[20:23]\n"
"s_mov_b64 s[64:65], s[48:49]\n"
"s_add_u32 s64, s64, 704512\ns_addc_u32 s65, s65, 0\n"
"ds_read_b64_tr_b16 a[12:13], v149 offset:512\n"
"v_mfma_f32_16x16x32_bf16 v[16:19], a[36:39], a[112:115], v[16:19]\n"
"s_mov_b32 s66, 131072\n"
"s_mov_b32 s67, 0x00020000\n"
"ds_read_b64_tr_b16 a[14:15], v149 offset:768\n"
"v_mfma_f32_16x16x32_bf16 v[32:35], a[40:43], a[112:115], v[32:35]\n"
"s_ashr_i32 s71, %[key], 31\n"
"s_not_b32 s71, s71\n"
"ds_read_b64_tr_b16 a[16:17], v148 offset:1024\n"
"v_mfma_f32_16x16x32_bf16 v[36:39], a[40:43], a[116:119], v[36:39]\n"
"s_or_b32 s71, s71, 0x80000000\n"
"s_xor_b32 s71, %[key], s71\n"
"ds_read_b64_tr_b16 a[18:19], v148 offset:1280\n"
"v_mfma_f32_16x16x32_bf16 v[40:43], a[40:43], a[120:123], v[40:43]\n"
"s_and_b32 s71, s71, 0xffff0000\n"
"ds_read_b64_tr_b16 a[20:21], v149 offset:1024\n"
"buffer_load_dwordx4 v151, s[36:39], s69 offen offset:1024 lds\n"
"v_mfma_f32_16x16x32_bf16 v[44:47], a[40:43], a[124:127], v[44:47]\n"
"ds_read_b64_tr_b16 a[22:23], v149 offset:1280\n"
"v_mov_b32 v246, 0xbfb8aa3b\n"
"v_mfma_f32_16x16x32_bf16 v[60:63], a[44:47], a[124:127], v[60:63]\n"
"ds_read_b64_tr_b16 a[24:25], v148 offset:1536\n"
"v_mul_f32 v246, s71, v246\n"
"v_mfma_f32_16x16x32_bf16 v[56:59], a[44:47], a[120:123], v[56:59]\n"
"ds_read_b64_tr_b16 a[26:27], v148 offset:1792\n"
"v_exp_f32 v246, v246\n"
"v_mfma_f32_16x16x32_bf16 v[52:55], a[44:47], a[116:119], v[52:55]\n"
"s_add_u32 m0, s68, 106496\n"
"ds_read_b64_tr_b16 a[28:29], v149 offset:1536\n"
"v_add_f32 v246, 1.0, v246\n"
"v_mfma_f32_16x16x32_bf16 v[48:51], a[44:47], a[112:115], v[48:51]\n"
"s_add_u32 s69, s89, 65536\n"
"ds_read_b64_tr_b16 a[30:31], v149 offset:1792\n"
"buffer_load_dwordx4 v150, s[36:39], s69 offen offset:0 lds\n"
"v_mfma_f32_16x16x32_bf16 v[64:67], a[48:51], a[112:115], v[64:67]\n"
"v_rcp_f32 v246, v246\n"
"v_mfma_f32_16x16x32_bf16 v[68:71], a[48:51], a[116:119], v[68:71]\n"
"v_cvt_pk_bf16_f32 v246, v246, v246\n"
"v_lshlrev_b32 v246, 16, v246\n"
"v_mfma_f32_16x16x32_bf16 v[72:75], a[48:51], a[120:123], v[72:75]\n"
"v_bfe_u32 v247, v158, 2, 1\n"
"v_lshlrev_b32 v247, 4, v247\n"
"v_mfma_f32_16x16x32_bf16 v[76:79], a[48:51], a[124:127], v[76:79]\n"
"v_lshrrev_b32 v248, 4, v158\n"
"v_lshl_add_u32 v247, v248, 2, v247\n"
"v_mfma_f32_16x16x32_bf16 v[92:95], a[52:55], a[124:127], v[92:95]\n"
"buffer_load_dwordx4 v151, s[36:39], s69 offen offset:1024 lds\n"
"v_and_b32 v248, 3, v158\n"
"v_mfma_f32_16x16x32_bf16 v[88:91], a[52:55], a[120:123], v[88:91]\n"
"v_add_u32 v247, v247, v248\n"
"v_mfma_f32_16x16x32_bf16 v[84:87], a[52:55], a[116:119], v[84:87]\n"
"s_lshl_b32 s71, s83, 2\n"
"s_add_u32 s71, s71, s82\n"
"s_lshl_b32 s72, s71, 5\n"
"v_mfma_f32_16x16x32_bf16 v[80:83], a[52:55], a[112:115], v[80:83]\n"
"v_add_u32 v247, s72, v247\n"
"v_lshlrev_b32 v250, 2, v247\n"
"v_mfma_f32_16x16x32_bf16 v[96:99], a[56:59], a[112:115], v[96:99]\n"
"s_add_u32 m0, s68, 114688\n"
"v_lshlrev_b32 v251, 1, v247\n"
"v_readfirstlane_b32 s88, v246\n"
"v_mfma_f32_16x16x32_bf16 v[100:103], a[56:59], a[116:119], v[100:103]\n"
"s_add_u32 s69, s89, 131072\n"
"buffer_load_dwordx4 v150, s[36:39], s69 offen offset:0 lds\n"
"v_lshlrev_b32 v249, 9, v158\n"
"v_mfma_f32_16x16x32_bf16 v[104:107], a[56:59], a[120:123], v[104:107]\n"
"s_lshl_b32 s72, s71, 4\n"
"v_add_u32 v249, s72, v249\n"
"v_mov_b32 v248, s78\n"
"v_mfma_f32_16x16x32_bf16 v[108:111], a[56:59], a[124:127], v[108:111]\n"
"v_lshlrev_b32 v248, 6, v248\n"
"v_mfma_f32_16x16x32_bf16 v[124:127], a[60:63], a[124:127], v[124:127]\n"
"s_lshl_b32 s72, s79, 4\n"
"v_add_u32 v248, s72, v248\n"
"v_cndmask_b32 v252, v248, v249, s[22:23]\n"
"v_mfma_f32_16x16x32_bf16 v[120:123], a[60:63], a[120:123], v[120:123]\n"
"s_lshl_b32 s85, s85, 11\n"
"s_mov_b32 s81, 688128\n"
"s_andn2_b32 s71, s78, 3\n"
"v_mfma_f32_16x16x32_bf16 v[116:119], a[60:63], a[116:119], v[116:119]\n"
"s_lshl_b32 s71, s71, 16\n"
"s_lshl_b32 s72, s79, 14\n"
"buffer_load_dwordx4 v151, s[36:39], s69 offen offset:1024 lds\n"
"v_mfma_f32_16x16x32_bf16 v[112:115], a[60:63], a[112:115], v[112:115]\n"
"s_add_u32 s71, s71, s72\n"
"s_waitcnt lgkmcnt(0)\n"
"s_waitcnt vmcnt(18)\n"
"v_mfma_f32_16x16x32_bf16 v[0:3], a[0:3], a[128:131], v[0:3]\n"
"s_add_u32 s71, s71, 15728640\n"
"s_add_u32 s60, s48, s71\ns_addc_u32 s61, s49, 0\n"
"ds_read_b64_tr_b16 a[32:33], v148 offset:8192\n"
"v_mfma_f32_16x16x32_bf16 v[4:7], a[0:3], a[132:135], v[4:7]\n"
"s_mov_b32 s62, 262144\n"
"s_mov_b32 s63, 0x00020000\n"
"ds_read_b64_tr_b16 a[34:35], v148 offset:8448\n"
"v_mfma_f32_16x16x32_bf16 v[8:11], a[0:3], a[136:139], v[8:11]\n"
"s_add_u32 m0, s68, 122880\n"
"s_lshl_b32 s29, s82, 16\n"
"ds_read_b64_tr_b16 a[36:37], v149 offset:8192\n"
"v_mfma_f32_16x16x32_bf16 v[12:15], a[0:3], a[140:143], v[12:15]\n"
"s_add_u32 s69, s89, 196608\n"
"ds_read_b64_tr_b16 a[38:39], v149 offset:8448\n"
"buffer_load_dwordx4 v150, s[36:39], s69 offen offset:0 lds\n"
"v_mfma_f32_16x16x32_bf16 v[28:31], a[4:7], a[140:143], v[28:31]\n"
"ds_read_b64_tr_b16 a[40:41], v148 offset:8704\n"
"buffer_load_dword v253, v252, s[48:51], s81 offen offset:0 sc1\n"
"v_mfma_f32_16x16x32_bf16 v[24:27], a[4:7], a[136:139], v[24:27]\n"
"ds_read_b64_tr_b16 a[42:43], v148 offset:8960\n"
"v_mfma_f32_16x16x32_bf16 v[20:23], a[4:7], a[132:135], v[20:23]\n"
"ds_read_b64_tr_b16 a[44:45], v149 offset:8704\n"
"v_mfma_f32_16x16x32_bf16 v[16:19], a[4:7], a[128:131], v[16:19]\n"
"ds_read_b64_tr_b16 a[46:47], v149 offset:8960\n"
"v_mfma_f32_16x16x32_bf16 v[32:35], a[8:11], a[128:131], v[32:35]\n"
"ds_read_b64_tr_b16 a[48:49], v148 offset:9216\n"
"buffer_load_dwordx4 v151, s[36:39], s69 offen offset:1024 lds\n"
"v_mfma_f32_16x16x32_bf16 v[36:39], a[8:11], a[132:135], v[36:39]\n"
"ds_read_b64_tr_b16 a[50:51], v148 offset:9472\n"
"v_mfma_f32_16x16x32_bf16 v[40:43], a[8:11], a[136:139], v[40:43]\n"
"ds_read_b64_tr_b16 a[52:53], v149 offset:9216\n"
"v_mfma_f32_16x16x32_bf16 v[44:47], a[8:11], a[140:143], v[44:47]\n"
"ds_read_b64_tr_b16 a[54:55], v149 offset:9472\n"
"v_mfma_f32_16x16x32_bf16 v[60:63], a[12:15], a[140:143], v[60:63]\n"
"ds_read_b64_tr_b16 a[56:57], v148 offset:9728\n"
"v_mfma_f32_16x16x32_bf16 v[56:59], a[12:15], a[136:139], v[56:59]\n"
"s_cmp_le_u32 s83, 4\ns_cselect_b32 s90, 5, 4\ns_lshl_b32 s90, s90, 12\n"
"s_add_u32 s70, s90, 0\n"
"ds_read_b64_tr_b16 a[58:59], v148 offset:9984\n"
"v_mfma_f32_16x16x32_bf16 v[52:55], a[12:15], a[132:135], v[52:55]\n"
"ds_read_b64_tr_b16 a[60:61], v149 offset:9728\n"
"buffer_load_dwordx4 a[192:195], v152, s[40:43], s70 offen offset:0 sc1\n"
"v_mfma_f32_16x16x32_bf16 v[48:51], a[12:15], a[128:131], v[48:51]\n"
"ds_read_b64_tr_b16 a[62:63], v149 offset:9984\n"
"v_mfma_f32_16x16x32_bf16 v[64:67], a[16:19], a[128:131], v[64:67]\n"
"v_mfma_f32_16x16x32_bf16 v[68:71], a[16:19], a[132:135], v[68:71]\n"
"v_mfma_f32_16x16x32_bf16 v[72:75], a[16:19], a[136:139], v[72:75]\n"
"v_mfma_f32_16x16x32_bf16 v[76:79], a[16:19], a[140:143], v[76:79]\n"
"s_add_u32 s70, s90, 131072\n"
"buffer_load_dwordx4 a[196:199], v152, s[40:43], s70 offen offset:0 sc1\n"
"v_mfma_f32_16x16x32_bf16 v[92:95], a[20:23], a[140:143], v[92:95]\n"
"v_mfma_f32_16x16x32_bf16 v[88:91], a[20:23], a[136:139], v[88:91]\n"
"v_mfma_f32_16x16x32_bf16 v[84:87], a[20:23], a[132:135], v[84:87]\n"
"v_mfma_f32_16x16x32_bf16 v[80:83], a[20:23], a[128:131], v[80:83]\n"
"v_mfma_f32_16x16x32_bf16 v[96:99], a[24:27], a[128:131], v[96:99]\n"
"s_add_u32 s70, s90, 262144\n"
"buffer_load_dwordx4 a[200:203], v152, s[40:43], s70 offen offset:0 sc1\n"
"v_mfma_f32_16x16x32_bf16 v[100:103], a[24:27], a[132:135], v[100:103]\n"
"v_mfma_f32_16x16x32_bf16 v[104:107], a[24:27], a[136:139], v[104:107]\n"
"v_mfma_f32_16x16x32_bf16 v[108:111], a[24:27], a[140:143], v[108:111]\n"
"v_mfma_f32_16x16x32_bf16 v[124:127], a[28:31], a[140:143], v[124:127]\n"
"v_mfma_f32_16x16x32_bf16 v[120:123], a[28:31], a[136:139], v[120:123]\n"
"s_add_u32 s70, s90, 393216\n"
"buffer_load_dwordx4 a[204:207], v152, s[40:43], s70 offen offset:0 sc1\n"
"v_mfma_f32_16x16x32_bf16 v[116:119], a[28:31], a[132:135], v[116:119]\n"
"v_mfma_f32_16x16x32_bf16 v[112:115], a[28:31], a[128:131], v[112:115]\n"
"s_waitcnt lgkmcnt(0)\n"
"s_waitcnt vmcnt(21)\n"
"v_mfma_f32_16x16x32_bf16 v[0:3], a[32:35], a[144:147], v[0:3]\n"
"ds_read_b64_tr_b16 a[0:1], v148 offset:16384\n"
"v_mfma_f32_16x16x32_bf16 v[4:7], a[32:35], a[148:151], v[4:7]\n"
"ds_read_b64_tr_b16 a[2:3], v148 offset:16640\n"
"v_mfma_f32_16x16x32_bf16 v[8:11], a[32:35], a[152:155], v[8:11]\n"
"s_add_u32 s70, s90, 0\n"
"ds_read_b64_tr_b16 a[4:5], v149 offset:16384\n"
"buffer_load_dwordx4 a[208:211], v152, s[40:43], s70 offen offset:1024 sc1\n"
"v_mfma_f32_16x16x32_bf16 v[12:15], a[32:35], a[156:159], v[12:15]\n"
"ds_read_b64_tr_b16 a[6:7], v149 offset:16640\n"
"v_mfma_f32_16x16x32_bf16 v[28:31], a[36:39], a[156:159], v[28:31]\n"
"ds_read_b64_tr_b16 a[8:9], v148 offset:16896\n"
"v_mfma_f32_16x16x32_bf16 v[24:27], a[36:39], a[152:155], v[24:27]\n"
"ds_read_b64_tr_b16 a[10:11], v148 offset:17152\n"
"v_mfma_f32_16x16x32_bf16 v[20:23], a[36:39], a[148:151], v[20:23]\n"
"ds_read_b64_tr_b16 a[12:13], v149 offset:16896\n"
"v_mfma_f32_16x16x32_bf16 v[16:19], a[36:39], a[144:147], v[16:19]\n"
"s_add_u32 s70, s90, 131072\n"
"ds_read_b64_tr_b16 a[14:15], v149 offset:17152\n"
"buffer_load_dwordx4 a[212:215], v152, s[40:43], s70 offen offset:1024 sc1\n"
"v_mfma_f32_16x16x32_bf16 v[32:35], a[40:43], a[144:147], v[32:35]\n"
"ds_read_b64_tr_b16 a[16:17], v148 offset:17408\n"
"v_mfma_f32_16x16x32_bf16 v[36:39], a[40:43], a[148:151], v[36:39]\n"
"ds_read_b64_tr_b16 a[18:19], v148 offset:17664\n"
"v_mfma_f32_16x16x32_bf16 v[40:43], a[40:43], a[152:155], v[40:43]\n"
"ds_read_b64_tr_b16 a[20:21], v149 offset:17408\n"
"v_mfma_f32_16x16x32_bf16 v[44:47], a[40:43], a[156:159], v[44:47]\n"
"ds_read_b64_tr_b16 a[22:23], v149 offset:17664\n"
"v_mfma_f32_16x16x32_bf16 v[60:63], a[44:47], a[156:159], v[60:63]\n"
"s_add_u32 s70, s90, 262144\n"
"ds_read_b64_tr_b16 a[24:25], v148 offset:17920\n"
"buffer_load_dwordx4 a[216:219], v152, s[40:43], s70 offen offset:1024 sc1\n"
"v_mfma_f32_16x16x32_bf16 v[56:59], a[44:47], a[152:155], v[56:59]\n"
"ds_read_b64_tr_b16 a[26:27], v148 offset:18176\n"
"v_mfma_f32_16x16x32_bf16 v[52:55], a[44:47], a[148:151], v[52:55]\n"
"ds_read_b64_tr_b16 a[28:29], v149 offset:17920\n"
"v_mfma_f32_16x16x32_bf16 v[48:51], a[44:47], a[144:147], v[48:51]\n"
"ds_read_b64_tr_b16 a[30:31], v149 offset:18176\n"
"v_mfma_f32_16x16x32_bf16 v[64:67], a[48:51], a[144:147], v[64:67]\n"
"v_mfma_f32_16x16x32_bf16 v[68:71], a[48:51], a[148:151], v[68:71]\n"
"s_add_u32 s70, s90, 393216\n"
"buffer_load_dwordx4 a[220:223], v152, s[40:43], s70 offen offset:1024 sc1\n"
"v_mfma_f32_16x16x32_bf16 v[72:75], a[48:51], a[152:155], v[72:75]\n"
"v_mfma_f32_16x16x32_bf16 v[76:79], a[48:51], a[156:159], v[76:79]\n"
"v_mfma_f32_16x16x32_bf16 v[92:95], a[52:55], a[156:159], v[92:95]\n"
"v_mfma_f32_16x16x32_bf16 v[88:91], a[52:55], a[152:155], v[88:91]\n"
"v_mfma_f32_16x16x32_bf16 v[84:87], a[52:55], a[148:151], v[84:87]\n"
"s_add_u32 s70, s90, 0\n"
"buffer_load_dwordx4 a[224:227], v152, s[40:43], s70 offen offset:2048 sc1\n"
"v_mfma_f32_16x16x32_bf16 v[80:83], a[52:55], a[144:147], v[80:83]\n"
"v_mfma_f32_16x16x32_bf16 v[96:99], a[56:59], a[144:147], v[96:99]\n"
"v_mfma_f32_16x16x32_bf16 v[100:103], a[56:59], a[148:151], v[100:103]\n"
"v_mfma_f32_16x16x32_bf16 v[104:107], a[56:59], a[152:155], v[104:107]\n"
"v_mfma_f32_16x16x32_bf16 v[108:111], a[56:59], a[156:159], v[108:111]\n"
"s_add_u32 s70, s90, 131072\n"
"buffer_load_dwordx4 a[228:231], v152, s[40:43], s70 offen offset:2048 sc1\n"
"v_mfma_f32_16x16x32_bf16 v[124:127], a[60:63], a[156:159], v[124:127]\n"
"v_mfma_f32_16x16x32_bf16 v[120:123], a[60:63], a[152:155], v[120:123]\n"
"v_mfma_f32_16x16x32_bf16 v[116:119], a[60:63], a[148:151], v[116:119]\n"
"v_mfma_f32_16x16x32_bf16 v[112:115], a[60:63], a[144:147], v[112:115]\n"
"s_waitcnt lgkmcnt(0)\n"
"s_waitcnt vmcnt(23)\n"
"v_mfma_f32_16x16x32_bf16 v[0:3], a[0:3], a[160:163], v[0:3]\n"
"s_add_u32 s70, s90, 262144\n"
"ds_read_b64_tr_b16 a[32:33], v148 offset:24576\n"
"buffer_load_dwordx4 a[232:235], v152, s[40:43], s70 offen offset:2048 sc1\n"
"v_mfma_f32_16x16x32_bf16 v[4:7], a[0:3], a[164:167], v[4:7]\n"
"ds_read_b64_tr_b16 a[34:35], v148 offset:24832\n"
"v_mfma_f32_16x16x32_bf16 v[8:11], a[0:3], a[168:171], v[8:11]\n"
"ds_read_b64_tr_b16 a[36:37], v149 offset:24576\n"
"v_mfma_f32_16x16x32_bf16 v[12:15], a[0:3], a[172:175], v[12:15]\n"
"ds_read_b64_tr_b16 a[38:39], v149 offset:24832\n"
"v_mfma_f32_16x16x32_bf16 v[28:31], a[4:7], a[172:175], v[28:31]\n"
"ds_read_b64_tr_b16 a[40:41], v148 offset:25088\n"
"s_waitcnt vmcnt(12)\n"
"v_cmp_ne_u32 vcc, s77, v253\n"
"s_nop 4\n"
"s_cbranch_vccnz LS4_%=\n"
"LB4_%=:\n"
"s_cmp_lg_u32 s22, 0\ns_cbranch_scc1 LYL_%=\nLYB_%=:\n"
"v_mfma_f32_16x16x32_bf16 v[24:27], a[4:7], a[168:171], v[24:27]\n"
"s_add_u32 s70, s90, 393216\n"
"ds_read_b64_tr_b16 a[42:43], v148 offset:25344\n"
"buffer_load_dwordx4 a[236:239], v152, s[40:43], s70 offen offset:2048 sc1\n"
"v_mfma_f32_16x16x32_bf16 v[20:23], a[4:7], a[164:167], v[20:23]\n"
"ds_read_b64_tr_b16 a[44:45], v149 offset:25088\n"
"v_mfma_f32_16x16x32_bf16 v[16:19], a[4:7], a[160:163], v[16:19]\n"
"ds_read_b64_tr_b16 a[46:47], v149 offset:25344\n"
"v_mfma_f32_16x16x32_bf16 v[32:35], a[8:11], a[160:163], v[32:35]\n"
"ds_read_b64_tr_b16 a[48:49], v148 offset:25600\n"
"v_mfma_f32_16x16x32_bf16 v[36:39], a[8:11], a[164:167], v[36:39]\n"
"ds_read_b64_tr_b16 a[50:51], v148 offset:25856\n"
"v_mfma_f32_16x16x32_bf16 v[40:43], a[8:11], a[168:171], v[40:43]\n"
"s_add_u32 s70, s90, 0\n"
"ds_read_b64_tr_b16 a[52:53], v149 offset:25600\n"
"buffer_load_dwordx4 a[240:243], v152, s[40:43], s70 offen offset:3072 sc1\n"
"v_mfma_f32_16x16x32_bf16 v[44:47], a[8:11], a[172:175], v[44:47]\n"
"ds_read_b64_tr_b16 a[54:55], v149 offset:25856\n"
"v_mfma_f32_16x16x32_bf16 v[60:63], a[12:15], a[172:175], v[60:63]\n"
"ds_read_b64_tr_b16 a[56:57], v148 offset:26112\n"
"v_mfma_f32_16x16x32_bf16 v[56:59], a[12:15], a[168:171], v[56:59]\n"
"ds_read_b64_tr_b16 a[58:59], v148 offset:26368\n"
"v_mfma_f32_16x16x32_bf16 v[52:55], a[12:15], a[164:167], v[52:55]\n"
"ds_read_b64_tr_b16 a[60:61], v149 offset:26112\n"
"v_mfma_f32_16x16x32_bf16 v[48:51], a[12:15], a[160:163], v[48:51]\n"
"s_add_u32 s70, s90, 131072\n"
"ds_read_b64_tr_b16 a[62:63], v149 offset:26368\n"
"buffer_load_dwordx4 a[244:247], v152, s[40:43], s70 offen offset:3072 sc1\n"
"v_mfma_f32_16x16x32_bf16 v[64:67], a[16:19], a[160:163], v[64:67]\n"
"v_mfma_f32_16x16x32_bf16 v[68:71], a[16:19], a[164:167], v[68:71]\n"
"v_mfma_f32_16x16x32_bf16 v[72:75], a[16:19], a[168:171], v[72:75]\n"
"v_mfma_f32_16x16x32_bf16 v[76:79], a[16:19], a[172:175], v[76:79]\n"
"v_mfma_f32_16x16x32_bf16 v[92:95], a[20:23], a[172:175], v[92:95]\n"
"s_add_u32 s70, s90, 262144\n"
"buffer_load_dwordx4 a[248:251], v152, s[40:43], s70 offen offset:3072 sc1\n"
"v_mfma_f32_16x16x32_bf16 v[88:91], a[20:23], a[168:171], v[88:91]\n"
"v_mfma_f32_16x16x32_bf16 v[84:87], a[20:23], a[164:167], v[84:87]\n"
"v_mfma_f32_16x16x32_bf16 v[80:83], a[20:23], a[160:163], v[80:83]\n"
"v_mfma_f32_16x16x32_bf16 v[96:99], a[24:27], a[160:163], v[96:99]\n"
"v_mfma_f32_16x16x32_bf16 v[100:103], a[24:27], a[164:167], v[100:103]\n"
"s_add_u32 s70, s90, 393216\n"
"buffer_load_dwordx4 a[252:255], v152, s[40:43], s70 offen offset:3072 sc1\n"
"v_mfma_f32_16x16x32_bf16 v[104:107], a[24:27], a[168:171], v[104:107]\n"
"v_mfma_f32_16x16x32_bf16 v[108:111], a[24:27], a[172:175], v[108:111]\n"
"v_mfma_f32_16x16x32_bf16 v[124:127], a[28:31], a[172:175], v[124:127]\n"
"v_mfma_f32_16x16x32_bf16 v[120:123], a[28:31], a[168:171], v[120:123]\n"
"v_mfma_f32_16x16x32_bf16 v[116:119], a[28:31], a[164:167], v[116:119]\n"
"v_mfma_f32_16x16x32_bf16 v[112:115], a[28:31], a[160:163], v[112:115]\n"
"s_cmp_le_u32 s83, 5\ns_cselect_b32 s89, 6, 5\ns_lshl_b32 s89, s89, 18\n"
"s_add_u32 m0, s68, 0\n"
"s_waitcnt lgkmcnt(0)\n"
"s_waitcnt vmcnt(25)\n"
"s_waitcnt vmcnt(16)\n"
"s_barrier\n"
"v_mfma_f32_16x16x32_bf16 v[0:3], a[32:35], a[176:179], v[0:3]\n"
"s_add_u32 s69, s89, 0\n"
"ds_read_b64_tr_b16 a[0:1], v146 offset:33280\n"
"buffer_load_dwordx4 v150, s[36:39], s69 offen offset:0 lds\n"
"v_mfma_f32_16x16x32_bf16 v[4:7], a[32:35], a[180:183], v[4:7]\n"
"ds_read_b64_tr_b16 a[2:3], v146 offset:33536\n"
"v_mfma_f32_16x16x32_bf16 v[8:11], a[32:35], a[184:187], v[8:11]\n"
"ds_read_b64_tr_b16 a[4:5], v147 offset:33280\n"
"v_mfma_f32_16x16x32_bf16 v[12:15], a[32:35], a[188:191], v[12:15]\n"
"ds_read_b64_tr_b16 a[6:7], v147 offset:33536\n"
"v_mfma_f32_16x16x32_bf16 v[28:31], a[36:39], a[188:191], v[28:31]\n"
"ds_read_b64_tr_b16 a[8:9], v146 offset:33792\n"
"v_mfma_f32_16x16x32_bf16 v[24:27], a[36:39], a[184:187], v[24:27]\n"
"ds_read_b64_tr_b16 a[10:11], v146 offset:34048\n"
"buffer_load_dwordx4 v151, s[36:39], s69 offen offset:1024 lds\n"
"v_mfma_f32_16x16x32_bf16 v[20:23], a[36:39], a[180:183], v[20:23]\n"
"ds_read_b64_tr_b16 a[12:13], v147 offset:33792\n"
"v_mfma_f32_16x16x32_bf16 v[16:19], a[36:39], a[176:179], v[16:19]\n"
"ds_read_b64_tr_b16 a[14:15], v147 offset:34048\n"
"v_mfma_f32_16x16x32_bf16 v[32:35], a[40:43], a[176:179], v[32:35]\n"
"ds_read_b64_tr_b16 a[16:17], v146 offset:34304\n"
"v_mfma_f32_16x16x32_bf16 v[36:39], a[40:43], a[180:183], v[36:39]\n"
"s_add_u32 m0, s68, 8192\n"
"ds_read_b64_tr_b16 a[18:19], v146 offset:34560\n"
"v_mfma_f32_16x16x32_bf16 v[40:43], a[40:43], a[184:187], v[40:43]\n"
"s_add_u32 s69, s89, 65536\n"
"ds_read_b64_tr_b16 a[20:21], v147 offset:34304\n"
"buffer_load_dwordx4 v150, s[36:39], s69 offen offset:0 lds\n"
"v_mfma_f32_16x16x32_bf16 v[44:47], a[40:43], a[188:191], v[44:47]\n"
"ds_read_b64_tr_b16 a[22:23], v147 offset:34560\n"
"v_mfma_f32_16x16x32_bf16 v[60:63], a[44:47], a[188:191], v[60:63]\n"
"v_mfma_f32_16x16x32_bf16 v[56:59], a[44:47], a[184:187], v[56:59]\n"
"v_mfma_f32_16x16x32_bf16 v[52:55], a[44:47], a[180:183], v[52:55]\n"
"v_mfma_f32_16x16x32_bf16 v[48:51], a[44:47], a[176:179], v[48:51]\n"
"buffer_load_dwordx4 v151, s[36:39], s69 offen offset:1024 lds\n"
"v_mfma_f32_16x16x32_bf16 v[64:67], a[48:51], a[176:179], v[64:67]\n"
"v_mfma_f32_16x16x32_bf16 v[68:71], a[48:51], a[180:183], v[68:71]\n"
"v_mfma_f32_16x16x32_bf16 v[72:75], a[48:51], a[184:187], v[72:75]\n"
"v_mfma_f32_16x16x32_bf16 v[76:79], a[48:51], a[188:191], v[76:79]\n"
"s_add_u32 m0, s68, 16384\n"
"v_mfma_f32_16x16x32_bf16 v[92:95], a[52:55], a[188:191], v[92:95]\n"
"s_add_u32 s69, s89, 131072\n"
"buffer_load_dwordx4 v150, s[36:39], s69 offen offset:0 lds\n"
"v_mfma_f32_16x16x32_bf16 v[88:91], a[52:55], a[184:187], v[88:91]\n"
"v_mfma_f32_16x16x32_bf16 v[84:87], a[52:55], a[180:183], v[84:87]\n"
"v_mfma_f32_16x16x32_bf16 v[80:83], a[52:55], a[176:179], v[80:83]\n"
"v_mfma_f32_16x16x32_bf16 v[96:99], a[56:59], a[176:179], v[96:99]\n"
"v_mfma_f32_16x16x32_bf16 v[100:103], a[56:59], a[180:183], v[100:103]\n"
"buffer_load_dwordx4 v151, s[36:39], s69 offen offset:1024 lds\n"
"v_mfma_f32_16x16x32_bf16 v[104:107], a[56:59], a[184:187], v[104:107]\n"
"v_mfma_f32_16x16x32_bf16 v[108:111], a[56:59], a[188:191], v[108:111]\n"
"v_mfma_f32_16x16x32_bf16 v[124:127], a[60:63], a[188:191], v[124:127]\n"
"v_mfma_f32_16x16x32_bf16 v[120:123], a[60:63], a[184:187], v[120:123]\n"
"s_add_u32 m0, s68, 24576\n"
"v_mfma_f32_16x16x32_bf16 v[116:119], a[60:63], a[180:183], v[116:119]\n"
"s_add_u32 s69, s89, 196608\n"
"buffer_load_dwordx4 v150, s[36:39], s69 offen offset:0 lds\n"
"v_mfma_f32_16x16x32_bf16 v[112:115], a[60:63], a[176:179], v[112:115]\n"
"s_waitcnt lgkmcnt(0)\n"
"s_waitcnt vmcnt(19)\n"
"v_mfma_f32_16x16x32_bf16 v[32:35], a[0:3], a[192:195], v[32:35]\n"
"s_lshr_b32 s74, s78, 3\n"
"s_mul_i32 s74, s74, 256\n"
"ds_read_b64_tr_b16 a[32:33], v146 offset:41472\n"
"v_mfma_f32_16x16x32_bf16 v[36:39], a[0:3], a[196:199], v[36:39]\n"
"ds_read_b64_tr_b16 a[34:35], v146 offset:41728\n"
"v_lshrrev_b32 v159, 4, v152\n"
"v_mfma_f32_16x16x32_bf16 v[40:43], a[0:3], a[200:203], v[40:43]\n"
"ds_read_b64_tr_b16 a[36:37], v147 offset:41472\n"
"v_min_u32 v159, 1, v159\n"
"v_mfma_f32_16x16x32_bf16 v[44:47], a[0:3], a[204:207], v[44:47]\n"
"ds_read_b64_tr_b16 a[38:39], v147 offset:41728\n"
"buffer_load_dwordx4 v151, s[36:39], s69 offen offset:1024 lds\n"
"v_mfma_f32_16x16x32_bf16 v[60:63], a[4:7], a[204:207], v[60:63]\n"
"ds_read_b64_tr_b16 a[40:41], v146 offset:41984\n"
"v_lshlrev_b32 v159, 7, v159\n"
"s_waitcnt vmcnt(13)\n"
"v_mfma_f32_16x16x32_bf16 v[56:59], a[4:7], a[200:203], v[56:59]\n"
"ds_read_b64_tr_b16 a[42:43], v146 offset:42240\n"
"v_add_u32 v159, s74, v159\n"
"v_mfma_f32_16x16x32_bf16 v[52:55], a[4:7], a[196:199], v[52:55]\n"
"s_getpc_b64 s[20:21]\nFK_H1:\ns_add_u32 s20, s20, FK_TAIL-FK_H1\ns_addc_u32 s21, s21, 0\n"
"ds_read_b64_tr_b16 a[44:45], v147 offset:41984\n"
"global_load_dword v254, v159, s[20:21]\n"
"v_mfma_f32_16x16x32_bf16 v[48:51], a[4:7], a[192:195], v[48:51]\n"
"ds_read_b64_tr_b16 a[46:47], v147 offset:42240\n"
"v_add_f32 v248, v176, v177\n"
"v_mfma_f32_16x16x32_bf16 v[64:67], a[8:11], a[192:195], v[64:67]\n"
"s_cmp_le_u32 s83, 5\ns_cselect_b32 s90, 6, 5\ns_lshl_b32 s90, s90, 12\n"
"s_add_u32 s70, s90, 0\n"
"ds_read_b64_tr_b16 a[48:49], v146 offset:42496\n"
"v_mfma_f32_16x16x32_bf16 v[68:71], a[8:11], a[196:199], v[68:71]\n"
"ds_read_b64_tr_b16 a[50:51], v146 offset:42752\n"
"buffer_load_dwordx4 a[64:67], v152, s[40:43], s70 offen offset:0 sc1\n"
"v_mfma_f32_16x16x32_bf16 v[72:75], a[8:11], a[200:203], v[72:75]\n"
"ds_read_b64_tr_b16 a[52:53], v147 offset:42496\n"
"v_add_f32 v249, v178, v179\n"
"v_mfma_f32_16x16x32_bf16 v[76:79], a[8:11], a[204:207], v[76:79]\n"
"ds_read_b64_tr_b16 a[54:55], v147 offset:42752\n"
"v_add_f32 v248, v248, v180\n"
"v_mfma_f32_16x16x32_bf16 v[92:95], a[12:15], a[204:207], v[92:95]\n"
"v_add_f32 v249, v249, v181\n"
"v_add_f32 v248, v248, v182\n"
"v_mfma_f32_16x16x32_bf16 v[88:91], a[12:15], a[200:203], v[88:91]\n"
"v_add_f32 v249, v249, v183\n"
"v_add_f32 v248, v248, v184\n"
"v_mfma_f32_16x16x32_bf16 v[84:87], a[12:15], a[196:199], v[84:87]\n"
"s_add_u32 s70, s90, 131072\n"
"buffer_load_dwordx4 a[68:71], v152, s[40:43], s70 offen offset:0 sc1\n"
"v_add_f32 v249, v249, v185\n"
"v_mfma_f32_16x16x32_bf16 v[80:83], a[12:15], a[192:195], v[80:83]\n"
"v_add_f32 v248, v248, v186\n"
"v_add_f32 v249, v249, v187\n"
"v_mfma_f32_16x16x32_bf16 v[96:99], a[16:19], a[192:195], v[96:99]\n"
"v_add_f32 v248, v248, v188\n"
"v_add_f32 v249, v249, v189\n"
"v_mfma_f32_16x16x32_bf16 v[100:103], a[16:19], a[196:199], v[100:103]\n"
"v_add_f32 v248, v248, v190\n"
"v_add_f32 v249, v249, v191\n"
"v_mfma_f32_16x16x32_bf16 v[104:107], a[16:19], a[200:203], v[104:107]\n"
"v_add_f32 v248, v248, v208\n"
"v_add_f32 v249, v249, v209\n"
"v_mfma_f32_16x16x32_bf16 v[108:111], a[16:19], a[204:207], v[108:111]\n"
"s_add_u32 s70, s90, 262144\n"
"buffer_load_dwordx4 a[72:75], v152, s[40:43], s70 offen offset:0 sc1\n"
"v_add_f32 v248, v248, v210\n"
"v_mfma_f32_16x16x32_bf16 v[124:127], a[20:23], a[204:207], v[124:127]\n"
"v_add_f32 v249, v249, v211\n"
"v_add_f32 v248, v248, v212\n"
"v_mfma_f32_16x16x32_bf16 v[120:123], a[20:23], a[200:203], v[120:123]\n"
"v_add_f32 v249, v249, v213\n"
"v_add_f32 v248, v248, v214\n"
"v_mfma_f32_16x16x32_bf16 v[116:119], a[20:23], a[196:199], v[116:119]\n"
"v_add_f32 v249, v249, v215\n"
"v_add_f32 v248, v248, v216\n"
"v_mfma_f32_16x16x32_bf16 v[112:115], a[20:23], a[192:195], v[112:115]\n"
"v_add_f32 v249, v249, v217\n"
"s_waitcnt lgkmcnt(0)\n"
"s_waitcnt vmcnt(20)\n"
"v_mfma_f32_16x16x32_bf16 v[32:35], a[32:35], a[208:211], v[32:35]\n"
"s_add_u32 s70, s90, 393216\n"
"ds_read_b64_tr_b16 a[0:1], v146 offset:49664\n"
"buffer_load_dwordx4 a[76:79], v152, s[40:43], s70 offen offset:0 sc1\n"
"v_mfma_f32_16x16x32_bf16 v[36:39], a[32:35], a[212:215], v[36:39]\n"
"ds_read_b64_tr_b16 a[2:3], v146 offset:49920\n"
"v_add_f32 v248, v248, v218\n"
"v_mfma_f32_16x16x32_bf16 v[40:43], a[32:35], a[216:219], v[40:43]\n"
"ds_read_b64_tr_b16 a[4:5], v147 offset:49664\n"
"v_add_f32 v249, v249, v219\n"
"v_mfma_f32_16x16x32_bf16 v[44:47], a[32:35], a[220:223], v[44:47]\n"
"ds_read_b64_tr_b16 a[6:7], v147 offset:49920\n"
"v_add_f32 v248, v248, v249\n"
"v_mfma_f32_16x16x32_bf16 v[60:63], a[36:39], a[220:223], v[60:63]\n"
"ds_read_b64_tr_b16 a[8:9], v146 offset:50176\n"
"v_lshlrev_b32 v251, 16, v251\n"
"v_mfma_f32_16x16x32_bf16 v[56:59], a[36:39], a[216:219], v[56:59]\n"
"s_add_u32 s70, s90, 0\n"
"ds_read_b64_tr_b16 a[10:11], v146 offset:50432\n"
"buffer_load_dwordx4 a[80:83], v152, s[40:43], s70 offen offset:1024 sc1\n"
"v_mfma_f32_16x16x32_bf16 v[52:55], a[36:39], a[212:215], v[52:55]\n"
"ds_read_b64_tr_b16 a[12:13], v147 offset:50176\n"
"v_subrev_f32 v251, s86, v251\n"
"v_mfma_f32_16x16x32_bf16 v[48:51], a[36:39], a[208:211], v[48:51]\n"
"ds_read_b64_tr_b16 a[14:15], v147 offset:50432\n"
"v_cvt_pk_bf16_f32 v248, v248, v248\n"
"v_mfma_f32_16x16x32_bf16 v[64:67], a[40:43], a[208:211], v[64:67]\n"
"ds_read_b64_tr_b16 a[16:17], v146 offset:50688\n"
"v_lshlrev_b32 v248, 16, v248\n"
"v_mfma_f32_16x16x32_bf16 v[68:71], a[40:43], a[212:215], v[68:71]\n"
"ds_read_b64_tr_b16 a[18:19], v146 offset:50944\n"
"v_cvt_pk_bf16_f32 v251, v251, v251\n"
"v_mfma_f32_16x16x32_bf16 v[72:75], a[40:43], a[216:219], v[72:75]\n"
"s_add_u32 s70, s90, 131072\n"
"ds_read_b64_tr_b16 a[20:21], v147 offset:50688\n"
"buffer_load_dwordx4 a[84:87], v152, s[40:43], s70 offen offset:1024 sc1\n"
"v_mfma_f32_16x16x32_bf16 v[76:79], a[40:43], a[220:223], v[76:79]\n"
"ds_read_b64_tr_b16 a[22:23], v147 offset:50944\n"
"v_lshlrev_b32 v251, 16, v251\n"
"v_mfma_f32_16x16x32_bf16 v[92:95], a[44:47], a[220:223], v[92:95]\n"
"v_mul_f32 v248, s88, v248\n"
"v_mul_f32 v251, s87, v251\n"
"v_mfma_f32_16x16x32_bf16 v[88:91], a[44:47], a[216:219], v[88:91]\n"
"v_cvt_pk_bf16_f32 v248, v248, v248\n"
"v_lshlrev_b32 v248, 16, v248\n"
"v_mfma_f32_16x16x32_bf16 v[84:87], a[44:47], a[212:215], v[84:87]\n"
"v_cvt_pk_bf16_f32 v251, v251, v251\n"
"v_lshlrev_b32 v251, 16, v251\n"
"v_mfma_f32_16x16x32_bf16 v[80:83], a[44:47], a[208:211], v[80:83]\n"
"s_add_u32 s70, s90, 262144\n"
"buffer_load_dwordx4 a[88:91], v152, s[40:43], s70 offen offset:1024 sc1\n"
"v_add_f32 v248, v248, v251\n"
"v_mfma_f32_16x16x32_bf16 v[96:99], a[48:51], a[208:211], v[96:99]\n"
"v_cvt_pk_bf16_f32 v248, v248, v248\n"
"v_lshlrev_b32 v248, 16, v248\n"
"v_mfma_f32_16x16x32_bf16 v[100:103], a[48:51], a[212:215], v[100:103]\n"
"v_mul_f32 v249, 0xbfb8aa3b, v248\n"
"v_mfma_f32_16x16x32_bf16 v[104:107], a[48:51], a[216:219], v[104:107]\n"
"v_exp_f32 v249, v249\n"
"v_mfma_f32_16x16x32_bf16 v[108:111], a[48:51], a[220:223], v[108:111]\n"
"v_add_f32 v249, 1.0, v249\n"
"v_mfma_f32_16x16x32_bf16 v[124:127], a[52:55], a[220:223], v[124:127]\n"
"s_add_u32 s70, s90, 393216\n"
"buffer_load_dwordx4 a[92:95], v152, s[40:43], s70 offen offset:1024 sc1\n"
"v_rcp_f32 v249, v249\n"
"v_mfma_f32_16x16x32_bf16 v[120:123], a[52:55], a[216:219], v[120:123]\n"
"v_mul_f32 v248, v248, v249\n"
"v_cvt_pk_bf16_f32 v248, v248, v248\n"
"v_mfma_f32_16x16x32_bf16 v[116:119], a[52:55], a[212:215], v[116:119]\n"
"v_lshlrev_b32 v248, 16, v248\n"
"v_mfma_f32_16x16x32_bf16 v[112:115], a[52:55], a[208:211], v[112:115]\n"
"s_cmp_eq_u32 s94, 0\ns_cselect_b32 vcc_lo, s95, -1\ns_cselect_b32 vcc_hi, s95, -1\n"
"s_waitcnt lgkmcnt(0)\n"
"s_waitcnt vmcnt(21)\n"
"v_mfma_f32_16x16x32_bf16 v[32:35], a[0:3], a[224:227], v[32:35]\n"
"ds_read_b64_tr_b16 a[32:33], v146 offset:57856\n"
"v_cndmask_b32_dpp v136, v248, v136, vcc row_newbcast:0 row_mask:0xf bank_mask:0xf\n"
"v_mfma_f32_16x16x32_bf16 v[36:39], a[0:3], a[228:231], v[36:39]\n"
"s_add_u32 s70, s90, 0\n"
"ds_read_b64_tr_b16 a[34:35], v146 offset:58112\n"
"buffer_load_dwordx4 a[96:99], v152, s[40:43], s70 offen offset:2048 sc1\n"
"v_mfma_f32_16x16x32_bf16 v[40:43], a[0:3], a[232:235], v[40:43]\n"
"ds_read_b64_tr_b16 a[36:37], v147 offset:57856\n"
"v_cndmask_b32_dpp v137, v248, v137, vcc row_newbcast:1 row_mask:0xf bank_mask:0xf\n"
"v_mfma_f32_16x16x32_bf16 v[44:47], a[0:3], a[236:239], v[44:47]\n"
"ds_read_b64_tr_b16 a[38:39], v147 offset:58112\n"
"v_cndmask_b32_dpp v138, v248, v138, vcc row_newbcast:2 row_mask:0xf bank_mask:0xf\n"
"v_mfma_f32_16x16x32_bf16 v[60:63], a[4:7], a[236:239], v[60:63]\n"
"ds_read_b64_tr_b16 a[40:41], v146 offset:58368\n"
"v_cndmask_b32_dpp v139, v248, v139, vcc row_newbcast:3 row_mask:0xf bank_mask:0xf\n"
"v_mfma_f32_16x16x32_bf16 v[56:59], a[4:7], a[232:235], v[56:59]\n"
"ds_read_b64_tr_b16 a[42:43], v146 offset:58624\n"
"v_cndmask_b32_dpp v140, v248, v140, vcc row_newbcast:4 row_mask:0xf bank_mask:0xf\n"
"v_mfma_f32_16x16x32_bf16 v[52:55], a[4:7], a[228:231], v[52:55]\n"
"s_add_u32 s70, s90, 131072\n"
"ds_read_b64_tr_b16 a[44:45], v147 offset:58368\n"
"buffer_load_dwordx4 a[100:103], v152, s[40:43], s70 offen offset:2048 sc1\n"
"v_mfma_f32_16x16x32_bf16 v[48:51], a[4:7], a[224:227], v[48:51]\n"
"ds_read_b64_tr_b16 a[46:47], v147 offset:58624\n"
"v_cndmask_b32_dpp v141, v248, v141, vcc row_newbcast:5 row_mask:0xf bank_mask:0xf\n"
"v_mfma_f32_16x16x32_bf16 v[64:67], a[8:11], a[224:227], v[64:67]\n"
"ds_read_b64_tr_b16 a[48:49], v146 offset:58880\n"
"v_cndmask_b32_dpp v142, v248, v142, vcc row_newbcast:6 row_mask:0xf bank_mask:0xf\n"
"v_mfma_f32_16x16x32_bf16 v[68:71], a[8:11], a[228:231], v[68:71]\n"
"ds_read_b64_tr_b16 a[50:51], v146 offset:59136\n"
"v_cndmask_b32_dpp v143, v248, v143, vcc row_newbcast:7 row_mask:0xf bank_mask:0xf\n"
"v_mfma_f32_16x16x32_bf16 v[72:75], a[8:11], a[232:235], v[72:75]\n"
"s_cmp_eq_u32 s94, 1\ns_cselect_b32 vcc_lo, s95, -1\ns_cselect_b32 vcc_hi, s95, -1\n"
"ds_read_b64_tr_b16 a[52:53], v147 offset:58880\n"
"v_cndmask_b32_dpp v168, v248, v168, vcc row_newbcast:0 row_mask:0xf bank_mask:0xf\n"
"v_mfma_f32_16x16x32_bf16 v[76:79], a[8:11], a[236:239], v[76:79]\n"
"s_add_u32 s70, s90, 262144\n"
"ds_read_b64_tr_b16 a[54:55], v147 offset:59136\n"
"buffer_load_dwordx4 a[104:107], v152, s[40:43], s70 offen offset:2048 sc1\n"
"v_mfma_f32_16x16x32_bf16 v[92:95], a[12:15], a[236:239], v[92:95]\n"
"v_cndmask_b32_dpp v169, v248, v169, vcc row_newbcast:1 row_mask:0xf bank_mask:0xf\n"
"v_cndmask_b32_dpp v170, v248, v170, vcc row_newbcast:2 row_mask:0xf bank_mask:0xf\n"
"v_mfma_f32_16x16x32_bf16 v[88:91], a[12:15], a[232:235], v[88:91]\n"
"v_cndmask_b32_dpp v171, v248, v171, vcc row_newbcast:3 row_mask:0xf bank_mask:0xf\n"
"v_cndmask_b32_dpp v172, v248, v172, vcc row_newbcast:4 row_mask:0xf bank_mask:0xf\n"
"v_mfma_f32_16x16x32_bf16 v[84:87], a[12:15], a[228:231], v[84:87]\n"
"v_cndmask_b32_dpp v173, v248, v173, vcc row_newbcast:5 row_mask:0xf bank_mask:0xf\n"
"v_cndmask_b32_dpp v174, v248, v174, vcc row_newbcast:6 row_mask:0xf bank_mask:0xf\n"
"v_mfma_f32_16x16x32_bf16 v[80:83], a[12:15], a[224:227], v[80:83]\n"
"v_cndmask_b32_dpp v175, v248, v175, vcc row_newbcast:7 row_mask:0xf bank_mask:0xf\n"
"v_mfma_f32_16x16x32_bf16 v[96:99], a[16:19], a[224:227], v[96:99]\n"
"s_add_u32 s70, s90, 393216\n"
"s_cmp_eq_u32 s94, 2\ns_cselect_b32 vcc_lo, s95, -1\ns_cselect_b32 vcc_hi, s95, -1\n"
"buffer_load_dwordx4 a[108:111], v152, s[40:43], s70 offen offset:2048 sc1\n"
"v_mfma_f32_16x16x32_bf16 v[100:103], a[16:19], a[228:231], v[100:103]\n"
"v_cndmask_b32_dpp v200, v248, v200, vcc row_newbcast:0 row_mask:0xf bank_mask:0xf\n"
"v_cndmask_b32_dpp v201, v248, v201, vcc row_newbcast:1 row_mask:0xf bank_mask:0xf\n"
"v_mfma_f32_16x16x32_bf16 v[104:107], a[16:19], a[232:235], v[104:107]\n"
"v_cndmask_b32_dpp v202, v248, v202, vcc row_newbcast:2 row_mask:0xf bank_mask:0xf\n"
"v_cndmask_b32_dpp v203, v248, v203, vcc row_newbcast:3 row_mask:0xf bank_mask:0xf\n"
"v_mfma_f32_16x16x32_bf16 v[108:111], a[16:19], a[236:239], v[108:111]\n"
"v_cndmask_b32_dpp v204, v248, v204, vcc row_newbcast:4 row_mask:0xf bank_mask:0xf\n"
"v_cndmask_b32_dpp v205, v248, v205, vcc row_newbcast:5 row_mask:0xf bank_mask:0xf\n"
"v_mfma_f32_16x16x32_bf16 v[124:127], a[20:23], a[236:239], v[124:127]\n"
"v_cndmask_b32_dpp v206, v248, v206, vcc row_newbcast:6 row_mask:0xf bank_mask:0xf\n"
"v_cndmask_b32_dpp v207, v248, v207, vcc row_newbcast:7 row_mask:0xf bank_mask:0xf\n"
"v_mfma_f32_16x16x32_bf16 v[120:123], a[20:23], a[232:235], v[120:123]\n"
"s_add_u32 s70, s90, 0\n"
"s_cmp_eq_u32 s94, 3\ns_cselect_b32 vcc_lo, s95, -1\ns_cselect_b32 vcc_hi, s95, -1\n"
"buffer_load_dwordx4 a[112:115], v152, s[40:43], s70 offen offset:3072 sc1\n"
"v_mfma_f32_16x16x32_bf16 v[116:119], a[20:23], a[228:231], v[116:119]\n"
"v_cndmask_b32_dpp v232, v248, v232, vcc row_newbcast:0 row_mask:0xf bank_mask:0xf\n"
"v_cndmask_b32_dpp v233, v248, v233, vcc row_newbcast:1 row_mask:0xf bank_mask:0xf\n"
"v_mfma_f32_16x16x32_bf16 v[112:115], a[20:23], a[224:227], v[112:115]\n"
"s_waitcnt lgkmcnt(0)\n"
"s_waitcnt vmcnt(22)\n"
"s_waitcnt vmcnt(14)\n"
"s_barrier\n"
"v_mfma_f32_16x16x32_bf16 v[32:35], a[32:35], a[240:243], v[32:35]\n"
"ds_read_b64_tr_b16 a[0:1], v144 offset:512\n"
"v_cndmask_b32_dpp v234, v248, v234, vcc row_newbcast:2 row_mask:0xf bank_mask:0xf\n"
"v_mfma_f32_16x16x32_bf16 v[36:39], a[32:35], a[244:247], v[36:39]\n"
"ds_read_b64_tr_b16 a[2:3], v144 offset:768\n"
"v_cndmask_b32_dpp v235, v248, v235, vcc row_newbcast:3 row_mask:0xf bank_mask:0xf\n"
"v_mfma_f32_16x16x32_bf16 v[40:43], a[32:35], a[248:251], v[40:43]\n"
"s_add_u32 s70, s90, 131072\n"
"ds_read_b64_tr_b16 a[4:5], v145 offset:512\n"
"buffer_load_dwordx4 a[116:119], v152, s[40:43], s70 offen offset:3072 sc1\n"
"v_mfma_f32_16x16x32_bf16 v[44:47], a[32:35], a[252:255], v[44:47]\n"
"ds_read_b64_tr_b16 a[6:7], v145 offset:768\n"
"v_cndmask_b32_dpp v236, v248, v236, vcc row_newbcast:4 row_mask:0xf bank_mask:0xf\n"
"v_mfma_f32_16x16x32_bf16 v[60:63], a[36:39], a[252:255], v[60:63]\n"
"ds_read_b64_tr_b16 a[8:9], v144 offset:1024\n"
"v_cndmask_b32_dpp v237, v248, v237, vcc row_newbcast:5 row_mask:0xf bank_mask:0xf\n"
"v_mfma_f32_16x16x32_bf16 v[56:59], a[36:39], a[248:251], v[56:59]\n"
"ds_read_b64_tr_b16 a[10:11], v144 offset:1280\n"
"v_cndmask_b32_dpp v238, v248, v238, vcc row_newbcast:6 row_mask:0xf bank_mask:0xf\n"
"v_mfma_f32_16x16x32_bf16 v[52:55], a[36:39], a[244:247], v[52:55]\n"
"ds_read_b64_tr_b16 a[12:13], v145 offset:1024\n"
"v_cndmask_b32_dpp v239, v248, v239, vcc row_newbcast:7 row_mask:0xf bank_mask:0xf\n"
"v_mfma_f32_16x16x32_bf16 v[48:51], a[36:39], a[240:243], v[48:51]\n"
"s_add_u32 s70, s90, 262144\n"
"ds_read_b64_tr_b16 a[14:15], v145 offset:1280\n"
"buffer_load_dwordx4 a[120:123], v152, s[40:43], s70 offen offset:3072 sc1\n"
"v_mfma_f32_16x16x32_bf16 v[64:67], a[40:43], a[240:243], v[64:67]\n"
"ds_read_b64_tr_b16 a[16:17], v144 offset:1536\n"
"v_mfma_f32_16x16x32_bf16 v[68:71], a[40:43], a[244:247], v[68:71]\n"
"ds_read_b64_tr_b16 a[18:19], v144 offset:1792\n"
"v_mfma_f32_16x16x32_bf16 v[72:75], a[40:43], a[248:251], v[72:75]\n"
"ds_read_b64_tr_b16 a[20:21], v145 offset:1536\n"
"v_mfma_f32_16x16x32_bf16 v[76:79], a[40:43], a[252:255], v[76:79]\n"
"ds_read_b64_tr_b16 a[22:23], v145 offset:1792\n"
"v_mfma_f32_16x16x32_bf16 v[92:95], a[44:47], a[252:255], v[92:95]\n"
"s_add_u32 s70, s90, 393216\n"
"buffer_load_dwordx4 a[124:127], v152, s[40:43], s70 offen offset:3072 sc1\n"
"v_mfma_f32_16x16x32_bf16 v[88:91], a[44:47], a[248:251], v[88:91]\n"
"v_mfma_f32_16x16x32_bf16 v[84:87], a[44:47], a[244:247], v[84:87]\n"
"v_mfma_f32_16x16x32_bf16 v[80:83], a[44:47], a[240:243], v[80:83]\n"
"v_mfma_f32_16x16x32_bf16 v[96:99], a[48:51], a[240:243], v[96:99]\n"
"v_mfma_f32_16x16x32_bf16 v[100:103], a[48:51], a[244:247], v[100:103]\n"
"v_mfma_f32_16x16x32_bf16 v[104:107], a[48:51], a[248:251], v[104:107]\n"
"s_cmp_le_u32 s83, 6\ns_cselect_b32 s89, 7, 6\ns_lshl_b32 s89, s89, 18\n"
"s_add_u32 m0, s68, 32768\n"
"v_mfma_f32_16x16x32_bf16 v[108:111], a[48:51], a[252:255], v[108:111]\n"
"s_add_u32 s69, s89, 0\n"
"buffer_load_dwordx4 v150, s[36:39], s69 offen offset:0 lds\n"
"v_mfma_f32_16x16x32_bf16 v[124:127], a[52:55], a[252:255], v[124:127]\n"
"v_mfma_f32_16x16x32_bf16 v[120:123], a[52:55], a[248:251], v[120:123]\n"
"v_mfma_f32_16x16x32_bf16 v[116:119], a[52:55], a[244:247], v[116:119]\n"
"v_mfma_f32_16x16x32_bf16 v[112:115], a[52:55], a[240:243], v[112:115]\n"
"s_waitcnt lgkmcnt(0)\n"
"s_waitcnt vmcnt(13)\n"
"v_mfma_f32_16x16x32_bf16 v[32:35], a[0:3], a[64:67], v[32:35]\n"
"ds_read_b64_tr_b16 a[32:33], v144 offset:8704\n"
"buffer_load_dwordx4 v151, s[36:39], s69 offen offset:1024 lds\n"
"v_mfma_f32_16x16x32_bf16 v[36:39], a[0:3], a[68:71], v[36:39]\n"
"ds_read_b64_tr_b16 a[34:35], v144 offset:8960\n"
"v_mfma_f32_16x16x32_bf16 v[40:43], a[0:3], a[72:75], v[40:43]\n"
"ds_read_b64_tr_b16 a[36:37], v145 offset:8704\n"
"v_mfma_f32_16x16x32_bf16 v[44:47], a[0:3], a[76:79], v[44:47]\n"
"ds_read_b64_tr_b16 a[38:39], v145 offset:8960\n"
"v_mfma_f32_16x16x32_bf16 v[60:63], a[4:7], a[76:79], v[60:63]\n"
"s_add_u32 m0, s68, 40960\n"
"ds_read_b64_tr_b16 a[40:41], v144 offset:9216\n"
"v_mfma_f32_16x16x32_bf16 v[56:59], a[4:7], a[72:75], v[56:59]\n"
"s_add_u32 s69, s89, 65536\n"
"ds_read_b64_tr_b16 a[42:43], v144 offset:9472\n"
"buffer_load_dwordx4 v150, s[36:39], s69 offen offset:0 lds\n"
"v_mfma_f32_16x16x32_bf16 v[52:55], a[4:7], a[68:71], v[52:55]\n"
"ds_read_b64_tr_b16 a[44:45], v145 offset:9216\n"
"v_mfma_f32_16x16x32_bf16 v[48:51], a[4:7], a[64:67], v[48:51]\n"
"ds_read_b64_tr_b16 a[46:47], v145 offset:9472\n"
"v_mfma_f32_16x16x32_bf16 v[64:67], a[8:11], a[64:67], v[64:67]\n"
"ds_read_b64_tr_b16 a[48:49], v144 offset:9728\n"
"v_mfma_f32_16x16x32_bf16 v[68:71], a[8:11], a[68:71], v[68:71]\n"
"ds_read_b64_tr_b16 a[50:51], v144 offset:9984\n"
"v_mfma_f32_16x16x32_bf16 v[72:75], a[8:11], a[72:75], v[72:75]\n"
"ds_read_b64_tr_b16 a[52:53], v145 offset:9728\n"
"buffer_load_dwordx4 v151, s[36:39], s69 offen offset:1024 lds\n"
"v_mfma_f32_16x16x32_bf16 v[76:79], a[8:11], a[76:79], v[76:79]\n"
"ds_read_b64_tr_b16 a[54:55], v145 offset:9984\n"
"v_mfma_f32_16x16x32_bf16 v[92:95], a[12:15], a[76:79], v[92:95]\n"
"v_mfma_f32_16x16x32_bf16 v[88:91], a[12:15], a[72:75], v[88:91]\n"
"v_mfma_f32_16x16x32_bf16 v[84:87], a[12:15], a[68:71], v[84:87]\n"
"s_add_u32 m0, s68, 49152\n"
"v_mfma_f32_16x16x32_bf16 v[80:83], a[12:15], a[64:67], v[80:83]\n"
"s_add_u32 s69, s89, 131072\n"
"buffer_load_dwordx4 v150, s[36:39], s69 offen offset:0 lds\n"
"v_mfma_f32_16x16x32_bf16 v[96:99], a[16:19], a[64:67], v[96:99]\n"
"v_mfma_f32_16x16x32_bf16 v[100:103], a[16:19], a[68:71], v[100:103]\n"
"v_mfma_f32_16x16x32_bf16 v[104:107], a[16:19], a[72:75], v[104:107]\n"
"v_mfma_f32_16x16x32_bf16 v[108:111], a[16:19], a[76:79], v[108:111]\n"
"v_mfma_f32_16x16x32_bf16 v[124:127], a[20:23], a[76:79], v[124:127]\n"
"buffer_load_dwordx4 v151, s[36:39], s69 offen offset:1024 lds\n"
"v_mfma_f32_16x16x32_bf16 v[120:123], a[20:23], a[72:75], v[120:123]\n"
"v_mfma_f32_16x16x32_bf16 v[116:119], a[20:23], a[68:71], v[116:119]\n"
"v_mfma_f32_16x16x32_bf16 v[112:115], a[20:23], a[64:67], v[112:115]\n"
"s_waitcnt lgkmcnt(0)\n"
"s_waitcnt vmcnt(14)\n"
"v_mfma_f32_16x16x32_bf16 v[32:35], a[32:35], a[80:83], v[32:35]\n"
"s_add_u32 m0, s68, 57344\n"
"ds_read_b64_tr_b16 a[0:1], v144 offset:16896\n"
"v_mfma_f32_16x16x32_bf16 v[36:39], a[32:35], a[84:87], v[36:39]\n"
"s_add_u32 s69, s89, 196608\n"
"ds_read_b64_tr_b16 a[2:3], v144 offset:17152\n"
"buffer_load_dwordx4 v150, s[36:39], s69 offen offset:0 lds\n"
"v_mfma_f32_16x16x32_bf16 v[40:43], a[32:35], a[88:91], v[40:43]\n"
"ds_read_b64_tr_b16 a[4:5], v145 offset:16896\n"
"v_mfma_f32_16x16x32_bf16 v[44:47], a[32:35], a[92:95], v[44:47]\n"
"ds_read_b64_tr_b16 a[6:7], v145 offset:17152\n"
"v_mfma_f32_16x16x32_bf16 v[60:63], a[36:39], a[92:95], v[60:63]\n"
"ds_read_b64_tr_b16 a[8:9], v144 offset:17408\n"
"v_mfma_f32_16x16x32_bf16 v[56:59], a[36:39], a[88:91], v[56:59]\n"
"ds_read_b64_tr_b16 a[10:11], v144 offset:17664\n"
"v_mfma_f32_16x16x32_bf16 v[52:55], a[36:39], a[84:87], v[52:55]\n"
"ds_read_b64_tr_b16 a[12:13], v145 offset:17408\n"
"buffer_load_dwordx4 v151, s[36:39], s69 offen offset:1024 lds\n"
"v_mfma_f32_16x16x32_bf16 v[48:51], a[36:39], a[80:83], v[48:51]\n"
"ds_read_b64_tr_b16 a[14:15], v145 offset:17664\n"
"v_mfma_f32_16x16x32_bf16 v[64:67], a[40:43], a[80:83], v[64:67]\n"
"ds_read_b64_tr_b16 a[16:17], v144 offset:17920\n"
"v_mfma_f32_16x16x32_bf16 v[68:71], a[40:43], a[84:87], v[68:71]\n"
"ds_read_b64_tr_b16 a[18:19], v144 offset:18176\n"
"v_mfma_f32_16x16x32_bf16 v[72:75], a[40:43], a[88:91], v[72:75]\n"
"ds_read_b64_tr_b16 a[20:21], v145 offset:17920\n"
"v_mfma_f32_16x16x32_bf16 v[76:79], a[40:43], a[92:95], v[76:79]\n"
"s_cmp_le_u32 s83, 6\ns_cselect_b32 s90, 7, 6\ns_lshl_b32 s90, s90, 12\n"
"s_add_u32 s70, s90, 0\n"
"ds_read_b64_tr_b16 a[22:23], v145 offset:18176\n"
"v_mfma_f32_16x16x32_bf16 v[92:95], a[44:47], a[92:95], v[92:95]\n"
"buffer_load_dwordx4 a[128:131], v152, s[40:43], s70 offen offset:0 sc1\n"
"v_mfma_f32_16x16x32_bf16 v[88:91], a[44:47], a[88:91], v[88:91]\n"
"v_mfma_f32_16x16x32_bf16 v[84:87], a[44:47], a[84:87], v[84:87]\n"
"v_mfma_f32_16x16x32_bf16 v[80:83], a[44:47], a[80:83], v[80:83]\n"
"v_mfma_f32_16x16x32_bf16 v[96:99], a[48:51], a[80:83], v[96:99]\n"
"v_mfma_f32_16x16x32_bf16 v[100:103], a[48:51], a[84:87], v[100:103]\n"
"s_add_u32 s70, s90, 131072\n"
"buffer_load_dwordx4 a[132:135], v152, s[40:43], s70 offen offset:0 sc1\n"
"v_mfma_f32_16x16x32_bf16 v[104:107], a[48:51], a[88:91], v[104:107]\n"
"v_mfma_f32_16x16x32_bf16 v[108:111], a[48:51], a[92:95], v[108:111]\n"
"v_mfma_f32_16x16x32_bf16 v[124:127], a[52:55], a[92:95], v[124:127]\n"
"v_mfma_f32_16x16x32_bf16 v[120:123], a[52:55], a[88:91], v[120:123]\n"
"v_mfma_f32_16x16x32_bf16 v[116:119], a[52:55], a[84:87], v[116:119]\n"
"s_add_u32 s70, s90, 262144\n"
"buffer_load_dwordx4 a[136:139], v152, s[40:43], s70 offen offset:0 sc1\n"
"v_mfma_f32_16x16x32_bf16 v[112:115], a[52:55], a[80:83], v[112:115]\n"
"s_waitcnt lgkmcnt(0)\n"
"s_waitcnt vmcnt(15)\n"
"v_mfma_f32_16x16x32_bf16 v[32:35], a[0:3], a[96:99], v[32:35]\n"
"ds_read_b64_tr_b16 a[32:33], v144 offset:25088\n"
"v_mfma_f32_16x16x32_bf16 v[36:39], a[0:3], a[100:103], v[36:39]\n"
"ds_read_b64_tr_b16 a[34:35], v144 offset:25344\n"
"v_mfma_f32_16x16x32_bf16 v[40:43], a[0:3], a[104:107], v[40:43]\n"
"ds_read_b64_tr_b16 a[36:37], v145 offset:25088\n"
"v_mfma_f32_16x16x32_bf16 v[44:47], a[0:3], a[108:111], v[44:47]\n"
"s_add_u32 s70, s90, 393216\n"
"ds_read_b64_tr_b16 a[38:39], v145 offset:25344\n"
"buffer_load_dwordx4 a[140:143], v152, s[40:43], s70 offen offset:0 sc1\n"
"v_mfma_f32_16x16x32_bf16 v[60:63], a[4:7], a[108:111], v[60:63]\n"
"ds_read_b64_tr_b16 a[40:41], v144 offset:25600\n"
"v_mfma_f32_16x16x32_bf16 v[56:59], a[4:7], a[104:107], v[56:59]\n"
"ds_read_b64_tr_b16 a[42:43], v144 offset:25856\n"
"v_mfma_f32_16x16x32_bf16 v[52:55], a[4:7], a[100:103], v[52:55]\n"
"ds_read_b64_tr_b16 a[44:45], v145 offset:25600\n"
"v_mfma_f32_16x16x32_bf16 v[48:51], a[4:7], a[96:99], v[48:51]\n"
"ds_read_b64_tr_b16 a[46:47], v145 offset:25856\n"
"v_mfma_f32_16x16x32_bf16 v[64:67], a[8:11], a[96:99], v[64:67]\n"
"s_add_u32 s70, s90, 0\n"
"ds_read_b64_tr_b16 a[48:49], v144 offset:26112\n"
"buffer_load_dwordx4 a[144:147], v152, s[40:43], s70 offen offset:1024 sc1\n"
"v_mfma_f32_16x16x32_bf16 v[68:71], a[8:11], a[100:103], v[68:71]\n"
"ds_read_b64_tr_b16 a[50:51], v144 offset:26368\n"
"v_mfma_f32_16x16x32_bf16 v[72:75], a[8:11], a[104:107], v[72:75]\n"
"ds_read_b64_tr_b16 a[52:53], v145 offset:26112\n"
"v_mfma_f32_16x16x32_bf16 v[76:79], a[8:11], a[108:111], v[76:79]\n"
"ds_read_b64_tr_b16 a[54:55], v145 offset:26368\n"
"v_mfma_f32_16x16x32_bf16 v[92:95], a[12:15], a[108:111], v[92:95]\n"
"v_mfma_f32_16x16x32_bf16 v[88:91], a[12:15], a[104:107], v[88:91]\n"
"s_add_u32 s70, s90, 131072\n"
"buffer_load_dwordx4 a[148:151], v152, s[40:43], s70 offen offset:1024 sc1\n"
"v_mfma_f32_16x16x32_bf16 v[84:87], a[12:15], a[100:103], v[84:87]\n"
"v_mfma_f32_16x16x32_bf16 v[80:83], a[12:15], a[96:99], v[80:83]\n"
"v_mfma_f32_16x16x32_bf16 v[96:99], a[16:19], a[96:99], v[96:99]\n"
"v_mfma_f32_16x16x32_bf16 v[100:103], a[16:19], a[100:103], v[100:103]\n"
"v_mfma_f32_16x16x32_bf16 v[104:107], a[16:19], a[104:107], v[104:107]\n"
"s_add_u32 s70, s90, 262144\n"
"buffer_load_dwordx4 a[152:155], v152, s[40:43], s70 offen offset:1024 sc1\n"
"v_mfma_f32_16x16x32_bf16 v[108:111], a[16:19], a[108:111], v[108:111]\n"
"v_mfma_f32_16x16x32_bf16 v[124:127], a[20:23], a[108:111], v[124:127]\n"
"v_mfma_f32_16x16x32_bf16 v[120:123], a[20:23], a[104:107], v[120:123]\n"
"v_mfma_f32_16x16x32_bf16 v[116:119], a[20:23], a[100:103], v[116:119]\n"
"v_mfma_f32_16x16x32_bf16 v[112:115], a[20:23], a[96:99], v[112:115]\n"
"s_add_u32 s70, s90, 393216\n"
"buffer_load_dwordx4 a[156:159], v152, s[40:43], s70 offen offset:1024 sc1\n"
"s_waitcnt lgkmcnt(0)\n"
"s_waitcnt vmcnt(16)\n"
"s_waitcnt vmcnt(8)\n"
"s_barrier\n"
"v_mfma_f32_16x16x32_bf16 v[32:35], a[32:35], a[112:115], v[32:35]\n"
"ds_read_b64_tr_b16 a[0:1], v144 offset:33280\n"
"v_mfma_f32_16x16x32_bf16 v[36:39], a[32:35], a[116:119], v[36:39]\n"
"ds_read_b64_tr_b16 a[2:3], v144 offset:33536\n"
"v_mfma_f32_16x16x32_bf16 v[40:43], a[32:35], a[120:123], v[40:43]\n"
"ds_read_b64_tr_b16 a[4:5], v145 offset:33280\n"
"v_mfma_f32_16x16x32_bf16 v[44:47], a[32:35], a[124:127], v[44:47]\n"
"ds_read_b64_tr_b16 a[6:7], v145 offset:33536\n"
"v_mfma_f32_16x16x32_bf16 v[60:63], a[36:39], a[124:127], v[60:63]\n"
"s_add_u32 s70, s90, 0\n"
"ds_read_b64_tr_b16 a[8:9], v144 offset:41472\n"
"buffer_load_dwordx4 a[160:163], v152, s[40:43], s70 offen offset:2048 sc1\n"
"v_mfma_f32_16x16x32_bf16 v[56:59], a[36:39], a[120:123], v[56:59]\n"
"ds_read_b64_tr_b16 a[10:11], v144 offset:41728\n"
"v_mfma_f32_16x16x32_bf16 v[52:55], a[36:39], a[116:119], v[52:55]\n"
"ds_read_b64_tr_b16 a[12:13], v145 offset:41472\n"
"v_mfma_f32_16x16x32_bf16 v[48:51], a[36:39], a[112:115], v[48:51]\n"
"ds_read_b64_tr_b16 a[14:15], v145 offset:41728\n"
"v_mfma_f32_16x16x32_bf16 v[64:67], a[40:43], a[112:115], v[64:67]\n"
"ds_read_b64_tr_b16 a[16:17], v144 offset:49664\n"
"v_mfma_f32_16x16x32_bf16 v[68:71], a[40:43], a[116:119], v[68:71]\n"
"s_add_u32 s70, s90, 131072\n"
"ds_read_b64_tr_b16 a[18:19], v144 offset:49920\n"
"buffer_load_dwordx4 a[164:167], v152, s[40:43], s70 offen offset:2048 sc1\n"
"v_mfma_f32_16x16x32_bf16 v[72:75], a[40:43], a[120:123], v[72:75]\n"
"ds_read_b64_tr_b16 a[20:21], v145 offset:49664\n"
"v_mfma_f32_16x16x32_bf16 v[76:79], a[40:43], a[124:127], v[76:79]\n"
"ds_read_b64_tr_b16 a[22:23], v145 offset:49920\n"
"v_mfma_f32_16x16x32_bf16 v[92:95], a[44:47], a[124:127], v[92:95]\n"
"ds_read_b64_tr_b16 a[24:25], v144 offset:57856\n"
"v_mfma_f32_16x16x32_bf16 v[88:91], a[44:47], a[120:123], v[88:91]\n"
"ds_read_b64_tr_b16 a[26:27], v144 offset:58112\n"
"v_mfma_f32_16x16x32_bf16 v[84:87], a[44:47], a[116:119], v[84:87]\n"
"s_add_u32 s70, s90, 262144\n"
"ds_read_b64_tr_b16 a[28:29], v145 offset:57856\n"
"buffer_load_dwordx4 a[168:171], v152, s[40:43], s70 offen offset:2048 sc1\n"
"v_mfma_f32_16x16x32_bf16 v[80:83], a[44:47], a[112:115], v[80:83]\n"
"ds_read_b64_tr_b16 a[30:31], v145 offset:58112\n"
"v_mfma_f32_16x16x32_bf16 v[96:99], a[48:51], a[112:115], v[96:99]\n"
"v_mfma_f32_16x16x32_bf16 v[100:103], a[48:51], a[116:119], v[100:103]\n"
"v_mfma_f32_16x16x32_bf16 v[104:107], a[48:51], a[120:123], v[104:107]\n"
"v_mfma_f32_16x16x32_bf16 v[108:111], a[48:51], a[124:127], v[108:111]\n"
"s_add_u32 s70, s90, 393216\n"
"buffer_load_dwordx4 a[172:175], v152, s[40:43], s70 offen offset:2048 sc1\n"
"v_mfma_f32_16x16x32_bf16 v[124:127], a[52:55], a[124:127], v[124:127]\n"
"v_mfma_f32_16x16x32_bf16 v[120:123], a[52:55], a[120:123], v[120:123]\n"
"v_mfma_f32_16x16x32_bf16 v[116:119], a[52:55], a[116:119], v[116:119]\n"
"v_mfma_f32_16x16x32_bf16 v[112:115], a[52:55], a[112:115], v[112:115]\n"
"s_waitcnt lgkmcnt(0)\n"
"s_waitcnt vmcnt(8)\n"
"v_mfma_f32_16x16x32_bf16 v[32:35], a[0:3], a[128:131], v[32:35]\n"
"s_add_u32 s70, s90, 0\n"
"ds_read_b64_tr_b16 a[32:33], v144 offset:33792\n"
"buffer_load_dwordx4 a[176:179], v152, s[40:43], s70 offen offset:3072 sc1\n"
"v_mfma_f32_16x16x32_bf16 v[36:39], a[0:3], a[132:135], v[36:39]\n"
"ds_read_b64_tr_b16 a[34:35], v144 offset:34048\n"
"v_mfma_f32_16x16x32_bf16 v[40:43], a[0:3], a[136:139], v[40:43]\n"
"ds_read_b64_tr_b16 a[36:37], v145 offset:33792\n"
"v_mfma_f32_16x16x32_bf16 v[44:47], a[0:3], a[140:143], v[44:47]\n"
"ds_read_b64_tr_b16 a[38:39], v145 offset:34048\n"
"v_mfma_f32_16x16x32_bf16 v[60:63], a[4:7], a[140:143], v[60:63]\n"
"ds_read_b64_tr_b16 a[40:41], v144 offset:41984\n"
"v_mfma_f32_16x16x32_bf16 v[56:59], a[4:7], a[136:139], v[56:59]\n"
"s_add_u32 s70, s90, 131072\n"
"ds_read_b64_tr_b16 a[42:43], v144 offset:42240\n"
"buffer_load_dwordx4 a[180:183], v152, s[40:43], s70 offen offset:3072 sc1\n"
"v_mfma_f32_16x16x32_bf16 v[52:55], a[4:7], a[132:135], v[52:55]\n"
"ds_read_b64_tr_b16 a[44:45], v145 offset:41984\n"
"v_mfma_f32_16x16x32_bf16 v[48:51], a[4:7], a[128:131], v[48:51]\n"
"ds_read_b64_tr_b16 a[46:47], v145 offset:42240\n"
"s_waitcnt vmcnt(6)\n"
"v_mfma_f32_16x16x32_bf16 v[32:35], a[8:11], a[144:147], v[32:35]\n"
"ds_read_b64_tr_b16 a[48:49], v144 offset:50176\n"
"v_mfma_f32_16x16x32_bf16 v[36:39], a[8:11], a[148:151], v[36:39]\n"
"ds_read_b64_tr_b16 a[50:51], v144 offset:50432\n"
"v_mfma_f32_16x16x32_bf16 v[40:43], a[8:11], a[152:155], v[40:43]\n"
"s_add_u32 s70, s90, 262144\n"
"ds_read_b64_tr_b16 a[52:53], v145 offset:50176\n"
"buffer_load_dwordx4 a[184:187], v152, s[40:43], s70 offen offset:3072 sc1\n"
"v_mfma_f32_16x16x32_bf16 v[44:47], a[8:11], a[156:159], v[44:47]\n"
"ds_read_b64_tr_b16 a[54:55], v145 offset:50432\n"
"v_mfma_f32_16x16x32_bf16 v[60:63], a[12:15], a[156:159], v[60:63]\n"
"ds_read_b64_tr_b16 a[56:57], v144 offset:58368\n"
"v_mfma_f32_16x16x32_bf16 v[56:59], a[12:15], a[152:155], v[56:59]\n"
"ds_read_b64_tr_b16 a[58:59], v144 offset:58624\n"
"v_mfma_f32_16x16x32_bf16 v[52:55], a[12:15], a[148:151], v[52:55]\n"
"ds_read_b64_tr_b16 a[60:61], v145 offset:58368\n"
"v_mfma_f32_16x16x32_bf16 v[48:51], a[12:15], a[144:147], v[48:51]\n"
"s_add_u32 s70, s90, 393216\n"
"ds_read_b64_tr_b16 a[62:63], v145 offset:58624\n"
"buffer_load_dwordx4 a[188:191], v152, s[40:43], s70 offen offset:3072 sc1\n"
"s_waitcnt vmcnt(4)\n"
"v_mfma_f32_16x16x32_bf16 v[32:35], a[16:19], a[160:163], v[32:35]\n"
"v_mfma_f32_16x16x32_bf16 v[36:39], a[16:19], a[164:167], v[36:39]\n"
"v_mfma_f32_16x16x32_bf16 v[40:43], a[16:19], a[168:171], v[40:43]\n"
"v_mfma_f32_16x16x32_bf16 v[44:47], a[16:19], a[172:175], v[44:47]\n"
"v_mfma_f32_16x16x32_bf16 v[60:63], a[20:23], a[172:175], v[60:63]\n"
"v_mfma_f32_16x16x32_bf16 v[56:59], a[20:23], a[168:171], v[56:59]\n"
"v_and_b32 v155, 3, v158\n"
"v_lshrrev_b32 v251, 2, v158\n"
"v_mfma_f32_16x16x32_bf16 v[52:55], a[20:23], a[164:167], v[52:55]\n"
"v_mul_u32_u24 v251, 80, v251\n"
"v_lshl_add_u32 v251, v155, 4, v251\n"
"v_mfma_f32_16x16x32_bf16 v[48:51], a[20:23], a[160:163], v[48:51]\n"
"s_lshl_b32 s71, s79, 13\n"
"s_add_u32 s71, s71, 65536\n"
"s_waitcnt vmcnt(0)\n"
"v_mfma_f32_16x16x32_bf16 v[32:35], a[24:27], a[176:179], v[32:35]\n"
"v_add_u32 v251, s71, v251\n"
"v_and_b32 v250, 15, v158\n"
"v_mfma_f32_16x16x32_bf16 v[36:39], a[24:27], a[180:183], v[36:39]\n"
"v_mul_u32_u24 v250, 80, v250\n"
"v_lshrrev_b32 v254, 4, v158\n"
"v_mfma_f32_16x16x32_bf16 v[40:43], a[24:27], a[184:187], v[40:43]\n"
"v_lshl_add_u32 v250, v254, 3, v250\n"
"v_add_u32 v250, s71, v250\n"
"v_mfma_f32_16x16x32_bf16 v[44:47], a[24:27], a[188:191], v[44:47]\n"
"v_max_u32 v155, 1, v155\n"
"v_lshlrev_b32 v155, 6, v155\n"
"v_mfma_f32_16x16x32_bf16 v[60:63], a[28:31], a[188:191], v[60:63]\n"
"v_mfma_f32_16x16x32_bf16 v[56:59], a[28:31], a[184:187], v[56:59]\n"
"v_mfma_f32_16x16x32_bf16 v[52:55], a[28:31], a[180:183], v[52:55]\n"
"v_mfma_f32_16x16x32_bf16 v[48:51], a[28:31], a[176:179], v[48:51]\n"
"s_waitcnt lgkmcnt(0)\n"
"v_mfma_f32_16x16x32_bf16 v[64:67], a[32:35], a[128:131], v[64:67]\n"
"ds_read_b64_tr_b16 a[0:1], v144 offset:34304\n"
"FK_TAIL:\n"
"v_mfma_f32_16x16x32_bf16 v[68:71], a[32:35], a[132:135], v[68:71]\n"
"ds_read_b64_tr_b16 a[2:3], v144 offset:34560\n"
"v_mfma_f32_16x16x32_bf16 v[72:75], a[32:35], a[136:139], v[72:75]\n"
"s_xor_b32 s69, s29, 65536\ns_add_u32 s69, s69, 4096\n"
"ds_read_b64_tr_b16 a[4:5], v145 offset:34304\n"
"v_cvt_pk_bf16_f32 v44, v44, v45\n"
"v_mfma_f32_16x16x32_bf16 v[76:79], a[32:35], a[140:143], v[76:79]\n"
"ds_read_b64_tr_b16 a[6:7], v145 offset:34560\n"
"v_cvt_pk_bf16_f32 v45, v46, v47\n"
"v_mfma_f32_16x16x32_bf16 v[92:95], a[36:39], a[140:143], v[92:95]\n"
"ds_read_b64_tr_b16 a[8:9], v144 offset:42496\n"
"v_cvt_pk_bf16_f32 v46, v60, v61\n"
"v_mfma_f32_16x16x32_bf16 v[88:91], a[36:39], a[136:139], v[88:91]\n"
"ds_read_b64_tr_b16 a[10:11], v144 offset:42752\n"
"v_cvt_pk_bf16_f32 v47, v62, v63\n"
"v_mfma_f32_16x16x32_bf16 v[84:87], a[36:39], a[132:135], v[84:87]\n"
"ds_read_b64_tr_b16 a[12:13], v145 offset:42496\n"
"buffer_store_dwordx4 v[44:47], v152, s[60:63], s69 offen offset:3072 sc1\n"
"v_mfma_f32_16x16x32_bf16 v[80:83], a[36:39], a[128:131], v[80:83]\n"
"ds_read_b64_tr_b16 a[14:15], v145 offset:42752\n"
"v_cvt_pk_bf16_f32 v40, v40, v41\n"
"v_mfma_f32_16x16x32_bf16 v[64:67], a[40:43], a[144:147], v[64:67]\n"
"ds_read_b64_tr_b16 a[16:17], v144 offset:50688\n"
"v_cvt_pk_bf16_f32 v41, v42, v43\n"
"v_mfma_f32_16x16x32_bf16 v[68:71], a[40:43], a[148:151], v[68:71]\n"
"ds_read_b64_tr_b16 a[18:19], v144 offset:50944\n"
"v_cvt_pk_bf16_f32 v42, v56, v57\n"
"v_mfma_f32_16x16x32_bf16 v[72:75], a[40:43], a[152:155], v[72:75]\n"
"ds_read_b64_tr_b16 a[20:21], v145 offset:50688\n"
"v_cvt_pk_bf16_f32 v43, v58, v59\n"
"v_mfma_f32_16x16x32_bf16 v[76:79], a[40:43], a[156:159], v[76:79]\n"
"ds_read_b64_tr_b16 a[22:23], v145 offset:50944\n"
"buffer_store_dwordx4 v[40:43], v152, s[60:63], s69 offen offset:2048 sc1\n"
"v_mfma_f32_16x16x32_bf16 v[92:95], a[44:47], a[156:159], v[92:95]\n"
"ds_read_b64_tr_b16 a[24:25], v144 offset:58880\n"
"v_cvt_pk_bf16_f32 v36, v36, v37\n"
"v_mfma_f32_16x16x32_bf16 v[88:91], a[44:47], a[152:155], v[88:91]\n"
"ds_read_b64_tr_b16 a[26:27], v144 offset:59136\n"
"v_cvt_pk_bf16_f32 v37, v38, v39\n"
"v_mfma_f32_16x16x32_bf16 v[84:87], a[44:47], a[148:151], v[84:87]\n"
"ds_read_b64_tr_b16 a[28:29], v145 offset:58880\n"
"v_cvt_pk_bf16_f32 v38, v52, v53\n"
"v_mfma_f32_16x16x32_bf16 v[80:83], a[44:47], a[144:147], v[80:83]\n"
"ds_read_b64_tr_b16 a[30:31], v145 offset:59136\n"
"v_cvt_pk_bf16_f32 v39, v54, v55\n"
"v_mfma_f32_16x16x32_bf16 v[64:67], a[48:51], a[160:163], v[64:67]\n"
"buffer_store_dwordx4 v[36:39], v152, s[60:63], s69 offen offset:1024 sc1\n"
"v_cvt_pk_bf16_f32 v32, v32, v33\n"
"v_mfma_f32_16x16x32_bf16 v[68:71], a[48:51], a[164:167], v[68:71]\n"
"v_cvt_pk_bf16_f32 v33, v34, v35\n"
"v_cvt_pk_bf16_f32 v34, v48, v49\n"
"v_mfma_f32_16x16x32_bf16 v[72:75], a[48:51], a[168:171], v[72:75]\n"
"v_cvt_pk_bf16_f32 v35, v50, v51\n"
"v_mfma_f32_16x16x32_bf16 v[76:79], a[48:51], a[172:175], v[76:79]\n"
"buffer_store_dwordx4 v[32:35], v152, s[60:63], s69 offen offset:0 sc1\n"
"v_mfma_f32_16x16x32_bf16 v[92:95], a[52:55], a[172:175], v[92:95]\n"
"v_mfma_f32_16x16x32_bf16 v[88:91], a[52:55], a[168:171], v[88:91]\n"
"v_mfma_f32_16x16x32_bf16 v[84:87], a[52:55], a[164:167], v[84:87]\n"
"v_mfma_f32_16x16x32_bf16 v[80:83], a[52:55], a[160:163], v[80:83]\n"
"v_mfma_f32_16x16x32_bf16 v[64:67], a[56:59], a[176:179], v[64:67]\n"
"v_mfma_f32_16x16x32_bf16 v[68:71], a[56:59], a[180:183], v[68:71]\n"
"v_mfma_f32_16x16x32_bf16 v[72:75], a[56:59], a[184:187], v[72:75]\n"
"v_mfma_f32_16x16x32_bf16 v[76:79], a[56:59], a[188:191], v[76:79]\n"
"v_mfma_f32_16x16x32_bf16 v[92:95], a[60:63], a[188:191], v[92:95]\n"
"v_mfma_f32_16x16x32_bf16 v[88:91], a[60:63], a[184:187], v[88:91]\n"
"v_mfma_f32_16x16x32_bf16 v[84:87], a[60:63], a[180:183], v[84:87]\n"
"v_mfma_f32_16x16x32_bf16 v[80:83], a[60:63], a[176:179], v[80:83]\n"
"s_waitcnt lgkmcnt(0)\n"
"v_mfma_f32_16x16x32_bf16 v[96:99], a[0:3], a[128:131], v[96:99]\n"
"ds_read_b64_tr_b16 a[32:33], v146 offset:32768\n"
"v_mfma_f32_16x16x32_bf16 v[100:103], a[0:3], a[132:135], v[100:103]\n"
"ds_read_b64_tr_b16 a[34:35], v146 offset:33024\n"
"v_mfma_f32_16x16x32_bf16 v[104:107], a[0:3], a[136:139], v[104:107]\n"
"s_xor_b32 s69, s29, 131072\ns_add_u32 s69, s69, 8192\n"
"ds_read_b64_tr_b16 a[36:37], v147 offset:32768\n"
"v_cvt_pk_bf16_f32 v76, v76, v77\n"
"v_mfma_f32_16x16x32_bf16 v[108:111], a[0:3], a[140:143], v[108:111]\n"
"ds_read_b64_tr_b16 a[38:39], v147 offset:33024\n"
"v_cvt_pk_bf16_f32 v77, v78, v79\n"
"v_mfma_f32_16x16x32_bf16 v[124:127], a[4:7], a[140:143], v[124:127]\n"
"ds_read_b64_tr_b16 a[40:41], v146 offset:40960\n"
"v_cvt_pk_bf16_f32 v78, v92, v93\n"
"v_mfma_f32_16x16x32_bf16 v[120:123], a[4:7], a[136:139], v[120:123]\n"
"ds_read_b64_tr_b16 a[42:43], v146 offset:41216\n"
"v_cvt_pk_bf16_f32 v79, v94, v95\n"
"v_mfma_f32_16x16x32_bf16 v[116:119], a[4:7], a[132:135], v[116:119]\n"
"ds_read_b64_tr_b16 a[44:45], v147 offset:40960\n"
"buffer_store_dwordx4 v[76:79], v152, s[60:63], s69 offen offset:3072 sc1\n"
"v_mfma_f32_16x16x32_bf16 v[112:115], a[4:7], a[128:131], v[112:115]\n"
"ds_read_b64_tr_b16 a[46:47], v147 offset:41216\n"
"v_cvt_pk_bf16_f32 v72, v72, v73\n"
"v_mfma_f32_16x16x32_bf16 v[96:99], a[8:11], a[144:147], v[96:99]\n"
"ds_read_b64_tr_b16 a[48:49], v146 offset:49152\n"
"v_cvt_pk_bf16_f32 v73, v74, v75\n"
"v_mfma_f32_16x16x32_bf16 v[100:103], a[8:11], a[148:151], v[100:103]\n"
"ds_read_b64_tr_b16 a[50:51], v146 offset:49408\n"
"v_cvt_pk_bf16_f32 v74, v88, v89\n"
"v_mfma_f32_16x16x32_bf16 v[104:107], a[8:11], a[152:155], v[104:107]\n"
"ds_read_b64_tr_b16 a[52:53], v147 offset:49152\n"
"v_cvt_pk_bf16_f32 v75, v90, v91\n"
"v_mfma_f32_16x16x32_bf16 v[108:111], a[8:11], a[156:159], v[108:111]\n"
"ds_read_b64_tr_b16 a[54:55], v147 offset:49408\n"
"buffer_store_dwordx4 v[72:75], v152, s[60:63], s69 offen offset:2048 sc1\n"
"v_mfma_f32_16x16x32_bf16 v[124:127], a[12:15], a[156:159], v[124:127]\n"
"ds_read_b64_tr_b16 a[56:57], v146 offset:57344\n"
"v_cvt_pk_bf16_f32 v68, v68, v69\n"
"v_mfma_f32_16x16x32_bf16 v[120:123], a[12:15], a[152:155], v[120:123]\n"
"ds_read_b64_tr_b16 a[58:59], v146 offset:57600\n"
"v_cvt_pk_bf16_f32 v69, v70, v71\n"
"v_mfma_f32_16x16x32_bf16 v[116:119], a[12:15], a[148:151], v[116:119]\n"
"ds_read_b64_tr_b16 a[60:61], v147 offset:57344\n"
"v_cvt_pk_bf16_f32 v70, v84, v85\n"
"v_mfma_f32_16x16x32_bf16 v[112:115], a[12:15], a[144:147], v[112:115]\n"
"ds_read_b64_tr_b16 a[62:63], v147 offset:57600\n"
"v_cvt_pk_bf16_f32 v71, v86, v87\n"
"v_mfma_f32_16x16x32_bf16 v[96:99], a[16:19], a[160:163], v[96:99]\n"
"buffer_store_dwordx4 v[68:71], v152, s[60:63], s69 offen offset:1024 sc1\n"
"v_cvt_pk_bf16_f32 v64, v64, v65\n"
"v_mfma_f32_16x16x32_bf16 v[100:103], a[16:19], a[164:167], v[100:103]\n"
"v_cvt_pk_bf16_f32 v65, v66, v67\n"
"v_cvt_pk_bf16_f32 v66, v80, v81\n"
"v_mfma_f32_16x16x32_bf16 v[104:107], a[16:19], a[168:171], v[104:107]\n"
"v_cvt_pk_bf16_f32 v67, v82, v83\n"
"v_mfma_f32_16x16x32_bf16 v[108:111], a[16:19], a[172:175], v[108:111]\n"
"buffer_store_dwordx4 v[64:67], v152, s[60:63], s69 offen offset:0 sc1\n"
"v_mfma_f32_16x16x32_bf16 v[124:127], a[20:23], a[172:175], v[124:127]\n"
"v_mfma_f32_16x16x32_bf16 v[120:123], a[20:23], a[168:171], v[120:123]\n"
"v_mfma_f32_16x16x32_bf16 v[116:119], a[20:23], a[164:167], v[116:119]\n"
"v_mfma_f32_16x16x32_bf16 v[112:115], a[20:23], a[160:163], v[112:115]\n"
"v_mfma_f32_16x16x32_bf16 v[96:99], a[24:27], a[176:179], v[96:99]\n"
"v_mfma_f32_16x16x32_bf16 v[100:103], a[24:27], a[180:183], v[100:103]\n"
"v_mfma_f32_16x16x32_bf16 v[104:107], a[24:27], a[184:187], v[104:107]\n"
"v_mfma_f32_16x16x32_bf16 v[108:111], a[24:27], a[188:191], v[108:111]\n"
"s_waitcnt vmcnt(4)\n"
"s_xor_b32 s73, s29, 65536\n"
"buffer_store_dword v153, v154, s[60:63], s73 offen offset:64 sc1\n"
"v_mfma_f32_16x16x32_bf16 v[124:127], a[28:31], a[188:191], v[124:127]\n"
"v_mfma_f32_16x16x32_bf16 v[120:123], a[28:31], a[184:187], v[120:123]\n"
"v_mfma_f32_16x16x32_bf16 v[116:119], a[28:31], a[180:183], v[116:119]\n"
"v_mfma_f32_16x16x32_bf16 v[112:115], a[28:31], a[176:179], v[112:115]\n"
"s_waitcnt lgkmcnt(0)\n"
"v_mfma_f32_16x16x32_bf16 v[0:3], a[32:35], a[192:195], v[0:3]\n"
"ds_read_b64_tr_b16 a[0:1], v144 offset:0\n"
"v_mfma_f32_16x16x32_bf16 v[4:7], a[32:35], a[196:199], v[4:7]\n"
"ds_read_b64_tr_b16 a[2:3], v144 offset:256\n"
"v_mfma_f32_16x16x32_bf16 v[8:11], a[32:35], a[200:203], v[8:11]\n"
"s_xor_b32 s69, s29, 196608\ns_add_u32 s69, s69, 12288\n"
"ds_read_b64_tr_b16 a[4:5], v145 offset:0\n"
"v_cvt_pk_bf16_f32 v108, v108, v109\n"
"v_mfma_f32_16x16x32_bf16 v[12:15], a[32:35], a[204:207], v[12:15]\n"
"ds_read_b64_tr_b16 a[6:7], v145 offset:256\n"
"v_cvt_pk_bf16_f32 v109, v110, v111\n"
"v_mfma_f32_16x16x32_bf16 v[28:31], a[36:39], a[204:207], v[28:31]\n"
"ds_read_b64_tr_b16 a[8:9], v144 offset:8192\n"
"v_cvt_pk_bf16_f32 v110, v124, v125\n"
"v_mfma_f32_16x16x32_bf16 v[24:27], a[36:39], a[200:203], v[24:27]\n"
"ds_read_b64_tr_b16 a[10:11], v144 offset:8448\n"
"v_cvt_pk_bf16_f32 v111, v126, v127\n"
"v_mfma_f32_16x16x32_bf16 v[20:23], a[36:39], a[196:199], v[20:23]\n"
"ds_read_b64_tr_b16 a[12:13], v145 offset:8192\n"
"buffer_store_dwordx4 v[108:111], v152, s[60:63], s69 offen offset:3072 sc1\n"
"v_mfma_f32_16x16x32_bf16 v[16:19], a[36:39], a[192:195], v[16:19]\n"
"ds_read_b64_tr_b16 a[14:15], v145 offset:8448\n"
"v_cvt_pk_bf16_f32 v104, v104, v105\n"
"v_mfma_f32_16x16x32_bf16 v[0:3], a[40:43], a[208:211], v[0:3]\n"
"ds_read_b64_tr_b16 a[16:17], v144 offset:16384\n"
"v_cvt_pk_bf16_f32 v105, v106, v107\n"
"v_mfma_f32_16x16x32_bf16 v[4:7], a[40:43], a[212:215], v[4:7]\n"
"ds_read_b64_tr_b16 a[18:19], v144 offset:16640\n"
"v_cvt_pk_bf16_f32 v106, v120, v121\n"
"v_mfma_f32_16x16x32_bf16 v[8:11], a[40:43], a[216:219], v[8:11]\n"
"ds_read_b64_tr_b16 a[20:21], v145 offset:16384\n"
"v_cvt_pk_bf16_f32 v107, v122, v123\n"
"v_mfma_f32_16x16x32_bf16 v[12:15], a[40:43], a[220:223], v[12:15]\n"
"ds_read_b64_tr_b16 a[22:23], v145 offset:16640\n"
"buffer_store_dwordx4 v[104:107], v152, s[60:63], s69 offen offset:2048 sc1\n"
"v_mfma_f32_16x16x32_bf16 v[28:31], a[44:47], a[220:223], v[28:31]\n"
"ds_read_b64_tr_b16 a[24:25], v144 offset:24576\n"
"v_cvt_pk_bf16_f32 v100, v100, v101\n"
"v_mfma_f32_16x16x32_bf16 v[24:27], a[44:47], a[216:219], v[24:27]\n"
"ds_read_b64_tr_b16 a[26:27], v144 offset:24832\n"
"v_cvt_pk_bf16_f32 v101, v102, v103\n"
"v_mfma_f32_16x16x32_bf16 v[20:23], a[44:47], a[212:215], v[20:23]\n"
"ds_read_b64_tr_b16 a[28:29], v145 offset:24576\n"
"v_cvt_pk_bf16_f32 v102, v116, v117\n"
"v_mfma_f32_16x16x32_bf16 v[16:19], a[44:47], a[208:211], v[16:19]\n"
"ds_read_b64_tr_b16 a[30:31], v145 offset:24832\n"
"v_cvt_pk_bf16_f32 v103, v118, v119\n"
"v_mfma_f32_16x16x32_bf16 v[0:3], a[48:51], a[224:227], v[0:3]\n"
"buffer_store_dwordx4 v[100:103], v152, s[60:63], s69 offen offset:1024 sc1\n"
"v_cvt_pk_bf16_f32 v96, v96, v97\n"
"v_mfma_f32_16x16x32_bf16 v[4:7], a[48:51], a[228:231], v[4:7]\n"
"v_cvt_pk_bf16_f32 v97, v98, v99\n"
"v_cvt_pk_bf16_f32 v98, v112, v113\n"
"v_mfma_f32_16x16x32_bf16 v[8:11], a[48:51], a[232:235], v[8:11]\n"
"v_cvt_pk_bf16_f32 v99, v114, v115\n"
"v_mfma_f32_16x16x32_bf16 v[12:15], a[48:51], a[236:239], v[12:15]\n"
"buffer_store_dwordx4 v[96:99], v152, s[60:63], s69 offen offset:0 sc1\n"
"v_mfma_f32_16x16x32_bf16 v[28:31], a[52:55], a[236:239], v[28:31]\n"
"v_mfma_f32_16x16x32_bf16 v[24:27], a[52:55], a[232:235], v[24:27]\n"
"v_mfma_f32_16x16x32_bf16 v[20:23], a[52:55], a[228:231], v[20:23]\n"
"v_mfma_f32_16x16x32_bf16 v[16:19], a[52:55], a[224:227], v[16:19]\n"
"v_mfma_f32_16x16x32_bf16 v[0:3], a[56:59], a[240:243], v[0:3]\n"
"v_mfma_f32_16x16x32_bf16 v[4:7], a[56:59], a[244:247], v[4:7]\n"
"v_mfma_f32_16x16x32_bf16 v[8:11], a[56:59], a[248:251], v[8:11]\n"
"v_mfma_f32_16x16x32_bf16 v[12:15], a[56:59], a[252:255], v[12:15]\n"
"s_waitcnt vmcnt(5)\n"
"s_xor_b32 s73, s29, 131072\n"
"buffer_store_dword v153, v154, s[60:63], s73 offen offset:128 sc1\n"
"v_mfma_f32_16x16x32_bf16 v[28:31], a[60:63], a[252:255], v[28:31]\n"
"v_mfma_f32_16x16x32_bf16 v[24:27], a[60:63], a[248:251], v[24:27]\n"
"v_mfma_f32_16x16x32_bf16 v[20:23], a[60:63], a[244:247], v[20:23]\n"
"v_mfma_f32_16x16x32_bf16 v[16:19], a[60:63], a[240:243], v[16:19]\n"
"s_waitcnt lgkmcnt(0)\n"
"v_mfma_f32_16x16x32_bf16 v[0:3], a[0:3], a[64:67], v[0:3]\n"
"ds_read_b64_tr_b16 a[32:33], v144 offset:32768\n"
"v_mfma_f32_16x16x32_bf16 v[4:7], a[0:3], a[68:71], v[4:7]\n"
"ds_read_b64_tr_b16 a[34:35], v144 offset:33024\n"
"v_mfma_f32_16x16x32_bf16 v[8:11], a[0:3], a[72:75], v[8:11]\n"
"ds_read_b64_tr_b16 a[36:37], v145 offset:32768\n"
"v_mfma_f32_16x16x32_bf16 v[12:15], a[0:3], a[76:79], v[12:15]\n"
"ds_read_b64_tr_b16 a[38:39], v145 offset:33024\n"
"buffer_load_dword v156, v155, s[60:63], s29 offen offset:0 sc1\n"
"v_mfma_f32_16x16x32_bf16 v[28:31], a[4:7], a[76:79], v[28:31]\n"
"ds_read_b64_tr_b16 a[40:41], v144 offset:40960\n"
"v_mfma_f32_16x16x32_bf16 v[24:27], a[4:7], a[72:75], v[24:27]\n"
"ds_read_b64_tr_b16 a[42:43], v144 offset:41216\n"
"v_mfma_f32_16x16x32_bf16 v[20:23], a[4:7], a[68:71], v[20:23]\n"
"ds_read_b64_tr_b16 a[44:45], v145 offset:40960\n"
"v_mfma_f32_16x16x32_bf16 v[16:19], a[4:7], a[64:67], v[16:19]\n"
"ds_read_b64_tr_b16 a[46:47], v145 offset:41216\n"
"v_mfma_f32_16x16x32_bf16 v[0:3], a[8:11], a[80:83], v[0:3]\n"
"ds_read_b64_tr_b16 a[48:49], v144 offset:49152\n"
"v_mfma_f32_16x16x32_bf16 v[4:7], a[8:11], a[84:87], v[4:7]\n"
"ds_read_b64_tr_b16 a[50:51], v144 offset:49408\n"
"v_mfma_f32_16x16x32_bf16 v[8:11], a[8:11], a[88:91], v[8:11]\n"
"ds_read_b64_tr_b16 a[52:53], v145 offset:49152\n"
"v_mfma_f32_16x16x32_bf16 v[12:15], a[8:11], a[92:95], v[12:15]\n"
"ds_read_b64_tr_b16 a[54:55], v145 offset:49408\n"
"v_mfma_f32_16x16x32_bf16 v[28:31], a[12:15], a[92:95], v[28:31]\n"
"ds_read_b64_tr_b16 a[56:57], v144 offset:57344\n"
"v_mfma_f32_16x16x32_bf16 v[24:27], a[12:15], a[88:91], v[24:27]\n"
"ds_read_b64_tr_b16 a[58:59], v144 offset:57600\n"
"v_mfma_f32_16x16x32_bf16 v[20:23], a[12:15], a[84:87], v[20:23]\n"
"ds_read_b64_tr_b16 a[60:61], v145 offset:57344\n"
"v_mfma_f32_16x16x32_bf16 v[16:19], a[12:15], a[80:83], v[16:19]\n"
"ds_read_b64_tr_b16 a[62:63], v145 offset:57600\n"
"v_mfma_f32_16x16x32_bf16 v[0:3], a[16:19], a[96:99], v[0:3]\n"
"v_mfma_f32_16x16x32_bf16 v[4:7], a[16:19], a[100:103], v[4:7]\n"
"v_mfma_f32_16x16x32_bf16 v[8:11], a[16:19], a[104:107], v[8:11]\n"
"v_mfma_f32_16x16x32_bf16 v[12:15], a[16:19], a[108:111], v[12:15]\n"
"v_mfma_f32_16x16x32_bf16 v[28:31], a[20:23], a[108:111], v[28:31]\n"
"v_mfma_f32_16x16x32_bf16 v[24:27], a[20:23], a[104:107], v[24:27]\n"
"v_mfma_f32_16x16x32_bf16 v[20:23], a[20:23], a[100:103], v[20:23]\n"
"v_mfma_f32_16x16x32_bf16 v[16:19], a[20:23], a[96:99], v[16:19]\n"
"v_mfma_f32_16x16x32_bf16 v[0:3], a[24:27], a[112:115], v[0:3]\n"
"v_mfma_f32_16x16x32_bf16 v[4:7], a[24:27], a[116:119], v[4:7]\n"
"v_mfma_f32_16x16x32_bf16 v[8:11], a[24:27], a[120:123], v[8:11]\n"
"v_mfma_f32_16x16x32_bf16 v[12:15], a[24:27], a[124:127], v[12:15]\n"
"s_waitcnt vmcnt(2)\n"
"s_xor_b32 s73, s29, 196608\n"
"buffer_store_dword v153, v154, s[60:63], s73 offen offset:192 sc1\n"
"s_waitcnt vmcnt(1)\n"
"v_cmp_ne_u32 vcc, s77, v156\n"
"s_nop 4\n"
"s_cbranch_vccnz LS5_%=\n"
"LB5_%=:\n"
"v_mfma_f32_16x16x32_bf16 v[28:31], a[28:31], a[124:127], v[28:31]\n"
"s_add_u32 s70, s29, 4096\n"
"buffer_load_dwordx4 v[48:51], v152, s[60:63], s70 offen offset:0 sc1\n"
"buffer_load_dwordx4 v[52:55], v152, s[60:63], s70 offen offset:1024 sc1\n"
"v_mfma_f32_16x16x32_bf16 v[24:27], a[28:31], a[120:123], v[24:27]\n"
"buffer_load_dwordx4 v[56:59], v152, s[60:63], s70 offen offset:2048 sc1\n"
"buffer_load_dwordx4 v[60:63], v152, s[60:63], s70 offen offset:3072 sc1\n"
"v_mfma_f32_16x16x32_bf16 v[20:23], a[28:31], a[116:119], v[20:23]\n"
"s_add_u32 s70, s29, 8192\n"
"buffer_load_dwordx4 v[80:83], v152, s[60:63], s70 offen offset:0 sc1\n"
"buffer_load_dwordx4 v[84:87], v152, s[60:63], s70 offen offset:1024 sc1\n"
"v_mfma_f32_16x16x32_bf16 v[16:19], a[28:31], a[112:115], v[16:19]\n"
"buffer_load_dwordx4 v[88:91], v152, s[60:63], s70 offen offset:2048 sc1\n"
"buffer_load_dwordx4 v[92:95], v152, s[60:63], s70 offen offset:3072 sc1\n"
"s_waitcnt lgkmcnt(0)\n"
"v_mfma_f32_16x16x32_bf16 v[0:3], a[32:35], a[128:131], v[0:3]\n"
"s_add_u32 s70, s29, 12288\n"
"buffer_load_dwordx4 v[112:115], v152, s[60:63], s70 offen offset:0 sc1\n"
"buffer_load_dwordx4 v[116:119], v152, s[60:63], s70 offen offset:1024 sc1\n"
"v_mfma_f32_16x16x32_bf16 v[4:7], a[32:35], a[132:135], v[4:7]\n"
"buffer_load_dwordx4 v[120:123], v152, s[60:63], s70 offen offset:2048 sc1\n"
"buffer_load_dwordx4 v[124:127], v152, s[60:63], s70 offen offset:3072 sc1\n"
"v_mfma_f32_16x16x32_bf16 v[8:11], a[32:35], a[136:139], v[8:11]\n"
"v_mfma_f32_16x16x32_bf16 v[12:15], a[32:35], a[140:143], v[12:15]\n"
"v_mfma_f32_16x16x32_bf16 v[28:31], a[36:39], a[140:143], v[28:31]\n"
"v_mfma_f32_16x16x32_bf16 v[24:27], a[36:39], a[136:139], v[24:27]\n"
"v_mfma_f32_16x16x32_bf16 v[20:23], a[36:39], a[132:135], v[20:23]\n"
"v_mfma_f32_16x16x32_bf16 v[16:19], a[36:39], a[128:131], v[16:19]\n"
"v_mfma_f32_16x16x32_bf16 v[0:3], a[40:43], a[144:147], v[0:3]\n"
"v_mfma_f32_16x16x32_bf16 v[4:7], a[40:43], a[148:151], v[4:7]\n"
"v_mfma_f32_16x16x32_bf16 v[8:11], a[40:43], a[152:155], v[8:11]\n"
"v_mfma_f32_16x16x32_bf16 v[12:15], a[40:43], a[156:159], v[12:15]\n"
"v_mfma_f32_16x16x32_bf16 v[28:31], a[44:47], a[156:159], v[28:31]\n"
"v_mfma_f32_16x16x32_bf16 v[24:27], a[44:47], a[152:155], v[24:27]\n"
"v_mfma_f32_16x16x32_bf16 v[20:23], a[44:47], a[148:151], v[20:23]\n"
"v_mfma_f32_16x16x32_bf16 v[16:19], a[44:47], a[144:147], v[16:19]\n"
"v_mfma_f32_16x16x32_bf16 v[0:3], a[48:51], a[160:163], v[0:3]\n"
"v_mfma_f32_16x16x32_bf16 v[4:7], a[48:51], a[164:167], v[4:7]\n"
"v_mfma_f32_16x16x32_bf16 v[8:11], a[48:51], a[168:171], v[8:11]\n"
"v_mfma_f32_16x16x32_bf16 v[12:15], a[48:51], a[172:175], v[12:15]\n"
"v_mfma_f32_16x16x32_bf16 v[28:31], a[52:55], a[172:175], v[28:31]\n"
"v_mfma_f32_16x16x32_bf16 v[24:27], a[52:55], a[168:171], v[24:27]\n"
"v_mfma_f32_16x16x32_bf16 v[20:23], a[52:55], a[164:167], v[20:23]\n"
"v_mfma_f32_16x16x32_bf16 v[16:19], a[52:55], a[160:163], v[16:19]\n"
"v_mfma_f32_16x16x32_bf16 v[0:3], a[56:59], a[176:179], v[0:3]\n"
"v_mfma_f32_16x16x32_bf16 v[4:7], a[56:59], a[180:183], v[4:7]\n"
"v_mfma_f32_16x16x32_bf16 v[8:11], a[56:59], a[184:187], v[8:11]\n"
"v_mfma_f32_16x16x32_bf16 v[12:15], a[56:59], a[188:191], v[12:15]\n"
"v_mfma_f32_16x16x32_bf16 v[28:31], a[60:63], a[188:191], v[28:31]\n"
"v_mfma_f32_16x16x32_bf16 v[24:27], a[60:63], a[184:187], v[24:27]\n"
"v_mfma_f32_16x16x32_bf16 v[20:23], a[60:63], a[180:183], v[20:23]\n"
"v_mfma_f32_16x16x32_bf16 v[16:19], a[60:63], a[176:179], v[16:19]\n"
"v_mov_b32 v246, 0x3f80\n"
"v_mov_b32 v247, 0x3f800000\n"
"s_waitcnt vmcnt(8)\n"
"v_dot2_f32_bf16 v32, v48, v246, v0\n"
"v_dot2_f32_bf16 v33, v48, v247, v1\n"
"v_dot2_f32_bf16 v34, v49, v246, v2\n"
"v_dot2_f32_bf16 v35, v49, v247, v3\n"
"v_dot2_f32_bf16 v36, v52, v246, v4\n"
"v_dot2_f32_bf16 v37, v52, v247, v5\n"
"v_dot2_f32_bf16 v38, v53, v246, v6\n"
"v_dot2_f32_bf16 v39, v53, v247, v7\n"
"v_dot2_f32_bf16 v40, v56, v246, v8\n"
"v_dot2_f32_bf16 v41, v56, v247, v9\n"
"v_dot2_f32_bf16 v42, v57, v246, v10\n"
"v_dot2_f32_bf16 v43, v57, v247, v11\n"
"v_dot2_f32_bf16 v44, v60, v246, v12\n"
"v_dot2_f32_bf16 v45, v60, v247, v13\n"
"v_dot2_f32_bf16 v46, v61, v246, v14\n"
"v_dot2_f32_bf16 v47, v61, v247, v15\n"
"v_dot2_f32_bf16 v64, v50, v246, v16\n"
"v_dot2_f32_bf16 v65, v50, v247, v17\n"
"v_dot2_f32_bf16 v66, v51, v246, v18\n"
"v_dot2_f32_bf16 v67, v51, v247, v19\n"
"v_dot2_f32_bf16 v68, v54, v246, v20\n"
"v_dot2_f32_bf16 v69, v54, v247, v21\n"
"v_dot2_f32_bf16 v70, v55, v246, v22\n"
"v_dot2_f32_bf16 v71, v55, v247, v23\n"
"v_dot2_f32_bf16 v72, v58, v246, v24\n"
"v_dot2_f32_bf16 v73, v58, v247, v25\n"
"v_dot2_f32_bf16 v74, v59, v246, v26\n"
"v_dot2_f32_bf16 v75, v59, v247, v27\n"
"v_dot2_f32_bf16 v76, v62, v246, v28\n"
"v_dot2_f32_bf16 v77, v62, v247, v29\n"
"v_dot2_f32_bf16 v78, v63, v246, v30\n"
"v_dot2_f32_bf16 v79, v63, v247, v31\n"
"s_waitcnt vmcnt(4)\n"
"v_dot2_f32_bf16 v32, v80, v246, v32\n"
"v_dot2_f32_bf16 v33, v80, v247, v33\n"
"v_dot2_f32_bf16 v34, v81, v246, v34\n"
"v_dot2_f32_bf16 v35, v81, v247, v35\n"
"v_dot2_f32_bf16 v36, v84, v246, v36\n"
"v_dot2_f32_bf16 v37, v84, v247, v37\n"
"v_dot2_f32_bf16 v38, v85, v246, v38\n"
"v_dot2_f32_bf16 v39, v85, v247, v39\n"
"v_dot2_f32_bf16 v40, v88, v246, v40\n"
"v_dot2_f32_bf16 v41, v88, v247, v41\n"
"v_dot2_f32_bf16 v42, v89, v246, v42\n"
"v_dot2_f32_bf16 v43, v89, v247, v43\n"
"v_dot2_f32_bf16 v44, v92, v246, v44\n"
"v_dot2_f32_bf16 v45, v92, v247, v45\n"
"v_dot2_f32_bf16 v46, v93, v246, v46\n"
"v_dot2_f32_bf16 v47, v93, v247, v47\n"
"v_dot2_f32_bf16 v64, v82, v246, v64\n"
"v_dot2_f32_bf16 v65, v82, v247, v65\n"
"v_dot2_f32_bf16 v66, v83, v246, v66\n"
"v_dot2_f32_bf16 v67, v83, v247, v67\n"
"v_dot2_f32_bf16 v68, v86, v246, v68\n"
"v_dot2_f32_bf16 v69, v86, v247, v69\n"
"v_dot2_f32_bf16 v70, v87, v246, v70\n"
"v_dot2_f32_bf16 v71, v87, v247, v71\n"
"v_dot2_f32_bf16 v72, v90, v246, v72\n"
"v_dot2_f32_bf16 v73, v90, v247, v73\n"
"v_dot2_f32_bf16 v74, v91, v246, v74\n"
"v_dot2_f32_bf16 v75, v91, v247, v75\n"
"v_dot2_f32_bf16 v76, v94, v246, v76\n"
"v_dot2_f32_bf16 v77, v94, v247, v77\n"
"v_dot2_f32_bf16 v78, v95, v246, v78\n"
"v_dot2_f32_bf16 v79, v95, v247, v79\n"
"s_waitcnt vmcnt(0)\n"
"v_dot2_f32_bf16 v32, v112, v246, v32\n"
"v_dot2_f32_bf16 v33, v112, v247, v33\n"
"v_dot2_f32_bf16 v34, v113, v246, v34\n"
"v_dot2_f32_bf16 v35, v113, v247, v35\n"
"v_dot2_f32_bf16 v36, v116, v246, v36\n"
"v_dot2_f32_bf16 v37, v116, v247, v37\n"
"v_dot2_f32_bf16 v38, v117, v246, v38\n"
"v_dot2_f32_bf16 v39, v117, v247, v39\n"
"v_dot2_f32_bf16 v40, v120, v246, v40\n"
"v_dot2_f32_bf16 v41, v120, v247, v41\n"
"v_dot2_f32_bf16 v42, v121, v246, v42\n"
"v_dot2_f32_bf16 v43, v121, v247, v43\n"
"v_dot2_f32_bf16 v44, v124, v246, v44\n"
"v_dot2_f32_bf16 v45, v124, v247, v45\n"
"v_dot2_f32_bf16 v46, v125, v246, v46\n"
"v_dot2_f32_bf16 v47, v125, v247, v47\n"
"v_dot2_f32_bf16 v64, v114, v246, v64\n"
"v_dot2_f32_bf16 v65, v114, v247, v65\n"
"v_dot2_f32_bf16 v66, v115, v246, v66\n"
"v_dot2_f32_bf16 v67, v115, v247, v67\n"
"v_dot2_f32_bf16 v68, v118, v246, v68\n"
"v_dot2_f32_bf16 v69, v118, v247, v69\n"
"v_dot2_f32_bf16 v70, v119, v246, v70\n"
"v_dot2_f32_bf16 v71, v119, v247, v71\n"
"v_dot2_f32_bf16 v72, v122, v246, v72\n"
"v_dot2_f32_bf16 v73, v122, v247, v73\n"
"v_dot2_f32_bf16 v74, v123, v246, v74\n"
"v_dot2_f32_bf16 v75, v123, v247, v75\n"
"v_dot2_f32_bf16 v76, v126, v246, v76\n"
"v_dot2_f32_bf16 v77, v126, v247, v77\n"
"v_dot2_f32_bf16 v78, v127, v246, v78\n"
"v_dot2_f32_bf16 v79, v127, v247, v79\n"
"v_pk_fma_f32 v[32:33], v[32:33], v[136:137], v[128:129]\n"
"v_pk_fma_f32 v[34:35], v[34:35], v[138:139], v[130:131]\n"
"v_cvt_pk_bf16_f32 v32, v32, v33\n"
"v_cvt_pk_bf16_f32 v33, v34, v35\n"
"ds_write_b64 v250, v[32:33] offset:0\n"
"v_pk_fma_f32 v[64:65], v[64:65], v[140:141], v[132:133]\n"
"v_pk_fma_f32 v[66:67], v[66:67], v[142:143], v[134:135]\n"
"v_cvt_pk_bf16_f32 v64, v64, v65\n"
"v_cvt_pk_bf16_f32 v65, v66, v67\n"
"ds_write_b64 v250, v[64:65] offset:32\n"
"ds_read_b128 v[0:3], v251 offset:0\n"
"v_pk_fma_f32 v[36:37], v[36:37], v[168:169], v[160:161]\n"
"v_pk_fma_f32 v[38:39], v[38:39], v[170:171], v[162:163]\n"
"v_cvt_pk_bf16_f32 v36, v36, v37\n"
"v_cvt_pk_bf16_f32 v37, v38, v39\n"
"ds_write_b64 v250, v[36:37] offset:1280\n"
"v_pk_fma_f32 v[68:69], v[68:69], v[172:173], v[164:165]\n"
"v_pk_fma_f32 v[70:71], v[70:71], v[174:175], v[166:167]\n"
"v_cvt_pk_bf16_f32 v68, v68, v69\n"
"v_cvt_pk_bf16_f32 v69, v70, v71\n"
"ds_write_b64 v250, v[68:69] offset:1312\n"
"ds_read_b128 v[4:7], v251 offset:1280\n"
"v_pk_fma_f32 v[40:41], v[40:41], v[200:201], v[192:193]\n"
"v_pk_fma_f32 v[42:43], v[42:43], v[202:203], v[194:195]\n"
"v_cvt_pk_bf16_f32 v40, v40, v41\n"
"v_cvt_pk_bf16_f32 v41, v42, v43\n"
"ds_write_b64 v250, v[40:41] offset:2560\n"
"v_pk_fma_f32 v[72:73], v[72:73], v[204:205], v[196:197]\n"
"v_pk_fma_f32 v[74:75], v[74:75], v[206:207], v[198:199]\n"
"v_cvt_pk_bf16_f32 v72, v72, v73\n"
"v_cvt_pk_bf16_f32 v73, v74, v75\n"
"ds_write_b64 v250, v[72:73] offset:2592\n"
"ds_read_b128 v[8:11], v251 offset:2560\n"
"v_pk_fma_f32 v[44:45], v[44:45], v[232:233], v[224:225]\n"
"v_pk_fma_f32 v[46:47], v[46:47], v[234:235], v[226:227]\n"
"v_cvt_pk_bf16_f32 v44, v44, v45\n"
"v_cvt_pk_bf16_f32 v45, v46, v47\n"
"ds_write_b64 v250, v[44:45] offset:3840\n"
"v_pk_fma_f32 v[76:77], v[76:77], v[236:237], v[228:229]\n"
"v_pk_fma_f32 v[78:79], v[78:79], v[238:239], v[230:231]\n"
"v_cvt_pk_bf16_f32 v76, v76, v77\n"
"v_cvt_pk_bf16_f32 v77, v78, v79\n"
"ds_write_b64 v250, v[76:77] offset:3872\n"
"ds_read_b128 v[12:15], v251 offset:3840\n"
"s_waitcnt lgkmcnt(9)\n"
"buffer_store_dwordx4 v[0:3], v157, s[56:59], 0 offen offset:0\n"
"s_waitcnt lgkmcnt(6)\n"
"buffer_store_dwordx4 v[4:7], v157, s[56:59], s26 offen offset:0\n"
"s_waitcnt lgkmcnt(3)\n"
"buffer_store_dwordx4 v[8:11], v157, s[56:59], s27 offen offset:0\n"
"s_waitcnt lgkmcnt(0)\n"
"buffer_store_dwordx4 v[12:15], v157, s[56:59], s28 offen offset:0\n"
"s_branch LGE_%=\n"
"LSO1_%=:\n"
"s_mov_b32 s76, 0\n"
"LSO1L_%=:\n"
"s_sleep 1\n"
"buffer_load_dword v156, v155, s[48:51], s75 offen offset:0 sc1\n"
"s_waitcnt vmcnt(0)\n"
"v_cmp_ne_u32 vcc, s77, v156\n"
"s_cmp_le_u32 s83, 0\ns_cselect_b32 s24, 2, 1\ns_mov_b32 s25, 0\n"
"s_nop 0\n"
"s_and_b64 vcc, vcc, s[24:25]\n"
"s_add_u32 s76, s76, 1\n"
"s_cmp_gt_u32 s76, 1048576\n"
"s_cbranch_scc1 LBO1_%=\n"
"s_nop 4\n"
"s_cbranch_vccnz LSO1L_%=\n"
"s_branch LBO1_%=\n"
"LSO2_%=:\n"
"s_mov_b32 s76, 0\n"
"LSO2L_%=:\n"
"s_sleep 1\n"
"buffer_load_dword v156, v155, s[48:51], s75 offen offset:0 sc1\n"
"s_waitcnt vmcnt(0)\n"
"v_cmp_ne_u32 vcc, s77, v156\n"
"s_lshl_b32 s24, 1, s83\ns_andn2_b32 s24, 15, s24\ns_mov_b32 s25, 0\n"
"s_nop 0\n"
"s_and_b64 vcc, vcc, s[24:25]\n"
"s_add_u32 s76, s76, 1\n"
"s_cmp_gt_u32 s76, 1048576\n"
"s_cbranch_scc1 LBO2_%=\n"
"s_nop 4\n"
"s_cbranch_vccnz LSO2L_%=\n"
"s_branch LBO2_%=\n"
"LSO4_%=:\n"
"s_mov_b32 s76, 0\n"
"LSO4L_%=:\n"
"s_sleep 1\n"
"buffer_load_dword v156, v155, s[48:51], s75 offen offset:0 sc1\n"
"s_waitcnt vmcnt(0)\n"
"v_cmp_ne_u32 vcc, s77, v156\n"
"s_lshl_b32 s24, 1, s83\ns_andn2_b32 s24, 255, s24\ns_mov_b32 s25, 0\n"
"s_nop 0\n"
"s_and_b64 vcc, vcc, s[24:25]\n"
"s_add_u32 s76, s76, 1\n"
"s_cmp_gt_u32 s76, 1048576\n"
"s_cbranch_scc1 LBO4_%=\n"
"s_nop 4\n"
"s_cbranch_vccnz LSO4L_%=\n"
"s_branch LBO4_%=\n"
"LS4_%=:\n"
"s_mov_b32 s76, 0\n"
"LS4L_%=:\n"
"s_sleep 1\n"
"buffer_load_dword v253, v252, s[48:51], s81 offen offset:0 sc1\n"
"s_waitcnt vmcnt(0)\n"
"v_cmp_ne_u32 vcc, s77, v253\n"
"s_add_u32 s76, s76, 1\n"
"s_cmp_gt_u32 s76, 1048576\n"
"s_cbranch_scc1 LB4_%=\n"
"s_nop 4\n"
"s_cbranch_vccnz LS4L_%=\n"
"s_branch LB4_%=\n"
"LS5_%=:\n"
"s_mov_b32 s76, 0\n"
"LS5L_%=:\n"
"s_sleep 1\n"
"buffer_load_dword v156, v155, s[60:63], s29 offen offset:0 sc1\n"
"s_waitcnt vmcnt(0)\n"
"v_cmp_ne_u32 vcc, s77, v156\n"
"s_add_u32 s76, s76, 1\n"
"s_cmp_gt_u32 s76, 1048576\n"
"s_cbranch_scc1 LB5_%=\n"
"s_nop 4\n"
"s_cbranch_vccnz LS5L_%=\n"
"s_branch LB5_%=\n"
"LYL_%=:\n"
"s_mov_b32 s76, 0\n"
"buffer_load_dword v176, v250, s[64:67], s76 offen offset:0 sc1\n"
"s_mov_b32 s76, 4096\n"
"buffer_load_dword v177, v250, s[64:67], s76 offen offset:0 sc1\n"
"s_mov_b32 s76, 8192\n"
"buffer_load_dword v178, v250, s[64:67], s76 offen offset:0 sc1\n"
"s_mov_b32 s76, 12288\n"
"buffer_load_dword v179, v250, s[64:67], s76 offen offset:0 sc1\n"
"s_mov_b32 s76, 16384\n"
"buffer_load_dword v180, v250, s[64:67], s76 offen offset:0 sc1\n"
"s_mov_b32 s76, 20480\n"
"buffer_load_dword v181, v250, s[64:67], s76 offen offset:0 sc1\n"
"s_mov_b32 s76, 24576\n"
"buffer_load_dword v182, v250, s[64:67], s76 offen offset:0 sc1\n"
"s_mov_b32 s76, 28672\n"
"buffer_load_dword v183, v250, s[64:67], s76 offen offset:0 sc1\n"
"s_mov_b32 s76, 32768\n"
"buffer_load_dword v184, v250, s[64:67], s76 offen offset:0 sc1\n"
"s_mov_b32 s76, 36864\n"
"buffer_load_dword v185, v250, s[64:67], s76 offen offset:0 sc1\n"
"s_mov_b32 s76, 40960\n"
"buffer_load_dword v186, v250, s[64:67], s76 offen offset:0 sc1\n"
"s_mov_b32 s76, 45056\n"
"buffer_load_dword v187, v250, s[64:67], s76 offen offset:0 sc1\n"
"s_mov_b32 s76, 49152\n"
"buffer_load_dword v188, v250, s[64:67], s76 offen offset:0 sc1\n"
"s_mov_b32 s76, 53248\n"
"buffer_load_dword v189, v250, s[64:67], s76 offen offset:0 sc1\n"
"s_mov_b32 s76, 57344\n"
"buffer_load_dword v190, v250, s[64:67], s76 offen offset:0 sc1\n"
"s_mov_b32 s76, 61440\n"
"buffer_load_dword v191, v250, s[64:67], s76 offen offset:0 sc1\n"
"s_mov_b32 s76, 65536\n"
"buffer_load_dword v208, v250, s[64:67], s76 offen offset:0 sc1\n"
"s_mov_b32 s76, 69632\n"
"buffer_load_dword v209, v250, s[64:67], s76 offen offset:0 sc1\n"
"s_mov_b32 s76, 73728\n"
"buffer_load_dword v210, v250, s[64:67], s76 offen offset:0 sc1\n"
"s_mov_b32 s76, 77824\n"
"buffer_load_dword v211, v250, s[64:67], s76 offen offset:0 sc1\n"
"s_mov_b32 s76, 81920\n"
"buffer_load_dword v212, v250, s[64:67], s76 offen offset:0 sc1\n"
"s_mov_b32 s76, 86016\n"
"buffer_load_dword v213, v250, s[64:67], s76 offen offset:0 sc1\n"
"s_mov_b32 s76, 90112\n"
"buffer_load_dword v214, v250, s[64:67], s76 offen offset:0 sc1\n"
"s_mov_b32 s76, 94208\n"
"buffer_load_dword v215, v250, s[64:67], s76 offen offset:0 sc1\n"
"s_mov_b32 s76, 98304\n"
"buffer_load_dword v216, v250, s[64:67], s76 offen offset:0 sc1\n"
"s_mov_b32 s76, 102400\n"
"buffer_load_dword v217, v250, s[64:67], s76 offen offset:0 sc1\n"
"s_mov_b32 s76, 106496\n"
"buffer_load_dword v218, v250, s[64:67], s76 offen offset:0 sc1\n"
"s_mov_b32 s76, 110592\n"
"buffer_load_dword v219, v250, s[64:67], s76 offen offset:0 sc1\n"
"buffer_load_ushort v251, v251, s[52:55], s85 offen offset:0\n"
"s_waitcnt vmcnt(30)\n"
"s_branch LYB_%=\n"
"LGE_%=:\n"
    : "+{v[128:131]}"(op[0]), "+{v[160:163]}"(op[1]), "+{v[192:195]}"(op[2]), "+{v[224:227]}"(op[3]), "+{v[132:135]}"(op[4]), "+{v[164:167]}"(op[5]), "+{v[196:199]}"(op[6]), "+{v[228:231]}"(op[7]), "+{v[136:139]}"(op[8]), "+{v[168:171]}"(op[9]), "+{v[200:203]}"(op[10]), "+{v[232:235]}"(op[11]), "+{v[140:143]}"(op[12]), "+{v[172:175]}"(op[13]), "+{v[204:207]}"(op[14]), "+{v[236:239]}"(op[15]), "+{v[176:179]}"(op[16]), "+{v[180:183]}"(op[17]), "+{v[184:187]}"(op[18]), "+{v[188:191]}"(op[19]), "+{v[240:243]}"(op[20])
    : [ka] "s"(ka), [b] "s"(b), [wid] "s"(wid), [tag] "s"(tag), [key] "s"(key), [rsel] "s"(rsel), [cls] "s"(cls)
    : "v0", "v1", "v2", "v3", "v4", "v5", "v6", "v7", "v8", "v9", "v10", "v11", "v12", "v13", "v14", "v15", "v16", "v17", "v18", "v19", "v20", "v21", "v22", "v23", "v24", "v25", "v26", "v27", "v28", "v29", "v30", "v31", "v32", "v33", "v34", "v35", "v36", "v37", "v38", "v39", "v40", "v41", "v42", "v43", "v44", "v45", "v46", "v47", "v48", "v49", "v50", "v51", "v52", "v53", "v54", "v55", "v56", "v57", "v58", "v59", "v60", "v61", "v62", "v63", "v64", "v65", "v66", "v67", "v68", "v69", "v70", "v71", "v72", "v73", "v74", "v75", "v76", "v77", "v78", "v79", "v80", "v81", "v82", "v83", "v84", "v85", "v86", "v87", "v88", "v89", "v90", "v91", "v92", "v93", "v94", "v95", "v96", "v97", "v98", "v99", "v100", "v101", "v102", "v103", "v104", "v105", "v106", "v107", "v108", "v109", "v110", "v111", "v112", "v113", "v114", "v115", "v116", "v117", "v118", "v119", "v120", "v121", "v122", "v123", "v124", "v125", "v126", "v127", "v144", "v145", "v146", "v147", "v148", "v149", "v150", "v151", "v152", "v153", "v154", "v155", "v156", "v157", "v158", "v159", "v208", "v209", "v210", "v211", "v212", "v213", "v214", "v215", "v216", "v217", "v218", "v219", "v220", "v221", "v222", "v223", "v244", "v245", "v246", "v247", "v248", "v249", "v250", "v251", "v252", "v253", "v254", "v255", "a0", "a1", "a2", "a3", "a4", "a5", "a6", "a7", "a8", "a9", "a10", "a11", "a12", "a13", "a14", "a15", "a16", "a17", "a18", "a19", "a20", "a21", "a22", "a23", "a24", "a25", "a26", "a27", "a28", "a29", "a30", "a31", "a32", "a33", "a34", "a35", "a36", "a37", "a38", "a39", "a40", "a41", "a42", "a43", "a44", "a45", "a46", "a47", "a48", "a49", "a50", "a51", "a52", "a53", "a54", "a55", "a56", "a57", "a58", "a59", "a60", "a61", "a62", "a63", "a64", "a65", "a66", "a67", "a68", "a69", "a70", "a71", "a72", "a73", "a74", "a75", "a76", "a77", "a78", "a79", "a80", "a81", "a82", "a83", "a84", "a85", "a86", "a87", "a88", "a89", "a90", "a91", "a92", "a93", "a94", "a95", "a96", "a97", "a98", "a99", "a100", "a101", "a102", "a103", "a104", "a105", "a106", "a107", "a108", "a109", "a110", "a111", "a112", "a113", "a114", "a115", "a116", "a117", "a118", "a119", "a120", "a121", "a122", "a123", "a124", "a125", "a126", "a127", "a128", "a129", "a130", "a131", "a132", "a133", "a134", "a135", "a136", "a137", "a138", "a139", "a140", "a141", "a142", "a143", "a144", "a145", "a146", "a147", "a148", "a149", "a150", "a151", "a152", "a153", "a154", "a155", "a156", "a157", "a158", "a159", "a160", "a161", "a162", "a163", "a164", "a165", "a166", "a167", "a168", "a169", "a170", "a171", "a172", "a173", "a174", "a175", "a176", "a177", "a178", "a179", "a180", "a181", "a182", "a183", "a184", "a185", "a186", "a187", "a188", "a189", "a190", "a191", "a192", "a193", "a194", "a195", "a196", "a197", "a198", "a199", "a200", "a201", "a202", "a203", "a204", "a205", "a206", "a207", "a208", "a209", "a210", "a211", "a212", "a213", "a214", "a215", "a216", "a217", "a218", "a219", "a220", "a221", "a222", "a223", "a224", "a225", "a226", "a227", "a228", "a229", "a230", "a231", "a232", "a233", "a234", "a235", "a236", "a237", "a238", "a239", "a240", "a241", "a242", "a243", "a244", "a245", "a246", "a247", "a248", "a249", "a250", "a251", "a252", "a253", "a254", "a255", "s20", "s21", "s22", "s23", "s24", "s25", "s26", "s27", "s28", "s29", "s30", "s31", "s36", "s37", "s38", "s39", "s40", "s41", "s42", "s43", "s44", "s45", "s46", "s47", "s48", "s49", "s50", "s51", "s52", "s53", "s54", "s55", "s56", "s57", "s58", "s59", "s60", "s61", "s62", "s63", "s64", "s65", "s66", "s67", "s68", "s69", "s70", "s71", "s72", "s73", "s74", "s75", "s76", "s77", "s78", "s79", "s80", "s81", "s82", "s83", "s84", "s85", "s86", "s87", "s88", "s89", "s90", "s91", "s92", "s93", "s94", "s95", "s96", "s97", "s98", "s99", "vcc", "scc", "memory");
  if (b == 0 && wid == 0) {
    typedef __attribute__((address_space(4))) const u64 c_u64;
    typedef __attribute__((address_space(1))) unsigned g_u32;
    *(g_u32*)((c_u64*)ka)[10] = tag;
  }
}

extern "C" void kernel_launch(void* const* d_in, const int* in_sizes, int n_in, void* d_out, int out_size, void* d_ws, size_t ws_size, hipStream_t stream) {
  (void)hipFuncSetAttribute((const void*)fused_k, hipFuncAttributeMaxDynamicSharedMemorySize, 163840);
  fused_k<<<256, 256, 163840, stream>>>((const u16*)d_in[0], (const u16*)d_in[1], (const u16*)d_in[2], (const u16*)d_in[3], (const u16*)d_in[4], (const u16*)d_in[6],
                                        (const u16*)d_in[7], (const u16*)d_in[8], (const u16*)d_in[9], (u16*)d_out, (char*)d_ws);
}
```
